```python
import torch

def _softmax(x, axis=-1):
    return torch.softmax(x, dim=axis)

def _gelu(x):
    return torch.nn.functional.gelu(x, approximate='tanh')

def _silu(x):
    return torch.nn.functional.silu(x)

def _sigmoid(x):
    return torch.sigmoid(x)

def _layernorm(x, axis=-1, eps=1e-05):
    m = x.mean(dim=axis, keepdim=True)
    v = x.var(dim=axis, keepdim=True, unbiased=False)
    return (x - m) / torch.sqrt(v + eps)

def _scatter_add(tgt, idx, src, axis=0):
    return tgt.clone().index_add_(axis, idx, src)

def _segment_sum(src, seg, n):
    out = torch.zeros((n,) + src.shape[1:], dtype=src.dtype)
    return out.index_add_(0, seg, src)

def _pad(x, pw):
    flat = tuple((v for pair in reversed(pw) for v in pair))
    return torch.nn.functional.pad(x, flat)
kernel_name = 'adaLN_cond__sliding_local_dilated__glu_gated_reglu__prompt'

def setup_inputs():

    _w = lambda a: (lambda t: t.to(torch.bfloat16) if t.is_floating_point() else t)(torch.as_tensor(a))

    def _A_setup():
        B, S, D = (1, 2048, 512)
        return {'x': _w((lambda *s: torch.randn(*s, dtype=torch.bfloat16) * 0.05)(B, S, D)), 'wqkv': _w((lambda *s: torch.randn(*s, dtype=torch.bfloat16) * 0.05)(D, 3 * D)), 'wo': _w((lambda *s: torch.randn(*s, dtype=torch.bfloat16) * 0.05)(D, D))}
    _A_in = _A_setup()

    def _B_setup():
        B, S, D, F = (1, 2048, 512, 1792)
        return {'x': _w((lambda *s: torch.randn(*s, dtype=torch.bfloat16) * 0.05)(B, S, D)), 'W_gate': _w((lambda *s: torch.randn(*s, dtype=torch.bfloat16) * 0.8)(D, F) * (2.0 / D) ** 0.5), 'W_up': _w((lambda *s: torch.randn(*s, dtype=torch.bfloat16) * 0.8)(D, F) * (2.0 / D) ** 0.5), 'W_down': _w((lambda *s: torch.randn(*s, dtype=torch.bfloat16) * 0.8)(F, D) * (1.0 / F) ** 0.5)}
    _B_in = _B_setup()
    return {'A_x': _A_in['x'], 'A_wqkv': _A_in['wqkv'], 'A_wo': _A_in['wo'], 'B_x': _B_in['x'], 'B_W_gate': _B_in['W_gate'], 'B_W_up': _B_in['W_up'], 'B_W_down': _B_in['W_down'], 'c': _w((lambda *s: torch.randn(*s, dtype=torch.bfloat16) * 0.1)(1, 512) * 8.0), 'w_mod': _w((lambda *s: torch.randn(*s, dtype=torch.bfloat16) * 0.1)(512, 6 * 512) * 512 ** (-0.5) * 8.0)}

def reference(A_x, A_wqkv, A_wo, B_x, B_W_gate, B_W_up, B_W_down, c, w_mod):

    def _A_ref(x, wqkv, wo):
        B, S, D = x.shape
        H = 32
        Dh = D // H
        W, dil = (8, 2)
        qkv = (lambda x, *d: x.permute(*d))((x @ wqkv).reshape(B, S, 3, H, Dh), 2, 0, 3, 1, 4)
        q, k, v = (qkv[0], qkv[1], qkv[2])
        scale = Dh ** (-0.5)
        pos = torch.arange(S)
        off = torch.arange(W) * dil
        idx = pos[:, None] - off[None, :]
        mask = idx >= 0
        idx_c = torch.where(mask, idx, 0).reshape(-1)
        kg = k[:, :, idx_c, :].reshape(B, H, S, W, Dh)
        vg = v[:, :, idx_c, :].reshape(B, H, S, W, Dh)
        logits = torch.einsum('bhsd,bhswd->bhsw', q, kg) * scale
        logits = torch.where(mask[None, None, :, :], logits, -1000000000.0)
        attn = _softmax(logits, axis=-1)
        o = torch.einsum('bhsw,bhswd->bhsd', attn, vg)
        return (lambda x, *d: x.permute(*d))(o, 0, 2, 1, 3).reshape(B, S, D) @ wo

    def _B_ref(x, W_gate, W_up, W_down):
        gate = torch.einsum('bsd,df->bsf', x, W_gate)
        up = torch.einsum('bsd,df->bsf', x, W_up)
        gate = gate * (lambda x, dt: x.to(dt))(gate > 0, gate.dtype)
        h = gate * up
        return torch.einsum('bsf,fd->bsd', h, W_down)
    x = A_x
    mod = (_silu(c) @ w_mod).reshape(c.shape[0], 6, 1, -1)
    sa, ca, ga, sb, cb, gb = (mod[:, 0].clone(), mod[:, 1].clone(), mod[:, 2].clone(), mod[:, 3].clone(), mod[:, 4].clone(), mod[:, 5].clone())
    h = _layernorm(x, axis=-1) * (1 + ca) + sa
    x = x + ga * _A_ref(h, A_wqkv, A_wo)
    h = _layernorm(x, axis=-1) * (1 + cb) + sb
    x = x + gb * _B_ref(h, B_W_gate, B_W_up, B_W_down)
    return x
```

```cpp
#include <hip/hip_runtime.h>
#include <hip/hip_bf16.h>
namespace naive {
typedef unsigned short u16;
__device__ __forceinline__ float bf2f(u16 b){ return __uint_as_float(((unsigned)b)<<16); }
__device__ __forceinline__ u16 f2bf(float f){ unsigned u=__float_as_uint(f); u += 0x7fffu + ((u>>16)&1u); return (u16)(u>>16); }
__device__ __forceinline__ float rb(float f){ return bf2f(f2bf(f)); }
constexpr int S=2048, D=512, F=1792, H=32, DH=16;
__global__ void k_mod(const u16* c, const u16* wmod, u16* mod){
  int n = blockIdx.x*blockDim.x+threadIdx.x; if(n>=6*D) return;
  float acc=0.f;
  for(int k=0;k<D;k++){ float cv=bf2f(c[k]); float s=rb(cv/(1.f+expf(-cv))); acc += s*bf2f(wmod[(size_t)k*6*D+n]); }
  mod[n]=f2bf(acc);
}
__global__ void k_ln(const u16* x, const u16* shift, const u16* scale, u16* h){
  int row=blockIdx.x, t=threadIdx.x; float v[8]; float s=0.f;
  for(int j=0;j<8;j++){ v[j]=bf2f(x[row*D+t*8+j]); s+=v[j]; }
  for(int o=32;o;o>>=1) s+=__shfl_xor(s,o);
  float mf=s/D; float q=0.f;
  for(int j=0;j<8;j++){ float d=v[j]-mf; q+=d*d; }
  for(int o=32;o;o>>=1) q+=__shfl_xor(q,o);
  float m=rb(mf), var=rb(q/D);
  float sd=rb(sqrtf(rb(var+1e-5f)));
  for(int j=0;j<8;j++){ int k=t*8+j; float a=rb(rb(v[j]-m)/sd); float g=rb(1.f+bf2f(scale[k])); h[row*D+k]=f2bf(rb(a*g)+bf2f(shift[k])); }
}
__global__ void k_gemm(const u16* A, const u16* B, u16* C, int M, int N, int K){
  __shared__ float sa[16][17], sb[16][17];
  int tx=threadIdx.x, ty=threadIdx.y, row=blockIdx.y*16+ty, col=blockIdx.x*16+tx; float acc=0.f;
  for(int k0=0;k0<K;k0+=16){ sa[ty][tx]=bf2f(A[(size_t)row*K+k0+tx]); sb[ty][tx]=bf2f(B[(size_t)(k0+ty)*N+col]); __syncthreads();
    for(int k=0;k<16;k++) acc+=sa[ty][k]*sb[k][tx]; __syncthreads(); }
  C[(size_t)row*N+col]=f2bf(acc);
}
__global__ void k_attn(const u16* qkv, u16* o){
  int i=blockIdx.x*blockDim.x+threadIdx.x; int s=i/H, h=i%H; if(s>=S) return;
  float q[DH]; for(int d=0;d<DH;d++) q[d]=bf2f(qkv[(size_t)s*3*D+h*DH+d]);
  float lg[8]; float mx=-1e30f;
  for(int w=0;w<8;w++){ int p=s-2*w; if(p>=0){ float a=0.f; for(int d=0;d<DH;d++) a+=q[d]*bf2f(qkv[(size_t)p*3*D+D+h*DH+d]); lg[w]=rb(rb(a)*0.25f);} else lg[w]=rb(-1e9f); mx=fmaxf(mx,lg[w]); }
  float sum=0.f; for(int w=0;w<8;w++){ lg[w]=expf(lg[w]-mx); sum+=lg[w]; }
  float acc[DH]; for(int d=0;d<DH;d++) acc[d]=0.f;
  for(int w=0;w<8;w++){ int p=s-2*w; if(p<0) p=0; float a=rb(lg[w]/sum); for(int d=0;d<DH;d++) acc[d]+=a*bf2f(qkv[(size_t)p*3*D+2*D+h*DH+d]); }
  for(int d=0;d<DH;d++) o[(size_t)s*D+h*DH+d]=f2bf(acc[d]);
}
__global__ void k_res(const u16* x, const u16* g, const u16* y, u16* out){
  int i=blockIdx.x*blockDim.x+threadIdx.x; int k=i%D; out[i]=f2bf(bf2f(x[i])+rb(bf2f(g[k])*bf2f(y[i])));
}
__global__ void k_glu(const u16* g, const u16* u, u16* a, int n){
  int i=blockIdx.x*blockDim.x+threadIdx.x; if(i>=n) return; float gv=bf2f(g[i]); gv = gv>0.f? gv:0.f; a[i]=f2bf(gv*bf2f(u[i]));
}
struct Bufs { u16 *mod,*h1,*qkv,*o,*ao,*x1,*h2,*g,*u,*act,*dn; };
inline Bufs carve(void* ws){ u16* p=(u16*)ws; Bufs b; b.mod=p; p+=8192; b.h1=p; p+=S*D; b.qkv=p; p+=S*3*D; b.o=p; p+=S*D; b.ao=p; p+=S*D; b.x1=p; p+=S*D; b.h2=p; p+=S*D; b.g=p; p+=S*F; b.u=p; p+=S*F; b.act=p; p+=S*F; b.dn=p; p+=S*D; return b; }
inline void run(void* const* d_in, void* d_out, void* ws, hipStream_t st){
  const u16* x=(const u16*)d_in[0]; const u16* wqkv=(const u16*)d_in[1]; const u16* wo=(const u16*)d_in[2];
  const u16* wg=(const u16*)d_in[4]; const u16* wu=(const u16*)d_in[5]; const u16* wd=(const u16*)d_in[6]; const u16* c=(const u16*)d_in[7]; const u16* wm=(const u16*)d_in[8];
  Bufs b=carve(ws); u16* out=(u16*)d_out;
  k_mod<<<12,256,0,st>>>(c,wm,b.mod);
  k_ln<<<S,64,0,st>>>(x,b.mod,b.mod+D,b.h1);
  k_gemm<<<dim3(3*D/16,S/16),dim3(16,16),0,st>>>(b.h1,wqkv,b.qkv,S,3*D,D);
  k_attn<<<S*H/256,256,0,st>>>(b.qkv,b.o);
  k_gemm<<<dim3(D/16,S/16),dim3(16,16),0,st>>>(b.o,wo,b.ao,S,D,D);
  k_res<<<S*D/256,256,0,st>>>(x,b.mod+2*D,b.ao,b.x1);
  k_ln<<<S,64,0,st>>>(b.x1,b.mod+3*D,b.mod+4*D,b.h2);
  k_gemm<<<dim3(F/16,S/16),dim3(16,16),0,st>>>(b.h2,wg,b.g,S,F,D);
  k_gemm<<<dim3(F/16,S/16),dim3(16,16),0,st>>>(b.h2,wu,b.u,S,F,D);
  k_glu<<<S*F/256,256,0,st>>>(b.g,b.u,b.act,S*F);
  k_gemm<<<dim3(D/16,S/16),dim3(16,16),0,st>>>(b.act,wd,b.dn,S,D,F);
  k_res<<<S*D/256,256,0,st>>>(b.x1,b.mod+5*D,b.dn,out);
}
}
extern "C" void kernel_launch(void* const* d_in, const int* in_sizes, int n_in, void* d_out, int out_size, void* d_ws, size_t ws_size, hipStream_t stream) {
  naive::run(d_in, d_out, d_ws, stream);
}
```

```cpp
#include <hip/hip_runtime.h>
#include <stdint.h>

typedef unsigned short u16;
typedef unsigned u32;
typedef unsigned long long u64;
typedef u32 u32x2 __attribute__((ext_vector_type(2)));
typedef u32 u32x4 __attribute__((ext_vector_type(4)));
typedef short s16x4 __attribute__((ext_vector_type(4)));
typedef short s16x8 __attribute__((ext_vector_type(8)));
typedef __bf16 bf16x2 __attribute__((ext_vector_type(2)));
typedef __bf16 bf16x8 __attribute__((ext_vector_type(8)));
typedef float f32x2 __attribute__((ext_vector_type(2)));
typedef float f32x4 __attribute__((ext_vector_type(4)));
typedef float f32x16 __attribute__((ext_vector_type(16)));
typedef __amdgpu_buffer_rsrc_t rsrc_t;
#define LAS __attribute__((address_space(3)))
#define DI __device__ __forceinline__

#define DIAG 0
#define XMAP 2

constexpr int S_ = 2048, D_ = 512, F_ = 1792;
constexpr u32 WS_E = 0, WS_MODG = 4096, WS_FO = 32768, WS_FY = 40960, WS_FA = 49152, WS_FA2 = 53248, WS_DBG = 65536;
constexpr u32 WS_O = 1u << 20, WS_Y = 3u << 20, WS_ST = 5u << 20, WS_ACT = 6u << 20, WS_BYTES = 13u << 20;
constexpr u32 L_AIMG = 0, L_RW = 81920, L_W1S = 12288;
constexpr u32 L_LNP = 73728, L_PRED = L_LNP + 5120, L_RSNB = L_PRED + 1088;
constexpr u32 L_XRES = 65536, L_WO = 73728, L_RED2 = 139264;
constexpr u32 L_P1B = 8192, L_P2B = 6400, L_P2M = 3200;
constexpr u32 L_RED4 = 65536;
constexpr u32 L_MISC = 155648, L_G1 = L_MISC, L_S1 = L_MISC + 2048, L_FIX = L_MISC;
constexpr u32 L_MU1 = L_MISC + 4096, L_GA = L_MU1 + 256, L_G2C = L_GA + 256, L_GBV = L_G2C + 256, L_GS = L_MISC + 5120;
constexpr u32 LDS_BYTES = 163840;
__host__ __device__ constexpr u32 wd_unit_off(int u) { return u == 0 || u == 4 ? 102400u : (u == 1 || u == 5 ? 0u : (u == 2 || u == 6 ? 32768u : 65536u)); }

template <int N> struct ic { static constexpr int value = N; };
struct Params { const void *x, *wqkv, *wo, *wg, *wu, *wd, *c, *wmod; void *out; void *ws; };

DI rsrc_t mk(const void* p, u32 bytes) { return __builtin_amdgcn_make_buffer_rsrc((void*)p, 0, bytes, 0x00020000); }
DI float bflo(u32 v) { return __builtin_bit_cast(float, v << 16); }
DI float bfhi(u32 v) { return __builtin_bit_cast(float, v & 0xffff0000u); }
DI u32 pk2(float a, float b) { bf16x2 r; r[0] = (__bf16)a; r[1] = (__bf16)b; return __builtin_bit_cast(u32, r); }
DI float rbf(float a) { return bflo(pk2(a, a)); }
DI f32x4 mfma16(u32x4 a, u32x4 b, f32x4 c) { return __builtin_amdgcn_mfma_f32_16x16x32_bf16(__builtin_bit_cast(bf16x8, a), __builtin_bit_cast(bf16x8, b), c, 0, 0, 0); }
DI f32x4 mfma16k16(u32x2 a, u32x2 b, f32x4 c) { return __builtin_amdgcn_mfma_f32_16x16x16bf16_1k(__builtin_bit_cast(s16x4, a), __builtin_bit_cast(s16x4, b), c, 0, 0, 0); }
DI f32x16 mfma32(u32x4 a, u32x4 b, f32x16 c) { return __builtin_amdgcn_mfma_f32_32x32x16_bf16(__builtin_bit_cast(bf16x8, a), __builtin_bit_cast(bf16x8, b), c, 0, 0, 0); }
DI u32x2 trd(LAS char* p) { return __builtin_bit_cast(u32x2, __builtin_amdgcn_ds_read_tr16_b64_v4i16((LAS s16x4*)p)); }
DI u32x4 trd2(LAS char* p, int d) { u32x2 a = trd(p), b = trd(p + d); return u32x4{a[0], a[1], b[0], b[1]}; }
DI void dma16(rsrc_t rs, u32 voff, u32 soff, u32 lds) {
  asm volatile("s_mov_b32 m0, %0\n\ts_nop 0\n\tbuffer_load_dwordx4 %1, %2, %3 offen lds" :: "s"(lds), "v"(voff), "s"(rs), "s"(soff) : "m0", "memory");
}
template <int OFF> DI void ald128(u32x4& d, rsrc_t rs, u32 voff, u32 soff) {
  asm volatile("buffer_load_dwordx4 %0, %1, %2, %3 offen offset:%c4" : "=v"(d) : "v"(voff), "s"(rs), "s"(soff), "i"(OFF) : "memory");
}
template <int OFF> DI void ald128_sc1(u32x4& d, rsrc_t rs, u32 voff, u32 soff) {
  asm volatile("buffer_load_dwordx4 %0, %1, %2, %3 offen offset:%c4 sc1" : "=v"(d) : "v"(voff), "s"(rs), "s"(soff), "i"(OFF) : "memory");
}
DI void ald64_sc1(u32x2& d, rsrc_t rs, u32 voff, u32 soff) {
  asm volatile("buffer_load_dwordx2 %0, %1, %2, %3 offen sc1" : "=v"(d) : "v"(voff), "s"(rs), "s"(soff) : "memory");
}
DI void ald64(u32x2& d, rsrc_t rs, u32 voff, u32 soff) {
  asm volatile("buffer_load_dwordx2 %0, %1, %2, %3 offen" : "=v"(d) : "v"(voff), "s"(rs), "s"(soff) : "memory");
}
DI void warm(u32& junk, rsrc_t rs, u32 voff, u32 soff) {
  asm volatile("buffer_load_dword %0, %1, %2, %3 offen" : "+v"(junk) : "v"(voff), "s"(rs), "s"(soff) : "memory");
}
template <int N> DI void waitv() { asm volatile("s_waitcnt vmcnt(%c0)" :: "i"(N) : "memory"); }
template <int N> DI void waitv4(u32x4& a, u32x4& b, u32x4& c, u32x4& d) { asm volatile("s_waitcnt vmcnt(%c4)" : "+v"(a), "+v"(b), "+v"(c), "+v"(d) : "i"(N) : "memory"); }
template <int N> DI void waitv1(u32x4& a) { asm volatile("s_waitcnt vmcnt(%c1)" : "+v"(a) : "i"(N) : "memory"); }
DI void bar() { asm volatile("s_waitcnt lgkmcnt(0)\n\ts_barrier" ::: "memory"); }
DI u32x4 ld128_sc1(rsrc_t rs, u32 voff, u32 soff) { return __builtin_amdgcn_raw_buffer_load_b128(rs, voff, soff, 16); }
DI u32x2 ld64_sc1(rsrc_t rs, u32 voff, u32 soff) { return __builtin_amdgcn_raw_buffer_load_b64(rs, voff, soff, 16); }
DI void st128_sc1(u32x4 v, rsrc_t rs, u32 voff, u32 soff) { __builtin_amdgcn_raw_buffer_store_b128(v, rs, voff, soff, 16); }
DI void st64_sc1(u32x2 v, rsrc_t rs, u32 voff, u32 soff) { __builtin_amdgcn_raw_buffer_store_b64(v, rs, voff, soff, 16); }
DI float f_(u32 v) { return __builtin_bit_cast(float, v); }
DI u32 u_(float v) { return __builtin_bit_cast(u32, v); }
DI float swap16_add(float a, float b) { auto r = __builtin_amdgcn_permlane16_swap(u_(a), u_(b), false, false); u32 r0 = r[0], r1 = r[1]; return f_(r0) + f_(r1); }
DI float swap32_add(float a, float b) { auto r = __builtin_amdgcn_permlane32_swap(u_(a), u_(b), false, false); u32 r0 = r[0], r1 = r[1]; return f_(r0) + f_(r1); }
DI float swap16_max(float a) { auto r = __builtin_amdgcn_permlane16_swap(u_(a), u_(a), false, false); u32 r0 = r[0], r1 = r[1]; return fmaxf(f_(r0), f_(r1)); }
DI float swap32_max(float a) { auto r = __builtin_amdgcn_permlane32_swap(u_(a), u_(a), false, false); u32 r0 = r[0], r1 = r[1]; return fmaxf(f_(r0), f_(r1)); }
template <int CTRL> DI float dpp_add(float a) { return a + f_(__builtin_amdgcn_update_dpp(0, u_(a), CTRL, 0xf, 0xf, true)); }
DI float dot2(u32 a, u32 b, float c) { return __builtin_amdgcn_fdot2_f32_bf16(__builtin_bit_cast(bf16x2, a), __builtin_bit_cast(bf16x2, b), c, false); }

constexpr u64 SPIN_TICKS = 20000;
DI bool spin_out(u64 t0) { __builtin_amdgcn_s_sleep(1); return (__builtin_amdgcn_s_memrealtime() - t0) > SPIN_TICKS; }

#define SB() __builtin_amdgcn_sched_barrier(0)
#define SGB_MFMA(n) __builtin_amdgcn_sched_group_barrier(0x8, n, 0)
#define SGB_DSR(n) __builtin_amdgcn_sched_group_barrier(0x100, n, 0)
#define SGB_DSW(n) __builtin_amdgcn_sched_group_barrier(0x200, n, 0)
#define STAMP(i) do {} while (0)

template <int NF> DI void wait_flags(rsrc_t rws, u32 off, int lane, u32 tlo, u32 thi) {
  const u32 vo = (u32)(lane & (NF - 1)) * 8u;
  const u64 t0 = __builtin_amdgcn_s_memrealtime();
  for (;;) {
    u32x2 f = ld64_sc1(rws, vo, off);
    if (__all(f[0] == tlo && f[1] == thi)) break;
    if (spin_out(t0)) break;
  }
  __builtin_amdgcn_fence(__ATOMIC_ACQUIRE, "wavefront");
}
DI bool gran_ok(u32x4 g, u32 glo, u32 ghi) { return (g[0] >> 16) == glo && g[1] == ghi && (g[2] >> 16) == glo && g[3] == ghi; }

__global__ __launch_bounds__(512, 2) void fused_block(Params p) {
  extern __shared__ __attribute__((aligned(16))) char smem_[];
  LAS char* L = (LAS char*)smem_;
  const u32 Lb = (u32)(size_t)L;
  const int tid = threadIdx.x, lane = tid & 63;
  const int w = __builtin_amdgcn_readfirstlane(tid >> 6), cw = w & 3;
  const bool isC = w < 4;
  const int b = blockIdx.x, xcd = b & 7, idx = b >> 3;
  const int tt = 8 * (xcd >> 1) + (idx >> 2), hgl = idx & 3, hg = 4 * (xcd & 1) + hgl;
  const int jt = 8 * (xcd & 1) + 4 * (tt & 1) + hgl;
  const int T = tt >> 1, cb = hg;
  STAMP(0);
  const rsrc_t rx = mk(p.x, S_ * D_ * 2), rw1 = mk(p.wqkv, D_ * 3 * D_ * 2), rwo = mk(p.wo, D_ * D_ * 2);
  const rsrc_t rwg = mk(p.wg, D_ * F_ * 2), rwu = mk(p.wu, D_ * F_ * 2), rwd = mk(p.wd, F_ * D_ * 2);
  const rsrc_t rc = mk(p.c, D_ * 2), rwm = mk(p.wmod, D_ * 6 * D_ * 2), rout = mk(p.out, S_ * D_ * 2), rws = mk(p.ws, WS_BYTES);
  const int g = lane >> 4, i16 = lane & 15, h = lane >> 5, tok = lane & 31, t4 = tid & 255;

  u32x2 ev; ald64_sc1(ev, rws, 0u, WS_E);
  const bool producer = isC && idx < 8, producer2 = !isC && idx >= 8 && idx < 24;
  const int cbk = idx < 8 ? xcd * 8 + idx : 64 + xcd * 16 + (idx - 8);
  u32x2 cv; u32x4 wm[4];
  auto mod_issue = [&]() {
#pragma unroll
    for (int i = 0; i < 4; ++i) ald128<0>(wm[i], rwm, (u32)((t4 >> 1) * 4 * 6144 + (t4 & 1) * 16), (u32)(i * 6144 + cbk * 32));
    ald64(cv, rc, (u32)((t4 >> 1) * 8), 0u);
  };
  if (producer) mod_issue();
  u32x4 xr[10];
  const int nrot = 2 * hgl;
#pragma unroll
  for (int n = 0; n < 10; ++n) {
    int r8 = n + nrot; r8 = r8 >= 10 ? r8 - 10 : r8;
    int row = 64 * tt - 16 + 8 * r8 + (lane >> 3); row = row < 0 ? 0 : row;
    ald128<0>(xr[n], rx, (u32)(row * 1024 + (lane & 7) * 16), (u32)(w * 128));
  }
  const int rot = 2 * (tt & 7);
  const u32 w1v = (u32)((lane >> 3) * 3072 + (((((lane >> 1) & 3) ^ ((lane >> 4) & 1)) ^ ((cw & 1) << 1)) * 32) + (lane & 1) * 16);
  auto w1slot = [](int n) { return n < 6 ? L_RW + n * L_W1S : (n < 12 ? (n - 6) * L_W1S : L_RW + (n - 12) * L_W1S); };
  auto dma_w1 = [&](int n0) {
#pragma unroll
    for (int n = n0; n < n0 + 4; ++n) {
      const int ks = (n + rot) & 15;
#pragma unroll
      for (int pp = 0; pp < 3; ++pp) dma16(rw1, w1v, (u32)((32 * ks + 8 * cw) * 3072 + pp * 1024 + hg * 128), Lb + (u32)(w1slot(n) + pp * 4096 + cw * 1024));
    }
  };
  if (!isC) dma_w1(0);
  STAMP(1);
  u32 tlo, thi;
  auto mod_finish = [&](u32 pred) {
    float a8[8];
#pragma unroll
    for (int e = 0; e < 8; ++e) a8[e] = 0.f;
#pragma unroll
    for (int i = 0; i < 4; ++i) {
      const float cf = (i & 1) ? bfhi(cv[i >> 1]) : bflo(cv[i >> 1]);
      const float sv = rbf(cf * __builtin_amdgcn_rcpf(1.f + __expf(-cf)));
#pragma unroll
      for (int e = 0; e < 4; ++e) { a8[2 * e] = fmaf(sv, bflo(wm[i][e]), a8[2 * e]); a8[2 * e + 1] = fmaf(sv, bfhi(wm[i][e]), a8[2 * e + 1]); }
    }
#pragma unroll
    for (int e = 0; e < 8; ++e) { a8[e] = dpp_add<0x122>(a8[e]); a8[e] = dpp_add<0x124>(a8[e]); a8[e] = dpp_add<0x128>(a8[e]); }
    if (i16 < 2) {
      LAS f32x4* d = (LAS f32x4*)(L + pred + (cw * 4 + g) * 64 + i16 * 32);
      d[0] = f32x4{a8[0], a8[1], a8[2], a8[3]}; d[1] = f32x4{a8[4], a8[5], a8[6], a8[7]};
    }
    asm volatile("s_waitcnt lgkmcnt(0)" ::: "memory");
    if (lane == 0) *(volatile LAS u32x2*)(L + pred + 1024 + cw * 8) = u32x2{tlo, thi};
    if (cw == 0) {
      const u64 t0 = __builtin_amdgcn_s_memrealtime();
      for (;;) {
        const u32x2 tg = *(volatile LAS u32x2*)(L + pred + 1024 + (lane & 3) * 8);
        if (__all(tg[0] == tlo && tg[1] == thi)) break;
        if ((__builtin_amdgcn_s_memrealtime() - t0) > SPIN_TICKS) break;
      }
      asm volatile("" ::: "memory");
      LAS float* s = (LAS float*)(L + pred) + g * 64 + i16;
      float v = (s[0] + s[16]) + (s[32] + s[48]);
      v = swap16_add(v, v); v = swap32_add(v, v);
      const u32 mb = pk2(v, v) & 0xffffu;
      if (lane < 16) st64_sc1(u32x2{mb | (tlo << 16), (tlo >> 16) | (thi << 16)}, rws, (u32)(lane * 8), WS_MODG + (u32)cbk * 128u);
    }
  };
  if (producer) {
    asm volatile("s_waitcnt vmcnt(10)" : "+v"(wm[0]), "+v"(wm[1]), "+v"(wm[2]), "+v"(wm[3]), "+v"(cv), "+v"(ev) :: "memory");
    const u64 e1 = (((u64)__builtin_amdgcn_readfirstlane(ev[1]) << 32) | (u64)__builtin_amdgcn_readfirstlane(ev[0])) + 1ull;
    tlo = (u32)e1; thi = (u32)(e1 >> 32);
    mod_finish(L_PRED);
  }
  STAMP(2);
  if (isC) { if (producer && w == 0) waitv<1>(); else waitv<0>(); } else waitv<12>();
#pragma unroll
  for (int n = 0; n < 10; n += 2) asm volatile("" : "+v"(xr[n]), "+v"(xr[n + 1]));
  asm volatile("" : "+v"(ev));
  {
    const u64 e1 = (((u64)__builtin_amdgcn_readfirstlane(ev[1]) << 32) | (u64)__builtin_amdgcn_readfirstlane(ev[0])) + 1ull;
    tlo = (u32)e1; thi = (u32)(e1 >> 32);
  }
  const u32 glo = tlo & 0xffffu, ghi = (tlo >> 16) | (thi << 16);
  STAMP(3);
  float xh[10][8];
  {
    const int c8 = lane & 7, r8l = lane >> 3;
    float pv[10];
#pragma unroll
    for (int n = 0; n < 10; ++n) {
      float s1 = 0.f, s2 = 0.f;
#pragma unroll
      for (int e = 0; e < 4; ++e) { s1 = dot2(xr[n][e], 0x3f803f80u, s1); s2 = dot2(xr[n][e], xr[n][e], s2); }
      const float keep = (c8 & 1) ? s2 : s1, give = (c8 & 1) ? s1 : s2;
      float v = keep + f_(__builtin_amdgcn_update_dpp(0, u_(give), 0xB1, 0xf, 0xf, true));
      v = dpp_add<0x4E>(v);
      pv[n] = v + f_(__builtin_amdgcn_update_dpp(0, u_(v), 0x104, 0xf, 0xf, true));
    }
    if (c8 < 2) {
#pragma unroll
      for (int n = 0; n < 10; ++n) ((LAS float*)(L + L_LNP))[(n * 8 + r8l) * 16 + w * 2 + c8] = pv[n];
    }
    bar();
    if (!isC) dma_w1(4);
#pragma unroll
    for (int k = 0; k < 2; ++k) {
      const int n = (k == 1 && c8 < 2) ? 8 + c8 : c8;
      const LAS f32x4* q = (const LAS f32x4*)(L + L_LNP + (n * 8 + r8l) * 64);
      const f32x4 a = q[0] + q[1], c4 = q[2] + q[3];
      const float s1 = (a[0] + a[2]) + (c4[0] + c4[2]), s2 = (a[1] + a[3]) + (c4[1] + c4[3]);
      const float mu = s1 * (1.f / 512.f);
      const float var = fmaxf(s2 * (1.f / 512.f) - mu * mu, 0.f);
      const float rs = __builtin_amdgcn_rsqf(var + 1e-5f);
      *(LAS f32x2*)(L + L_RSNB + (n * 8 + r8l) * 8) = f32x2{rs, -mu * rs};
      int r8 = n + nrot; r8 = r8 >= 10 ? r8 - 10 : r8;
      if (r8 >= 2 && w == 0) ((LAS float*)(L + L_MU1))[(r8 - 2) * 8 + r8l] = mu;
    }
    asm volatile("s_waitcnt lgkmcnt(0)" ::: "memory");
#pragma unroll
    for (int n = 0; n < 10; ++n) {
      const f32x2 rn = *(const LAS f32x2*)(L + L_RSNB + (n * 8 + r8l) * 8);
#pragma unroll
      for (int e = 0; e < 4; ++e) { xh[n][2 * e] = fmaf(bflo(xr[n][e]), rn[0], rn[1]); xh[n][2 * e + 1] = fmaf(bfhi(xr[n][e]), rn[0], rn[1]); }
    }
  }
#pragma unroll
  for (int n = 0; n < 10; ++n)
    asm volatile("" : "+v"(xh[n][0]), "+v"(xh[n][1]), "+v"(xh[n][2]), "+v"(xh[n][3]), "+v"(xh[n][4]), "+v"(xh[n][5]), "+v"(xh[n][6]), "+v"(xh[n][7]));
  STAMP(4);
  if (!isC) { dma_w1(8); waitv<0>(); }
  bar();
  u32x4 wr[6][3];
  LAS char* w1rd = L + ((8 * g + (i16 >> 2)) * 128 + ((cw ^ (((i16 >> 3) & 1) | ((g & 1) << 1))) * 32) + (lane & 3) * 8);
  if (isC) {
#pragma unroll
    for (int n = 0; n < 6; ++n)
#pragma unroll
      for (int pp = 0; pp < 3; ++pp) wr[n][pp] = trd2(w1rd + n * L_W1S + pp * 4096, 512);
    STAMP(5);
    u32x4 ga_, gc_;
    const u64 t0 = __builtin_amdgcn_s_memrealtime();
    for (;;) {
      ga_ = ld128_sc1(rws, (u32)(tid * 16), WS_MODG); gc_ = ld128_sc1(rws, (u32)(tid * 16), WS_MODG + 4096);
      if (__all(gran_ok(ga_, glo, ghi) && gran_ok(gc_, glo, ghi))) break;
      if (spin_out(t0)) break;
    }
    *(LAS f32x2*)(L + L_S1 + tid * 8) = f32x2{bflo(ga_[0]), bflo(ga_[2])};
    *(LAS f32x2*)(L + L_G1 + tid * 8) = f32x2{rbf(1.f + bflo(gc_[0])), rbf(1.f + bflo(gc_[2]))};
  }
  STAMP(6);
  bar();
  {
    const int c8 = lane & 7, r8l = lane >> 3;
    const LAS f32x4* gp = (const LAS f32x4*)(L + L_G1 + (w * 64 + c8 * 8) * 4);
    const LAS f32x4* sp = (const LAS f32x4*)(L + L_S1 + (w * 64 + c8 * 8) * 4);
    const f32x4 g0 = gp[0], g1 = gp[1], s0 = sp[0], s1 = sp[1];
    LAS char* ab = L + L_AIMG + (2 * w + (c8 >> 2)) * 1024 + (c8 & 3) * 256 + ((r8l ^ c8) * 16);
#pragma unroll
    for (int n = 0; n < 10; ++n) {
      u32x4 o;
      o[0] = pk2(fmaf(xh[n][0], g0[0], s0[0]), fmaf(xh[n][1], g0[1], s0[1]));
      o[1] = pk2(fmaf(xh[n][2], g0[2], s0[2]), fmaf(xh[n][3], g0[3], s0[3]));
      o[2] = pk2(fmaf(xh[n][4], g1[0], s1[0]), fmaf(xh[n][5], g1[1], s1[1]));
      o[3] = pk2(fmaf(xh[n][6], g1[2], s1[2]), fmaf(xh[n][7], g1[3], s1[3]));
      int r8 = n + nrot; r8 = r8 >= 10 ? r8 - 10 : r8;
      *(LAS u32x4*)(ab + (r8 >> 1) * 16384 + (r8 & 1) * 128) = o;
    }
  }
  bar();
  STAMP(7);
  const int p0 = jt & 3, rotg = 4 * (T & 3), rotd = tt & 3;
  const u32 wov = (u32)((lane >> 3) * 1024 + ((((lane >> 2) & 1) ^ ((lane >> 4) & 1)) * 64) + (lane & 3) * 16);
  const int mrow = (lane >> 4) & 1, q4 = i16 >> 2;
  const u32 p1base = (u32)(mrow * 4096 + (4 * h + q4) * 128 + (lane & 3) * 8), p1x = (u32)((q4 >> 1) | (mrow << 1));
  const u32 p2base = (u32)(mrow * L_P2M + (4 * h + q4) * 96 + (lane & 3) * 8);

  if (!isC) {
    if (producer2) mod_issue();
    bar();
    dma_w1(12);
    waitv<0>(); bar();
    STAMP(8);
    if (producer2) {
      asm volatile("" : "+v"(wm[0]), "+v"(wm[1]), "+v"(wm[2]), "+v"(wm[3]), "+v"(cv));
      mod_finish(L_G1);
    }
    STAMP(9);
    bar();
#pragma unroll
    for (int q = 0; q < 16; ++q) { const int qq = (q + rot) & 15; dma16(rwo, wov, (u32)((128 * cw + 8 * qq) * 1024 + cb * 128), Lb + (u32)(L_WO + (16 * cw + qq) * 1024)); }
#pragma unroll
    for (int q = 0; q < 2; ++q) dma16(rx, (u32)((lane >> 3) * 1024 + (lane & 7) * 16), (u32)((64 * tt + 16 * cw + 8 * q) * 1024 + cb * 128), Lb + (u32)(L_XRES + (2 * cw + q) * 1024));
    const u32 p1v = (u32)((lane >> 3) * 3584 + ((((lane >> 1) & 3) ^ ((lane >> 4) & 1)) * 32) + (lane & 1) * 16);
    auto dma_p1 = [&](int k) {
      const u32 so = (u32)((32 * ((k + rotg) & 15) + 8 * cw) * 3584 + jt * 224 + p0 * 32);
      dma16(rwg, p1v, so, Lb + (u32)(k * L_P1B + cw * 1024));
      dma16(rwu, p1v ^ 64u, so, Lb + (u32)(k * L_P1B + 4096 + cw * 1024));
    };
    waitv<0>(); STAMP(10); bar();
#pragma unroll
    for (int k = 0; k < 8; ++k) dma_p1(k);
    {
      u32x4 gs_, gc_, gg_;
      const u64 t0 = __builtin_amdgcn_s_memrealtime();
      for (;;) {
        gs_ = ld128_sc1(rws, (u32)(t4 * 16), WS_MODG + 3 * 4096); gc_ = ld128_sc1(rws, (u32)(t4 * 16), WS_MODG + 4 * 4096);
        gg_ = ld128_sc1(rws, (u32)(tok * 16 + h * 3 * 4096), WS_MODG + 2 * 4096 + (u32)cb * 512u);
        if (__all(gran_ok(gs_, glo, ghi) && gran_ok(gc_, glo, ghi) && gran_ok(gg_, glo, ghi))) break;
        if (spin_out(t0)) break;
      }
      const float g2a = rbf(1.f + bflo(gc_[0])), g2b = rbf(1.f + bflo(gc_[2]));
      const int kk = (2 * t4) & 31;
      LAS char* gd = L + L_GS + (t4 >> 4) * 128 + ((kk >> 2) & 3) * 32 + ((((kk >> 4) << 2) | (kk & 3)) * 2);
      *(LAS u32*)gd = pk2(g2a, g2b);
      *(LAS u32*)(gd + 16) = (gs_[0] & 0xffffu) | (gs_[2] << 16);
      if ((t4 >> 5) == cb) *(LAS f32x2*)(L + L_G2C + tok * 8) = f32x2{g2a, g2b};
      if (cw == 0) *(LAS f32x2*)(L + (h ? L_GBV : L_GA) + tok * 8) = f32x2{bflo(gg_[0]), bflo(gg_[2])};
    }
    waitv<0>(); STAMP(11); bar();
#pragma unroll
    for (int k = 8; k < 16; ++k) dma_p1(k);
    bar();
    f32x4 ua, ub;
    auto ut_blocks = [&](LAS char* ba, LAS char* bb, int bstride, int tstride, int k0, int k1) {
      u32x4 fa[2], fb[2], gv[2];
      auto ld = [&](int k, int bi) {
        fa[bi] = trd2(ba + k * bstride, tstride); fb[bi] = trd2(bb + k * bstride, tstride);
        gv[bi] = *(const LAS u32x4*)(L + L_GS + ((k + rotg) & 15) * 128 + g * 32 + (lane & 1) * 16);
      };
      ld(k0, 0);
#pragma unroll
      for (int k = k0; k < k1; ++k) {
        if (k + 1 < k1) ld(k + 1, (k + 1 - k0) & 1);
        SB();
        ua = mfma16(gv[(k - k0) & 1], fa[(k - k0) & 1], ua); ub = mfma16(gv[(k - k0) & 1], fb[(k - k0) & 1], ub);
        SB();
      }
      asm volatile("" : "+v"(ua), "+v"(ub));
    };
    auto put_fix = [&](const f32x4& v, int slot, int m) {
      const u32 hi = pk2(v[0], v[1]), lo = pk2(v[0] - bflo(hi), v[1] - bfhi(hi));
      const u32 u1 = hi & 0xffffu, t1 = hi >> 16, u2 = lo & 0xffffu, t2 = lo >> 16;
      const u32x4 o = u32x4{u1 | (u1 << 16), u2 | (u2 << 16), t1 | (t1 << 16), t2 | (t2 << 16)};
      if (lane < 16) { LAS char* d = L + L_FIX + slot * 1024 + (16 * m + lane) * 16; *(LAS u32x4*)d = o; *(LAS u32x4*)(d + 512) = o; }
    };
    LAS char* u1a = L + ((4 * g + q4) * 128 + (lane & 3) * 8 + ((cw ^ (q4 >> 1)) * 32));
    LAS char* u1b = L + (4096 + (4 * g + q4) * 128 + (lane & 3) * 8 + ((cw ^ (q4 >> 1) ^ 2) * 32));
    ua = f32x4{0, 0, 0, 0}; ub = f32x4{0, 0, 0, 0};
    ut_blocks(u1a, u1b, L_P1B, 2048, 0, 8);
    waitv<0>(); STAMP(12); bar();
    u32 p2v[3];
#pragma unroll
    for (int i = 0; i < 3; ++i) { const int n = 64 * i + lane, row = n / 6, c = n - row * 6; p2v[i] = (u32)(row * 3584 + c * 16 + ((c >> 1) >= p0 ? 128 : 0)); }
    const rsrc_t rgu = (cw & 1) ? rwu : rwg;
    auto dma_p2 = [&](int k0, int k1) {
#pragma unroll
      for (int k = k0; k < k1; ++k)
        if ((k & 1) == (cw >> 1)) {
#pragma unroll
          for (int i = 0; i < 3; ++i) dma16(rgu, p2v[i], (u32)(((k + rotg) & 15) * 32 * 3584 + jt * 224), Lb + (u32)(k * L_P2B + (cw & 1) * L_P2M + i * 1024));
        }
    };
    dma_p2(0, 5);
    ut_blocks(u1a, u1b, L_P1B, 2048, 8, 16);
    put_fix(ua, cw, 0); put_fix(ub, cw, 1);
    STAMP(13); bar();
    dma_p2(5, 10);
    waitv<6>(); STAMP(14); bar();
    dma_p2(10, 15);
    LAS char* u2a = L + ((cw & 1) * L_P2M + (4 * g + q4) * 96 + (lane & 3) * 8 + (cw >> 1) * 32);
    LAS char* u2b = L + ((cw & 1) * L_P2M + (4 * g + q4) * 96 + (lane & 3) * 8 + 64);
    ua = f32x4{0, 0, 0, 0}; ub = f32x4{0, 0, 0, 0};
    ut_blocks(u2a, u2b, L_P2B, 1536, 0, 5);
    waitv<6>(); STAMP(15); bar();
    dma_p2(15, 16);
    auto dma_wd = [&](int u) {
#pragma unroll
      for (int q = 0; q < 8; ++q) {
        const int n = 8 * (u - 4) + q;
        const int jj = ((u < 4 ? u : n / 6) + rotd) & 3, r6 = n % 6;
        const int row = u < 4 ? 112 * jj + 16 * jj + 8 * q : 112 * jj + 8 * r6 + ((r6 >> 1) >= jj ? 64 : 0);
        dma16(rwd, wov, (u32)((448 * cw + row) * 1024 + cb * 128), Lb + (u32)(wd_unit_off(u) + cw * 8192 + q * 1024));
      }
    };
    dma_wd(0);
    ut_blocks(u2a, u2b, L_P2B, 1536, 5, 10);
    waitv<8>(); STAMP(16); bar();
    ut_blocks(u2a, u2b, L_P2B, 1536, 10, 16);
    put_fix(ua, cw >> 1, cw & 1); if (cw < 2) put_fix(ub, 2, cw & 1);
    bar();
    dma_wd(1);
    bar();
    dma_wd(2);
    waitv<16>();
    bar();
    dma_wd(3);
    waitv<16>(); STAMP(17); bar();
    dma_wd(4);
    waitv<16>(); bar();
    dma_wd(5);
    waitv<16>(); bar();
    dma_wd(6);
    waitv<16>(); STAMP(18); bar();
    waitv<8>(); bar();
    waitv<0>(); bar();
    bar();
  } else {
    f32x4 Kt[5], Qt[4], Vn[5];
#pragma unroll
    for (int r = 0; r < 5; ++r) { Kt[r] = f32x4{0, 0, 0, 0}; Vn[r] = f32x4{0, 0, 0, 0}; if (r < 4) Qt[r] = f32x4{0, 0, 0, 0}; }
    {
      u32x4 a[2][5], wl[2][3];
      auto ld = [&](int n, int bi) {
        LAS char* ab = L + L_AIMG + ((n + rot) & 15) * 1024 + g * 256 + ((i16 ^ (((n & 1) << 2) | g)) * 16);
#pragma unroll
        for (int rb = 0; rb < 5; ++rb) a[bi][rb] = *(const LAS u32x4*)(ab + rb * 16384);
        if (n < 6 || n >= 12) {
#pragma unroll
          for (int pp = 0; pp < 3; ++pp) wl[bi][pp] = trd2(w1rd + w1slot(n) + pp * 4096, 512);
        }
      };
      ld(0, 0); SB();
#pragma unroll
      for (int n = 0; n < 16; ++n) {
        if (n == 3) bar();
        if (n == 11) bar();
        if (n < 15) ld(n + 1, (n + 1) & 1);
        const bool rg = n >= 6 && n < 12;
        const u32x4 wq = rg ? wr[rg ? n - 6 : 0][0] : wl[n & 1][0], wk = rg ? wr[rg ? n - 6 : 0][1] : wl[n & 1][1], wv = rg ? wr[rg ? n - 6 : 0][2] : wl[n & 1][2];
#pragma unroll
        for (int rb = 0; rb < 5; ++rb) {
          Kt[rb] = mfma16(wk, a[n & 1][rb], Kt[rb]);
          Vn[rb] = mfma16(a[n & 1][rb], wv, Vn[rb]);
          if (rb > 0) Qt[rb - 1] = mfma16(wq, a[n & 1][rb], Qt[rb - 1]);
        }
#pragma unroll
        for (int k = 0; k < 11; ++k) { SGB_MFMA(1); SGB_DSR(1); }
        SGB_MFMA(3);
        SB();
      }
    }
    bar();
    STAMP(8);
    const int tb = cw & 1, kh = cw >> 1;
    {
      u32x2 kp[5], vp[5];
#pragma unroll
      for (int r = 0; r < 5; ++r) { kp[r] = u32x2{pk2(Kt[r][0], Kt[r][1]), pk2(Kt[r][2], Kt[r][3])}; vp[r] = u32x2{pk2(Vn[r][0], Vn[r][1]), pk2(Vn[r][2], Vn[r][3])}; }
      f32x4 m0, m1, m0f;
#pragma unroll
      for (int r = 0; r < 4; ++r) {
        const int d1 = i16 - 4 * g - r, d0 = d1 + 16;
        m0[r] = (d0 <= 14 && !(d0 & 1)) ? 0.f : -1e30f;
        m1[r] = (d1 >= 0 && d1 <= 14 && !(d1 & 1)) ? 0.f : -1e30f;
        m0f[r] = tt == 0 ? -1e30f : m0[r];
      }
      u32x2 op[4];
      const u32x4 ones = u32x4{0x3f803f80u, 0x3f803f80u, 0x3f803f80u, 0x3f803f80u};
#pragma unroll
      for (int qb = 0; qb < 4; ++qb) {
        const u32x2 qp = u32x2{pk2(Qt[qb][0], Qt[qb][1]), pk2(Qt[qb][2], Qt[qb][3])};
        const f32x4 s0 = mfma16k16(kp[qb], qp, qb == 0 ? m0f : m0), s1 = mfma16k16(kp[qb + 1], qp, m1);
        float mx = fmaxf(fmaxf(fmaxf(s0[0], s0[1]), fmaxf(s0[2], s0[3])), fmaxf(fmaxf(s1[0], s1[1]), fmaxf(s1[2], s1[3])));
        mx = swap16_max(mx); mx = swap32_max(mx);
        const float cs = 0.25f * 1.44269504088896f, nm = -mx * cs;
        float e0[4], e1[4];
#pragma unroll
        for (int r = 0; r < 4; ++r) { e0[r] = __builtin_amdgcn_exp2f(fmaf(s0[r], cs, nm)); e1[r] = __builtin_amdgcn_exp2f(fmaf(s1[r], cs, nm)); }
        const u32x4 pf = u32x4{pk2(e0[0], e0[1]), pk2(e0[2], e0[3]), pk2(e1[0], e1[1]), pk2(e1[2], e1[3])};
        const f32x4 z = f32x4{0, 0, 0, 0};
        const f32x4 ot = mfma16(u32x4{vp[qb][0], vp[qb][1], vp[qb + 1][0], vp[qb + 1][1]}, pf, z);
        const f32x4 sm = mfma16(ones, pf, z);
        const float ri = __builtin_amdgcn_rcpf(sm[0]);
        op[qb] = u32x2{pk2(ot[0] * ri, ot[1] * ri), pk2(ot[2] * ri, ot[3] * ri)};
      }
#pragma unroll
      for (int t2 = 0; t2 < 2; ++t2) {
        auto r0 = __builtin_amdgcn_permlane16_swap(op[2 * t2][0], op[2 * t2 + 1][0], false, false);
        auto r1 = __builtin_amdgcn_permlane16_swap(op[2 * t2][1], op[2 * t2 + 1][1], false, false);
        const u32 a0 = r0[0], b0 = r0[1], a1 = r1[0], b1 = r1[1];
        st128_sc1(u32x4{a0, a1, b0, b1}, rws, (u32)(lane * 16), WS_O + (u32)(((2 * tt + t2) * 32 + 4 * hg + cw) * 1024));
      }
    }
    STAMP(9);
    waitv<0>(); bar();
    if (tid == 0) st64_sc1(u32x2{tlo, thi}, rws, 0u, WS_FO + (u32)(tt * 256 + hg * 8));
    STAMP(10);
    wait_flags<8>(rws, WS_FO + (u32)(tt * 256), lane, tlo, thi);
    STAMP(11);
    u32 x1p[8];
    {
      u32x4 of[16];
      const u32 ob = WS_O + (u32)(((2 * tt + tb) * 32 + 16 * kh) * 1024);
#pragma unroll
      for (int s = 0; s < 16; ++s) of[s] = ld128_sc1(rws, (u32)(lane * 16), ob + (u32)(s * 1024));
      SB();
      LAS char* q0 = L + (L_WO + (256 * kh + 8 * h + q4) * 128 + mrow * 32 + (lane & 3) * 8);
      LAS char* qa = q0 + (q4 >> 1) * 64; LAS char* qb = q0 + ((q4 >> 1) ^ 1) * 64;
      f32x16 a0, a1;
#pragma unroll
      for (int r = 0; r < 16; ++r) { a0[r] = 0.f; a1[r] = 0.f; }
      u32x4 wl[2][2];
      wl[0][0] = trd2(qa, 512); wl[0][1] = trd2(qb, 512); SB();
#pragma unroll
      for (int s = 0; s < 16; ++s) {
        if (s < 15) { wl[(s + 1) & 1][0] = trd2(qa + (s + 1) * 2048, 512); wl[(s + 1) & 1][1] = trd2(qb + (s + 1) * 2048, 512); }
        a0 = mfma32(wl[s & 1][0], of[s], a0); a1 = mfma32(wl[s & 1][1], of[s], a1);
        SGB_MFMA(1); SGB_DSR(2); SGB_MFMA(1); SGB_DSR(2);
        SB();
      }
      u32x2 xres[4];
#pragma unroll
      for (int q = 0; q < 4; ++q) xres[q] = *(const LAS u32x2*)(L + L_XRES + (32 * tb + tok) * 128 + (32 * kh + 8 * q + 4 * h) * 2);
      const f32x16 snd = kh ? a0 : a1; f32x16 acc = kh ? a1 : a0;
#pragma unroll
      for (int q = 0; q < 4; ++q) *(LAS f32x4*)(L + L_RED2 + cw * 4096 + q * 1024 + lane * 16) = f32x4{snd[4 * q], snd[4 * q + 1], snd[4 * q + 2], snd[4 * q + 3]};
      STAMP(19);
      bar();
      STAMP(20);
      const float mu1 = ((const LAS float*)(L + L_MU1))[32 * tb + tok];
      float s1 = 0.f, s2 = 0.f; u32 yp[8];
#pragma unroll
      for (int q = 0; q < 4; ++q) {
        const f32x4 o4 = *(const LAS f32x4*)(L + L_RED2 + (cw ^ 2) * 4096 + q * 1024 + lane * 16);
        const u32x2 xq = xres[q];
        const f32x4 ga4 = *(const LAS f32x4*)(L + L_GA + (32 * kh + 8 * q + 4 * h) * 4), g24 = *(const LAS f32x4*)(L + L_G2C + (32 * kh + 8 * q + 4 * h) * 4);
        const float v0 = fmaf(ga4[0], acc[4 * q] + o4[0], bflo(xq[0])), v1 = fmaf(ga4[1], acc[4 * q + 1] + o4[1], bfhi(xq[0]));
        const float v2 = fmaf(ga4[2], acc[4 * q + 2] + o4[2], bflo(xq[1])), v3 = fmaf(ga4[3], acc[4 * q + 3] + o4[3], bfhi(xq[1]));
        x1p[2 * q] = pk2(v0, v1); x1p[2 * q + 1] = pk2(v2, v3);
        const float d0 = bflo(x1p[2 * q]) - mu1, d1 = bfhi(x1p[2 * q]) - mu1, d2 = bflo(x1p[2 * q + 1]) - mu1, d3 = bfhi(x1p[2 * q + 1]) - mu1;
        s1 += (d0 + d1) + (d2 + d3); s2 = fmaf(d0, d0, fmaf(d1, d1, fmaf(d2, d2, fmaf(d3, d3, s2))));
        yp[2 * q] = pk2(d0 * g24[0], d1 * g24[1]); yp[2 * q + 1] = pk2(d2 * g24[2], d3 * g24[3]);
      }
      const u32 yb = WS_Y + (u32)(((2 * tt + tb) * 32 + 4 * cb + 2 * kh) * 1024);
      st128_sc1(u32x4{yp[0], yp[1], yp[2], yp[3]}, rws, (u32)(lane * 16), yb);
      st128_sc1(u32x4{yp[4], yp[5], yp[6], yp[7]}, rws, (u32)(lane * 16), yb + 1024);
      s1 = swap32_add(s1, s1); s2 = swap32_add(s2, s2);
      if (lane < 32) st64_sc1(u32x2{u_(s1), u_(s2)}, rws, (u32)(tok * 8), WS_ST + (u32)(((2 * tt + tb) * 16 + 2 * cb + kh) * 256));
    }
    STAMP(12);
    waitv<0>(); bar();
    if (tid == 0) st64_sc1(u32x2{tlo, thi}, rws, 0u, WS_FY + (u32)(tt * 256 + cb * 8));
    STAMP(13);
    wait_flags<8>(rws, WS_FY + (u32)((2 * T + (cw >> 1)) * 256), lane, tlo, thi);
    STAMP(14);
    const int tb32 = 4 * T + cw;
    u32x2 stv[8];
#pragma unroll
    for (int q = 0; q < 8; ++q) stv[q] = ld64_sc1(rws, (u32)(tok * 8 + h * 2048), WS_ST + (u32)((tb32 * 16 + q) * 256));
    u32x4 yf[32];
#pragma unroll
    for (int s = 0; s < 32; ++s) yf[s] = ld128_sc1(rws, (u32)(lane * 16), WS_Y + (u32)((tb32 * 32 + ((s + 2 * rotg) & 31)) * 1024));
    SB();
    u32 mz, sz; float r2;
    {
      float s1 = 0.f, s2 = 0.f;
#pragma unroll
      for (int q = 0; q < 8; ++q) { s1 += f_(stv[q][0]); s2 += f_(stv[q][1]); }
      s1 = swap32_add(s1, s1); s2 = swap32_add(s2, s2);
      const float md = s1 * (1.f / 512.f);
      const float ve = fmaxf(s2 * (1.f / 512.f) - md * md, 0.f) + 1e-5f;
      const float sg = __builtin_amdgcn_sqrtf(ve); r2 = __builtin_amdgcn_rcpf(ve);
      const u32 hi = pk2(-md, sg), lo = pk2(-md - bflo(hi), sg - bfhi(hi));
      const u32 mm = (hi & 0xffffu) | (lo << 16), ss = (hi >> 16) | (lo & 0xffff0000u);
      mz = h ? 0u : mm; sz = h ? 0u : ss;
      asm volatile("" : "+v"(mz), "+v"(sz), "+v"(r2));
    }
    SB();
    auto act_store = [&](const f32x16& ac, int pp) {
      u32x4 o;
#pragma unroll
      for (int e = 0; e < 4; ++e) o[e] = pk2(fmaxf(ac[2 * e], 0.f) * r2 * ac[8 + 2 * e], fmaxf(ac[2 * e + 1], 0.f) * r2 * ac[9 + 2 * e]);
      st128_sc1(o, rws, (u32)(lane * 16), WS_ACT + (u32)((tb32 * 112 + 7 * jt + pp) * 1024));
    };
    {
      f32x16 acc[4];
#pragma unroll
      for (int q = 0; q < 4; ++q)
#pragma unroll
        for (int r = 0; r < 16; ++r) acc[q][r] = 0.f;
      LAS char* pa[4];
#pragma unroll
      for (int pp = 0; pp < 4; ++pp) pa[pp] = L + (p1base + (((u32)pp ^ p1x) * 32));
      u32x4 wf[2][4];
      auto ldw = [&](int st, int bi) {
#pragma unroll
        for (int pp = 0; pp < 4; ++pp) wf[bi][pp] = trd2(pa[pp] + (st >> 1) * L_P1B + (st & 1) * 2048, 1024);
      };
      ldw(0, 0); SB();
#pragma unroll
      for (int st = 0; st < 32; ++st) {
        if (st == 7 || st == 15 || st == 23) bar();
        if (st + 1 < 32) ldw(st + 1, (st + 1) & 1);
#pragma unroll
        for (int pp = 0; pp < 4; ++pp) acc[pp] = mfma32(wf[st & 1][pp], yf[st], acc[pp]);
#pragma unroll
        for (int pp = 0; pp < 4; ++pp) { SGB_MFMA(1); SGB_DSR(2); }
        SB();
      }
#pragma unroll
      for (int pp = 0; pp < 4; ++pp) acc[pp] = mfma32(*(const LAS u32x4*)(L + L_FIX + pp * 1024 + lane * 16), u32x4{mz, mz, sz, sz}, acc[pp]);
      bar();
      STAMP(15);
#pragma unroll
      for (int pp = 0; pp < 4; ++pp) act_store(acc[pp], p0 + pp);
    }
    u32x4 af[16][2];
    {
      f32x16 acc[3];
#pragma unroll
      for (int q = 0; q < 3; ++q)
#pragma unroll
        for (int r = 0; r < 16; ++r) acc[q][r] = 0.f;
      LAS char* pb = L + p2base;
      u32x4 wf[2][3];
      auto ldw = [&](int st, int bi) {
#pragma unroll
        for (int pp = 0; pp < 3; ++pp) wf[bi][pp] = trd2(pb + (st >> 1) * L_P2B + (st & 1) * 1536 + pp * 32, 768);
      };
      ldw(0, 0); SB();
#pragma unroll
      for (int st = 0; st < 32; ++st) {
        if (st == 7) { waitv<0>(); bar(); if (tid == 0) st64_sc1(u32x2{tlo, thi}, rws, 0u, WS_FA + (u32)(T * 256 + jt * 8)); }
        if (st == 15) bar();
        if (st == 23) bar();
        if (st == 16) { STAMP(23); wait_flags<4>(rws, WS_FA + (u32)(T * 256 + cw * 32), lane, tlo, thi); }
        if (st + 1 < 32) ldw(st + 1, (st + 1) & 1);
        if (st >= 16) {
          const int s = st - 16;
#pragma unroll
          for (int t2 = 0; t2 < 2; ++t2) af[s][t2] = ld128_sc1(rws, (u32)(lane * 16), WS_ACT + (u32)(((2 * tt + t2) * 112 + 28 * cw + 8 * (((s >> 2) + rotd) & 3) + (s & 3)) * 1024));
        }
#pragma unroll
        for (int pp = 0; pp < 3; ++pp) acc[pp] = mfma32(wf[st & 1][pp], yf[st], acc[pp]);
#pragma unroll
        for (int pp = 0; pp < 3; ++pp) { SGB_MFMA(1); SGB_DSR(2); }
        SB();
      }
#pragma unroll
      for (int pp = 0; pp < 3; ++pp) acc[pp] = mfma32(*(const LAS u32x4*)(L + L_FIX + pp * 1024 + lane * 16), u32x4{mz, mz, sz, sz}, acc[pp]);
      bar();
      STAMP(16);
#pragma unroll
      for (int pp = 0; pp < 3; ++pp) act_store(acc[pp], pp >= p0 ? pp + 4 : pp);
    }
    SB();
    {
      f32x16 acc[4];
#pragma unroll
      for (int q = 0; q < 4; ++q)
#pragma unroll
        for (int r = 0; r < 16; ++r) acc[q][r] = 0.f;
      LAS char* d0 = L + (cw * 8192 + (4 * h + q4) * 128 + mrow * 32 + (lane & 3) * 8);
      LAS char* da = d0 + (q4 >> 1) * 64; LAS char* db = d0 + ((q4 >> 1) ^ 1) * 64;
      u32x4 wl[2][2], a2[12][2];
      auto ldw = [&](int s, int bi) { const u32 o = wd_unit_off(s >> 2) + (s & 3) * 2048; wl[bi][0] = trd2(da + o, 1024); wl[bi][1] = trd2(db + o, 1024); };
      ldw(0, 0); SB();
#pragma unroll
      for (int s = 0; s < 28; ++s) {
        if (s == 3) { waitv<0>(); bar(); if (tid == 0) st64_sc1(u32x2{tlo, thi}, rws, 0u, WS_FA2 + (u32)(T * 256 + jt * 8)); }
        if (s == 7 || s == 11 || s == 15 || s == 19 || s == 23) bar();
        if (s == 12) {
          STAMP(17);
          wait_flags<4>(rws, WS_FA2 + (u32)(T * 256 + cw * 32), lane, tlo, thi);
          STAMP(21);
#pragma unroll
          for (int s2 = 0; s2 < 12; ++s2)
#pragma unroll
            for (int t2 = 0; t2 < 2; ++t2) {
              const int jj = (s2 / 3 + rotd) & 3, pp = s2 % 3;
              a2[s2][t2] = ld128_sc1(rws, (u32)(lane * 16), WS_ACT + (u32)(((2 * tt + t2) * 112 + 28 * cw + 7 * jj + (pp >= jj ? pp + 4 : pp)) * 1024));
            }
          SB();
        }
        if (s + 1 < 28) ldw(s + 1, (s + 1) & 1);
        const u32x4 b0 = s < 16 ? af[s < 16 ? s : 0][0] : a2[s < 16 ? 0 : s - 16][0], b1 = s < 16 ? af[s < 16 ? s : 0][1] : a2[s < 16 ? 0 : s - 16][1];
        acc[0] = mfma32(wl[s & 1][0], b0, acc[0]); acc[2] = mfma32(wl[s & 1][1], b0, acc[2]);
        acc[1] = mfma32(wl[s & 1][0], b1, acc[1]); acc[3] = mfma32(wl[s & 1][1], b1, acc[3]);
        SGB_MFMA(1); SGB_DSR(2); SGB_MFMA(1); SGB_DSR(2); SGB_MFMA(2);
        SB();
      }
      STAMP(22);
#pragma unroll
      for (int o = 0; o < 4; ++o) {
        if (o != cw) {
          const int sl = ((cw - o) & 3) - 1;
#pragma unroll
          for (int q = 0; q < 4; ++q) *(LAS f32x4*)(L + L_RED4 + (o * 3 + sl) * 4096 + q * 1024 + lane * 16) = f32x4{acc[o][4 * q], acc[o][4 * q + 1], acc[o][4 * q + 2], acc[o][4 * q + 3]};
        }
      }
      f32x16 mine = cw == 0 ? acc[0] : cw == 1 ? acc[1] : cw == 2 ? acc[2] : acc[3];
      bar();
      u32 ob[8];
#pragma unroll
      for (int q = 0; q < 4; ++q) {
        const LAS char* rp = L + L_RED4 + cw * 3 * 4096 + q * 1024 + lane * 16;
        const f32x4 r0 = *(const LAS f32x4*)rp, r1 = *(const LAS f32x4*)(rp + 4096), r2_ = *(const LAS f32x4*)(rp + 8192);
        const f32x4 gb4 = *(const LAS f32x4*)(L + L_GBV + (32 * kh + 8 * q + 4 * h) * 4);
        float v[4];
#pragma unroll
        for (int e = 0; e < 4; ++e) v[e] = (mine[4 * q + e] + r0[e]) + (r1[e] + r2_[e]);
        ob[2 * q] = pk2(fmaf(gb4[0], v[0], bflo(x1p[2 * q])), fmaf(gb4[1], v[1], bfhi(x1p[2 * q])));
        ob[2 * q + 1] = pk2(fmaf(gb4[2], v[2], bflo(x1p[2 * q + 1])), fmaf(gb4[3], v[3], bfhi(x1p[2 * q + 1])));
      }
#pragma unroll
      for (int m = 0; m < 2; ++m) {
        auto r0 = __builtin_amdgcn_permlane32_swap(ob[4 * m], ob[4 * m + 2], false, false);
        auto r1 = __builtin_amdgcn_permlane32_swap(ob[4 * m + 1], ob[4 * m + 3], false, false);
        const u32 a0 = r0[0], b0 = r0[1], a1 = r1[0], b1 = r1[1];
        __builtin_amdgcn_raw_buffer_store_b128(u32x4{a0, a1, b0, b1}, rout, (u32)((64 * tt + 32 * tb + tok) * 1024 + (64 * cb + 32 * kh + 16 * m + 8 * h) * 2), 0, 0);
      }
    }
    STAMP(18);
    if (b == 0 && cw == 0) {
      const u64 t0 = __builtin_amdgcn_s_memrealtime();
      for (;;) {
        bool ok = true;
#pragma unroll
        for (int k = 0; k < 4; ++k) { const int f = 64 * k + lane; const u32x2 v = ld64_sc1(rws, (u32)((f >> 3) * 256 + (f & 7) * 8), WS_FO); ok = ok && v[0] == tlo && v[1] == thi; }
        if (__all(ok)) break;
        if (spin_out(t0)) break;
      }
      if (lane == 0) st64_sc1(u32x2{tlo, thi}, rws, 0u, WS_E);
    }
  }
}

extern "C" void kernel_launch(void* const* d_in, const int* in_sizes, int n_in, void* d_out, int out_size, void* d_ws, size_t ws_size, hipStream_t stream) {
  Params p;
  p.x = d_in[0]; p.wqkv = d_in[1]; p.wo = d_in[2]; p.wg = d_in[4]; p.wu = d_in[5]; p.wd = d_in[6]; p.c = d_in[7]; p.wmod = d_in[8];
  p.out = d_out; p.ws = d_ws;
  (void)hipFuncSetAttribute((const void*)fused_block, hipFuncAttributeMaxDynamicSharedMemorySize, LDS_BYTES);
  fused_block<<<256, 512, LDS_BYTES, stream>>>(p);
}
```

```cpp
#include <hip/hip_runtime.h>
#include <stdint.h>

typedef unsigned short u16;
typedef unsigned u32;
typedef unsigned long long u64;
typedef u32 u32x2 __attribute__((ext_vector_type(2)));
typedef u32 u32x4 __attribute__((ext_vector_type(4)));
typedef short s16x4 __attribute__((ext_vector_type(4)));
typedef short s16x8 __attribute__((ext_vector_type(8)));
typedef __bf16 bf16x2 __attribute__((ext_vector_type(2)));
typedef __bf16 bf16x8 __attribute__((ext_vector_type(8)));
typedef float f32x2 __attribute__((ext_vector_type(2)));
typedef float f32x4 __attribute__((ext_vector_type(4)));
typedef float f32x16 __attribute__((ext_vector_type(16)));
typedef __amdgpu_buffer_rsrc_t rsrc_t;
#define LAS __attribute__((address_space(3)))
#define DI __device__ __forceinline__

#define TOUCH 2
constexpr unsigned CODE_BYTES = 50000;
#define DIAG 0
#define XMAP 2

constexpr int S_ = 2048, D_ = 512, F_ = 1792;
constexpr u32 WS_E = 0, WS_MODG = 4096, WS_FO = 32768, WS_FY = 40960, WS_FA = 49152, WS_FA2 = 53248, WS_DBG = 65536;
constexpr u32 WS_O = 1u << 20, WS_Y = 3u << 20, WS_ST = 5u << 20, WS_ACT = 6u << 20, WS_BYTES = 13u << 20;
constexpr u32 L_AIMG = 0, L_RW = 81920, L_W1S = 12288;
constexpr u32 L_LNP = 73728, L_PRED = L_LNP + 5120, L_RSNB = L_PRED + 1088;
constexpr u32 L_XRES = 65536, L_WO = 73728, L_RED2 = 139264;
constexpr u32 L_P1B = 8192, L_P2B = 6400, L_P2M = 3200;
constexpr u32 L_RED4 = 65536;
constexpr u32 L_MISC = 155648, L_G1 = L_MISC, L_S1 = L_MISC + 2048, L_FIX = L_MISC;
constexpr u32 L_MU1 = L_MISC + 4096, L_GA = L_MU1 + 256, L_G2C = L_GA + 256, L_GBV = L_G2C + 256, L_GS = L_MISC + 5120;
constexpr u32 LDS_BYTES = 163840;
__host__ __device__ constexpr u32 wd_unit_off(int u) { return u == 0 || u == 4 ? 102400u : (u == 1 || u == 5 ? 0u : (u == 2 || u == 6 ? 32768u : 65536u)); }

template <int N> struct ic { static constexpr int value = N; };
struct Params { const void *x, *wqkv, *wo, *wg, *wu, *wd, *c, *wmod; void *out; void *ws; };

DI rsrc_t mk(const void* p, u32 bytes) { return __builtin_amdgcn_make_buffer_rsrc((void*)p, 0, bytes, 0x00020000); }
DI float bflo(u32 v) { return __builtin_bit_cast(float, v << 16); }
DI float bfhi(u32 v) { return __builtin_bit_cast(float, v & 0xffff0000u); }
DI u32 pk2(float a, float b) { bf16x2 r; r[0] = (__bf16)a; r[1] = (__bf16)b; return __builtin_bit_cast(u32, r); }
DI float rbf(float a) { return bflo(pk2(a, a)); }
DI f32x4 mfma16(u32x4 a, u32x4 b, f32x4 c) { return __builtin_amdgcn_mfma_f32_16x16x32_bf16(__builtin_bit_cast(bf16x8, a), __builtin_bit_cast(bf16x8, b), c, 0, 0, 0); }
DI f32x4 mfma16k16(u32x2 a, u32x2 b, f32x4 c) { return __builtin_amdgcn_mfma_f32_16x16x16bf16_1k(__builtin_bit_cast(s16x4, a), __builtin_bit_cast(s16x4, b), c, 0, 0, 0); }
DI f32x16 mfma32(u32x4 a, u32x4 b, f32x16 c) { return __builtin_amdgcn_mfma_f32_32x32x16_bf16(__builtin_bit_cast(bf16x8, a), __builtin_bit_cast(bf16x8, b), c, 0, 0, 0); }
DI u32x2 trd(LAS char* p) { return __builtin_bit_cast(u32x2, __builtin_amdgcn_ds_read_tr16_b64_v4i16((LAS s16x4*)p)); }
DI u32x4 trd2(LAS char* p, int d) { u32x2 a = trd(p), b = trd(p + d); return u32x4{a[0], a[1], b[0], b[1]}; }
DI void dma16(rsrc_t rs, u32 voff, u32 soff, u32 lds) {
  asm volatile("s_mov_b32 m0, %0\n\ts_nop 0\n\tbuffer_load_dwordx4 %1, %2, %3 offen lds" :: "s"(lds), "v"(voff), "s"(rs), "s"(soff) : "m0", "memory");
}
template <int OFF> DI void ald128(u32x4& d, rsrc_t rs, u32 voff, u32 soff) {
  asm volatile("buffer_load_dwordx4 %0, %1, %2, %3 offen offset:%c4" : "=v"(d) : "v"(voff), "s"(rs), "s"(soff), "i"(OFF) : "memory");
}
template <int OFF> DI void ald128_sc1(u32x4& d, rsrc_t rs, u32 voff, u32 soff) {
  asm volatile("buffer_load_dwordx4 %0, %1, %2, %3 offen offset:%c4 sc1" : "=v"(d) : "v"(voff), "s"(rs), "s"(soff), "i"(OFF) : "memory");
}
DI void ald64_sc1(u32x2& d, rsrc_t rs, u32 voff, u32 soff) {
  asm volatile("buffer_load_dwordx2 %0, %1, %2, %3 offen sc1" : "=v"(d) : "v"(voff), "s"(rs), "s"(soff) : "memory");
}
DI void ald64(u32x2& d, rsrc_t rs, u32 voff, u32 soff) {
  asm volatile("buffer_load_dwordx2 %0, %1, %2, %3 offen" : "=v"(d) : "v"(voff), "s"(rs), "s"(soff) : "memory");
}
DI void warm(u32& junk, rsrc_t rs, u32 voff, u32 soff) {
  asm volatile("buffer_load_dword %0, %1, %2, %3 offen" : "+v"(junk) : "v"(voff), "s"(rs), "s"(soff) : "memory");
}
template <int N> DI void waitv() { asm volatile("s_waitcnt vmcnt(%c0)" :: "i"(N) : "memory"); }
template <int N> DI void waitv4(u32x4& a, u32x4& b, u32x4& c, u32x4& d) { asm volatile("s_waitcnt vmcnt(%c4)" : "+v"(a), "+v"(b), "+v"(c), "+v"(d) : "i"(N) : "memory"); }
template <int N> DI void waitv1(u32x4& a) { asm volatile("s_waitcnt vmcnt(%c1)" : "+v"(a) : "i"(N) : "memory"); }
DI void bar() { asm volatile("s_waitcnt lgkmcnt(0)\n\ts_barrier" ::: "memory"); }
DI u32x4 ld128_sc1(rsrc_t rs, u32 voff, u32 soff) { return __builtin_amdgcn_raw_buffer_load_b128(rs, voff, soff, 16); }
DI u32x2 ld64_sc1(rsrc_t rs, u32 voff, u32 soff) { return __builtin_amdgcn_raw_buffer_load_b64(rs, voff, soff, 16); }
DI void st128_sc1(u32x4 v, rsrc_t rs, u32 voff, u32 soff) { __builtin_amdgcn_raw_buffer_store_b128(v, rs, voff, soff, 16); }
DI void st64_sc1(u32x2 v, rsrc_t rs, u32 voff, u32 soff) { __builtin_amdgcn_raw_buffer_store_b64(v, rs, voff, soff, 16); }
DI float f_(u32 v) { return __builtin_bit_cast(float, v); }
DI u32 u_(float v) { return __builtin_bit_cast(u32, v); }
DI float swap16_add(float a, float b) { auto r = __builtin_amdgcn_permlane16_swap(u_(a), u_(b), false, false); u32 r0 = r[0], r1 = r[1]; return f_(r0) + f_(r1); }
DI float swap32_add(float a, float b) { auto r = __builtin_amdgcn_permlane32_swap(u_(a), u_(b), false, false); u32 r0 = r[0], r1 = r[1]; return f_(r0) + f_(r1); }
DI float swap16_max(float a) { auto r = __builtin_amdgcn_permlane16_swap(u_(a), u_(a), false, false); u32 r0 = r[0], r1 = r[1]; return fmaxf(f_(r0), f_(r1)); }
DI float swap32_max(float a) { auto r = __builtin_amdgcn_permlane32_swap(u_(a), u_(a), false, false); u32 r0 = r[0], r1 = r[1]; return fmaxf(f_(r0), f_(r1)); }
template <int CTRL> DI float dpp_add(float a) { return a + f_(__builtin_amdgcn_update_dpp(0, u_(a), CTRL, 0xf, 0xf, true)); }
DI float dot2(u32 a, u32 b, float c) { return __builtin_amdgcn_fdot2_f32_bf16(__builtin_bit_cast(bf16x2, a), __builtin_bit_cast(bf16x2, b), c, false); }

constexpr u64 SPIN_TICKS = 20000;
DI bool spin_out(u64 t0) { __builtin_amdgcn_s_sleep(1); return (__builtin_amdgcn_s_memrealtime() - t0) > SPIN_TICKS; }

#define SB() __builtin_amdgcn_sched_barrier(0)
#define SGB_MFMA(n) __builtin_amdgcn_sched_group_barrier(0x8, n, 0)
#define SGB_DSR(n) __builtin_amdgcn_sched_group_barrier(0x100, n, 0)
#define SGB_DSW(n) __builtin_amdgcn_sched_group_barrier(0x200, n, 0)
#define STAMP(i) do {} while (0)

template <int NF> DI void wait_flags(rsrc_t rws, u32 off, int lane, u32 tlo, u32 thi) {
  const u32 vo = (u32)(lane & (NF - 1)) * 8u;
  const u64 t0 = __builtin_amdgcn_s_memrealtime();
  for (;;) {
    u32x2 f = ld64_sc1(rws, vo, off);
    if (__all(f[0] == tlo && f[1] == thi)) break;
    if (spin_out(t0)) break;
  }
  __builtin_amdgcn_fence(__ATOMIC_ACQUIRE, "wavefront");
}
DI bool gran_ok(u32x4 g, u32 glo, u32 ghi) { return (g[0] >> 16) == glo && g[1] == ghi && (g[2] >> 16) == glo && g[3] == ghi; }

__global__ __launch_bounds__(512, 2) void fused_block(Params p) {
  extern __shared__ __attribute__((aligned(16))) char smem_[];
  LAS char* L = (LAS char*)smem_;
  const u32 Lb = (u32)(size_t)L;
  const int tid = threadIdx.x, lane = tid & 63;
  const int w = __builtin_amdgcn_readfirstlane(tid >> 6), cw = w & 3;
  const bool isC = w < 4;
  const int b = blockIdx.x, xcd = b & 7, idx = b >> 3;
  const int tt = 8 * (xcd >> 1) + (idx >> 2), hgl = idx & 3, hg = 4 * (xcd & 1) + hgl;
  const int jt = 8 * (xcd & 1) + 4 * (tt & 1) + hgl;
  const int T = tt >> 1, cb = hg;
  STAMP(0);
  const rsrc_t rx = mk(p.x, S_ * D_ * 2), rw1 = mk(p.wqkv, D_ * 3 * D_ * 2), rwo = mk(p.wo, D_ * D_ * 2);
  const rsrc_t rwg = mk(p.wg, D_ * F_ * 2), rwu = mk(p.wu, D_ * F_ * 2), rwd = mk(p.wd, F_ * D_ * 2);
  const rsrc_t rc = mk(p.c, D_ * 2), rwm = mk(p.wmod, D_ * 6 * D_ * 2), rout = mk(p.out, S_ * D_ * 2), rws = mk(p.ws, WS_BYTES);
  const int g = lane >> 4, i16 = lane & 15, h = lane >> 5, tok = lane & 31, t4 = tid & 255;

  u32x2 ev; ald64_sc1(ev, rws, 0u, WS_E);
  const bool producer = isC && idx < 8, producer2 = !isC && idx >= 8 && idx < 24;
  const int cbk = idx < 8 ? xcd * 8 + idx : 64 + xcd * 16 + (idx - 8);
  u32x2 cv; u32x4 wm[4];
  auto mod_issue = [&]() {
#pragma unroll
    for (int i = 0; i < 4; ++i) ald128<0>(wm[i], rwm, (u32)((t4 >> 1) * 4 * 6144 + (t4 & 1) * 16), (u32)(i * 6144 + cbk * 32));
    ald64(cv, rc, (u32)((t4 >> 1) * 8), 0u);
  };
  if (producer) mod_issue();
  u32x4 xr[10];
  const int nrot = 2 * hgl;
#pragma unroll
  for (int n = 0; n < 10; ++n) {
    int r8 = n + nrot; r8 = r8 >= 10 ? r8 - 10 : r8;
    int row = 64 * tt - 16 + 8 * r8 + (lane >> 3); row = row < 0 ? 0 : row;
    ald128<0>(xr[n], rx, (u32)(row * 1024 + (lane & 7) * 16), (u32)(w * 128));
  }
  const int rot = 2 * (tt & 7);
  const u32 w1v = (u32)((lane >> 3) * 3072 + (((((lane >> 1) & 3) ^ ((lane >> 4) & 1)) ^ ((cw & 1) << 1)) * 32) + (lane & 1) * 16);
  auto w1slot = [](int n) { return n < 6 ? L_RW + n * L_W1S : (n < 12 ? (n - 6) * L_W1S : L_RW + (n - 12) * L_W1S); };
  auto dma_w1 = [&](int n0) {
#pragma unroll
    for (int n = n0; n < n0 + 4; ++n) {
      const int ks = (n + rot) & 15;
#pragma unroll
      for (int pp = 0; pp < 3; ++pp) dma16(rw1, w1v, (u32)((32 * ks + 8 * cw) * 3072 + pp * 1024 + hg * 128), Lb + (u32)(w1slot(n) + pp * 4096 + cw * 1024));
    }
  };
  u32 junk = 0;
  if (!isC) {
    u64 pc; asm volatile("s_getpc_b64 %0" : "=s"(pc));
    const rsrc_t rcode = mk((const void*)(pc & ~(u64)255), CODE_BYTES);
    warm(junk, rcode, (u32)((idx * 4 + cw) * 4 + (lane & 3)) * 128u, 0u);
  }
  if (!isC) dma_w1(0);
  STAMP(1);
  u32 tlo, thi;
  auto mod_finish = [&](u32 pred) {
    float a8[8];
#pragma unroll
    for (int e = 0; e < 8; ++e) a8[e] = 0.f;
#pragma unroll
    for (int i = 0; i < 4; ++i) {
      const float cf = (i & 1) ? bfhi(cv[i >> 1]) : bflo(cv[i >> 1]);
      const float sv = rbf(cf * __builtin_amdgcn_rcpf(1.f + __expf(-cf)));
#pragma unroll
      for (int e = 0; e < 4; ++e) { a8[2 * e] = fmaf(sv, bflo(wm[i][e]), a8[2 * e]); a8[2 * e + 1] = fmaf(sv, bfhi(wm[i][e]), a8[2 * e + 1]); }
    }
#pragma unroll
    for (int e = 0; e < 8; ++e) { a8[e] = dpp_add<0x122>(a8[e]); a8[e] = dpp_add<0x124>(a8[e]); a8[e] = dpp_add<0x128>(a8[e]); }
    if (i16 < 2) {
      LAS f32x4* d = (LAS f32x4*)(L + pred + (cw * 4 + g) * 64 + i16 * 32);
      d[0] = f32x4{a8[0], a8[1], a8[2], a8[3]}; d[1] = f32x4{a8[4], a8[5], a8[6], a8[7]};
    }
    asm volatile("s_waitcnt lgkmcnt(0)" ::: "memory");
    if (lane == 0) *(volatile LAS u32x2*)(L + pred + 1024 + cw * 8) = u32x2{tlo, thi};
    if (cw == 0) {
      const u64 t0 = __builtin_amdgcn_s_memrealtime();
      for (;;) {
        const u32x2 tg = *(volatile LAS u32x2*)(L + pred + 1024 + (lane & 3) * 8);
        if (__all(tg[0] == tlo && tg[1] == thi)) break;
        if ((__builtin_amdgcn_s_memrealtime() - t0) > SPIN_TICKS) break;
      }
      asm volatile("" ::: "memory");
      LAS float* s = (LAS float*)(L + pred) + g * 64 + i16;
      float v = (s[0] + s[16]) + (s[32] + s[48]);
      v = swap16_add(v, v); v = swap32_add(v, v);
      const u32 mb = pk2(v, v) & 0xffffu;
      if (lane < 16) st64_sc1(u32x2{mb | (tlo << 16), (tlo >> 16) | (thi << 16)}, rws, (u32)(lane * 8), WS_MODG + (u32)cbk * 128u);
    }
  };
  if (producer) {
    asm volatile("s_waitcnt vmcnt(10)" : "+v"(wm[0]), "+v"(wm[1]), "+v"(wm[2]), "+v"(wm[3]), "+v"(cv), "+v"(ev) :: "memory");
    const u64 e1 = (((u64)__builtin_amdgcn_readfirstlane(ev[1]) << 32) | (u64)__builtin_amdgcn_readfirstlane(ev[0])) + 1ull;
    tlo = (u32)e1; thi = (u32)(e1 >> 32);
    mod_finish(L_PRED);
  }
  STAMP(2);
  if (isC) { if (producer && w == 0) waitv<1>(); else waitv<0>(); } else waitv<12 + ((TOUCH & 1) ? 2 : 0) + ((TOUCH & 2) ? 1 : 0)>();
#pragma unroll
  for (int n = 0; n < 10; n += 2) asm volatile("" : "+v"(xr[n]), "+v"(xr[n + 1]));
  asm volatile("" : "+v"(ev));
  {
    const u64 e1 = (((u64)__builtin_amdgcn_readfirstlane(ev[1]) << 32) | (u64)__builtin_amdgcn_readfirstlane(ev[0])) + 1ull;
    tlo = (u32)e1; thi = (u32)(e1 >> 32);
  }
  const u32 glo = tlo & 0xffffu, ghi = (tlo >> 16) | (thi << 16);
  STAMP(3);
  float xh[10][8];
  {
    const int c8 = lane & 7, r8l = lane >> 3;
    float pv[10];
#pragma unroll
    for (int n = 0; n < 10; ++n) {
      float s1 = 0.f, s2 = 0.f;
#pragma unroll
      for (int e = 0; e < 4; ++e) { s1 = dot2(xr[n][e], 0x3f803f80u, s1); s2 = dot2(xr[n][e], xr[n][e], s2); }
      const float keep = (c8 & 1) ? s2 : s1, give = (c8 & 1) ? s1 : s2;
      float v = keep + f_(__builtin_amdgcn_update_dpp(0, u_(give), 0xB1, 0xf, 0xf, true));
      v = dpp_add<0x4E>(v);
      pv[n] = v + f_(__builtin_amdgcn_update_dpp(0, u_(v), 0x104, 0xf, 0xf, true));
    }
    if (c8 < 2) {
#pragma unroll
      for (int n = 0; n < 10; ++n) ((LAS float*)(L + L_LNP))[(n * 8 + r8l) * 16 + w * 2 + c8] = pv[n];
    }
    bar();
    if (!isC) dma_w1(4);
#pragma unroll
    for (int k = 0; k < 2; ++k) {
      const int n = (k == 1 && c8 < 2) ? 8 + c8 : c8;
      const LAS f32x4* q = (const LAS f32x4*)(L + L_LNP + (n * 8 + r8l) * 64);
      const f32x4 a = q[0] + q[1], c4 = q[2] + q[3];
      const float s1 = (a[0] + a[2]) + (c4[0] + c4[2]), s2 = (a[1] + a[3]) + (c4[1] + c4[3]);
      const float mu = s1 * (1.f / 512.f);
      const float var = fmaxf(s2 * (1.f / 512.f) - mu * mu, 0.f);
      const float rs = __builtin_amdgcn_rsqf(var + 1e-5f);
      *(LAS f32x2*)(L + L_RSNB + (n * 8 + r8l) * 8) = f32x2{rs, -mu * rs};
      int r8 = n + nrot; r8 = r8 >= 10 ? r8 - 10 : r8;
      if (r8 >= 2 && w == 0) ((LAS float*)(L + L_MU1))[(r8 - 2) * 8 + r8l] = mu;
    }
    asm volatile("s_waitcnt lgkmcnt(0)" ::: "memory");
#pragma unroll
    for (int n = 0; n < 10; ++n) {
      const f32x2 rn = *(const LAS f32x2*)(L + L_RSNB + (n * 8 + r8l) * 8);
#pragma unroll
      for (int e = 0; e < 4; ++e) { xh[n][2 * e] = fmaf(bflo(xr[n][e]), rn[0], rn[1]); xh[n][2 * e + 1] = fmaf(bfhi(xr[n][e]), rn[0], rn[1]); }
    }
  }
#pragma unroll
  for (int n = 0; n < 10; ++n)
    asm volatile("" : "+v"(xh[n][0]), "+v"(xh[n][1]), "+v"(xh[n][2]), "+v"(xh[n][3]), "+v"(xh[n][4]), "+v"(xh[n][5]), "+v"(xh[n][6]), "+v"(xh[n][7]));
  STAMP(4);
  if (!isC) { dma_w1(8); waitv<0>(); }
  asm volatile("" :: "v"(junk));
  bar();
  u32x4 wr[6][3];
  LAS char* w1rd = L + ((8 * g + (i16 >> 2)) * 128 + ((cw ^ (((i16 >> 3) & 1) | ((g & 1) << 1))) * 32) + (lane & 3) * 8);
  if (isC) {
#pragma unroll
    for (int n = 0; n < 6; ++n)
#pragma unroll
      for (int pp = 0; pp < 3; ++pp) wr[n][pp] = trd2(w1rd + n * L_W1S + pp * 4096, 512);
    STAMP(5);
    u32x4 ga_, gc_;
    const u64 t0 = __builtin_amdgcn_s_memrealtime();
    for (;;) {
      ga_ = ld128_sc1(rws, (u32)(tid * 16), WS_MODG); gc_ = ld128_sc1(rws, (u32)(tid * 16), WS_MODG + 4096);
      if (__all(gran_ok(ga_, glo, ghi) && gran_ok(gc_, glo, ghi))) break;
      if (spin_out(t0)) break;
    }
    *(LAS f32x2*)(L + L_S1 + tid * 8) = f32x2{bflo(ga_[0]), bflo(ga_[2])};
    *(LAS f32x2*)(L + L_G1 + tid * 8) = f32x2{rbf(1.f + bflo(gc_[0])), rbf(1.f + bflo(gc_[2]))};
  }
  STAMP(6);
  bar();
  {
    const int c8 = lane & 7, r8l = lane >> 3;
    const LAS f32x4* gp = (const LAS f32x4*)(L + L_G1 + (w * 64 + c8 * 8) * 4);
    const LAS f32x4* sp = (const LAS f32x4*)(L + L_S1 + (w * 64 + c8 * 8) * 4);
    const f32x4 g0 = gp[0], g1 = gp[1], s0 = sp[0], s1 = sp[1];
    LAS char* ab = L + L_AIMG + (2 * w + (c8 >> 2)) * 1024 + (c8 & 3) * 256 + ((r8l ^ c8) * 16);
#pragma unroll
    for (int n = 0; n < 10; ++n) {
      u32x4 o;
      o[0] = pk2(fmaf(xh[n][0], g0[0], s0[0]), fmaf(xh[n][1], g0[1], s0[1]));
      o[1] = pk2(fmaf(xh[n][2], g0[2], s0[2]), fmaf(xh[n][3], g0[3], s0[3]));
      o[2] = pk2(fmaf(xh[n][4], g1[0], s1[0]), fmaf(xh[n][5], g1[1], s1[1]));
      o[3] = pk2(fmaf(xh[n][6], g1[2], s1[2]), fmaf(xh[n][7], g1[3], s1[3]));
      int r8 = n + nrot; r8 = r8 >= 10 ? r8 - 10 : r8;
      *(LAS u32x4*)(ab + (r8 >> 1) * 16384 + (r8 & 1) * 128) = o;
    }
  }
  bar();
  STAMP(7);
  const int p0 = jt & 3, rotg = 4 * (T & 3), rotd = tt & 3;
  const u32 wov = (u32)((lane >> 3) * 1024 + ((((lane >> 2) & 1) ^ ((lane >> 4) & 1)) * 64) + (lane & 3) * 16);
  const int mrow = (lane >> 4) & 1, q4 = i16 >> 2;
  const u32 p1base = (u32)(mrow * 4096 + (4 * h + q4) * 128 + (lane & 3) * 8), p1x = (u32)((q4 >> 1) | (mrow << 1));
  const u32 p2base = (u32)(mrow * L_P2M + (4 * h + q4) * 96 + (lane & 3) * 8);

  if (!isC) {
    if (producer2) mod_issue();
    bar();
    dma_w1(12);
    waitv<0>(); bar();
    STAMP(8);
    if (producer2) {
      asm volatile("" : "+v"(wm[0]), "+v"(wm[1]), "+v"(wm[2]), "+v"(wm[3]), "+v"(cv));
      mod_finish(L_G1);
    }
    STAMP(9);
    bar();
#pragma unroll
    for (int q = 0; q < 16; ++q) { const int qq = (q + rot) & 15; dma16(rwo, wov, (u32)((128 * cw + 8 * qq) * 1024 + cb * 128), Lb + (u32)(L_WO + (16 * cw + qq) * 1024)); }
#pragma unroll
    for (int q = 0; q < 2; ++q) dma16(rx, (u32)((lane >> 3) * 1024 + (lane & 7) * 16), (u32)((64 * tt + 16 * cw + 8 * q) * 1024 + cb * 128), Lb + (u32)(L_XRES + (2 * cw + q) * 1024));
    const u32 p1v = (u32)((lane >> 3) * 3584 + ((((lane >> 1) & 3) ^ ((lane >> 4) & 1)) * 32) + (lane & 1) * 16);
    auto dma_p1 = [&](int k) {
      const u32 so = (u32)((32 * ((k + rotg) & 15) + 8 * cw) * 3584 + jt * 224 + p0 * 32);
      dma16(rwg, p1v, so, Lb + (u32)(k * L_P1B + cw * 1024));
      dma16(rwu, p1v ^ 64u, so, Lb + (u32)(k * L_P1B + 4096 + cw * 1024));
    };
    waitv<0>(); STAMP(10); bar();
#pragma unroll
    for (int k = 0; k < 8; ++k) dma_p1(k);
    {
      u32x4 gs_, gc_, gg_;
      const u64 t0 = __builtin_amdgcn_s_memrealtime();
      for (;;) {
        gs_ = ld128_sc1(rws, (u32)(t4 * 16), WS_MODG + 3 * 4096); gc_ = ld128_sc1(rws, (u32)(t4 * 16), WS_MODG + 4 * 4096);
        gg_ = ld128_sc1(rws, (u32)(tok * 16 + h * 3 * 4096), WS_MODG + 2 * 4096 + (u32)cb * 512u);
        if (__all(gran_ok(gs_, glo, ghi) && gran_ok(gc_, glo, ghi) && gran_ok(gg_, glo, ghi))) break;
        if (spin_out(t0)) break;
      }
      const float g2a = rbf(1.f + bflo(gc_[0])), g2b = rbf(1.f + bflo(gc_[2]));
      const int kk = (2 * t4) & 31;
      LAS char* gd = L + L_GS + (t4 >> 4) * 128 + ((kk >> 2) & 3) * 32 + ((((kk >> 4) << 2) | (kk & 3)) * 2);
      *(LAS u32*)gd = pk2(g2a, g2b);
      *(LAS u32*)(gd + 16) = (gs_[0] & 0xffffu) | (gs_[2] << 16);
      if ((t4 >> 5) == cb) *(LAS f32x2*)(L + L_G2C + tok * 8) = f32x2{g2a, g2b};
      if (cw == 0) *(LAS f32x2*)(L + (h ? L_GBV : L_GA) + tok * 8) = f32x2{bflo(gg_[0]), bflo(gg_[2])};
    }
    waitv<0>(); STAMP(11); bar();
#pragma unroll
    for (int k = 8; k < 16; ++k) dma_p1(k);
    bar();
    f32x4 ua, ub;
    auto ut_blocks = [&](LAS char* ba, LAS char* bb, int bstride, int tstride, int k0, int k1) {
      u32x4 fa[2], fb[2], gv[2];
      auto ld = [&](int k, int bi) {
        fa[bi] = trd2(ba + k * bstride, tstride); fb[bi] = trd2(bb + k * bstride, tstride);
        gv[bi] = *(const LAS u32x4*)(L + L_GS + ((k + rotg) & 15) * 128 + g * 32 + (lane & 1) * 16);
      };
      ld(k0, 0);
#pragma unroll
      for (int k = k0; k < k1; ++k) {
        if (k + 1 < k1) ld(k + 1, (k + 1 - k0) & 1);
        SB();
        ua = mfma16(gv[(k - k0) & 1], fa[(k - k0) & 1], ua); ub = mfma16(gv[(k - k0) & 1], fb[(k - k0) & 1], ub);
        SB();
      }
      asm volatile("" : "+v"(ua), "+v"(ub));
    };
    auto put_fix = [&](const f32x4& v, int slot, int m) {
      const u32 hi = pk2(v[0], v[1]), lo = pk2(v[0] - bflo(hi), v[1] - bfhi(hi));
      const u32 u1 = hi & 0xffffu, t1 = hi >> 16, u2 = lo & 0xffffu, t2 = lo >> 16;
      const u32x4 o = u32x4{u1 | (u1 << 16), u2 | (u2 << 16), t1 | (t1 << 16), t2 | (t2 << 16)};
      if (lane < 16) { LAS char* d = L + L_FIX + slot * 1024 + (16 * m + lane) * 16; *(LAS u32x4*)d = o; *(LAS u32x4*)(d + 512) = o; }
    };
    LAS char* u1a = L + ((4 * g + q4) * 128 + (lane & 3) * 8 + ((cw ^ (q4 >> 1)) * 32));
    LAS char* u1b = L + (4096 + (4 * g + q4) * 128 + (lane & 3) * 8 + ((cw ^ (q4 >> 1) ^ 2) * 32));
    ua = f32x4{0, 0, 0, 0}; ub = f32x4{0, 0, 0, 0};
    ut_blocks(u1a, u1b, L_P1B, 2048, 0, 8);
    waitv<0>(); STAMP(12); bar();
    u32 p2v[3];
#pragma unroll
    for (int i = 0; i < 3; ++i) { const int n = 64 * i + lane, row = n / 6, c = n - row * 6; p2v[i] = (u32)(row * 3584 + c * 16 + ((c >> 1) >= p0 ? 128 : 0)); }
    const rsrc_t rgu = (cw & 1) ? rwu : rwg;
    auto dma_p2 = [&](int k0, int k1) {
#pragma unroll
      for (int k = k0; k < k1; ++k)
        if ((k & 1) == (cw >> 1)) {
#pragma unroll
          for (int i = 0; i < 3; ++i) dma16(rgu, p2v[i], (u32)(((k + rotg) & 15) * 32 * 3584 + jt * 224), Lb + (u32)(k * L_P2B + (cw & 1) * L_P2M + i * 1024));
        }
    };
    dma_p2(0, 5);
    ut_blocks(u1a, u1b, L_P1B, 2048, 8, 16);
    put_fix(ua, cw, 0); put_fix(ub, cw, 1);
    STAMP(13); bar();
    dma_p2(5, 10);
    waitv<6>(); STAMP(14); bar();
    dma_p2(10, 15);
    LAS char* u2a = L + ((cw & 1) * L_P2M + (4 * g + q4) * 96 + (lane & 3) * 8 + (cw >> 1) * 32);
    LAS char* u2b = L + ((cw & 1) * L_P2M + (4 * g + q4) * 96 + (lane & 3) * 8 + 64);
    ua = f32x4{0, 0, 0, 0}; ub = f32x4{0, 0, 0, 0};
    ut_blocks(u2a, u2b, L_P2B, 1536, 0, 5);
    waitv<6>(); STAMP(15); bar();
    dma_p2(15, 16);
    auto dma_wd = [&](int u) {
#pragma unroll
      for (int q = 0; q < 8; ++q) {
        const int n = 8 * (u - 4) + q;
        const int jj = ((u < 4 ? u : n / 6) + rotd) & 3, r6 = n % 6;
        const int row = u < 4 ? 112 * jj + 16 * jj + 8 * q : 112 * jj + 8 * r6 + ((r6 >> 1) >= jj ? 64 : 0);
        dma16(rwd, wov, (u32)((448 * cw + row) * 1024 + cb * 128), Lb + (u32)(wd_unit_off(u) + cw * 8192 + q * 1024));
      }
    };
    dma_wd(0);
    ut_blocks(u2a, u2b, L_P2B, 1536, 5, 10);
    waitv<8>(); STAMP(16); bar();
    ut_blocks(u2a, u2b, L_P2B, 1536, 10, 16);
    put_fix(ua, cw >> 1, cw & 1); if (cw < 2) put_fix(ub, 2, cw & 1);
    bar();
    dma_wd(1);
    bar();
    dma_wd(2);
    waitv<16>();
    bar();
    dma_wd(3);
    waitv<16>(); STAMP(17); bar();
    dma_wd(4);
    waitv<16>(); bar();
    dma_wd(5);
    waitv<16>(); bar();
    dma_wd(6);
    waitv<16>(); STAMP(18); bar();
    waitv<8>(); bar();
    waitv<0>(); bar();
    bar();
  } else {
    f32x4 Kt[5], Qt[4], Vn[5];
#pragma unroll
    for (int r = 0; r < 5; ++r) { Kt[r] = f32x4{0, 0, 0, 0}; Vn[r] = f32x4{0, 0, 0, 0}; if (r < 4) Qt[r] = f32x4{0, 0, 0, 0}; }
    {
      u32x4 a[2][5], wl[2][3];
      auto ld = [&](int n, int bi) {
        LAS char* ab = L + L_AIMG + ((n + rot) & 15) * 1024 + g * 256 + ((i16 ^ (((n & 1) << 2) | g)) * 16);
#pragma unroll
        for (int rb = 0; rb < 5; ++rb) a[bi][rb] = *(const LAS u32x4*)(ab + rb * 16384);
        if (n < 6 || n >= 12) {
#pragma unroll
          for (int pp = 0; pp < 3; ++pp) wl[bi][pp] = trd2(w1rd + w1slot(n) + pp * 4096, 512);
        }
      };
      ld(0, 0); SB();
#pragma unroll
      for (int n = 0; n < 16; ++n) {
        if (n == 3) bar();
        if (n == 11) bar();
        if (n < 15) ld(n + 1, (n + 1) & 1);
        const bool rg = n >= 6 && n < 12;
        const u32x4 wq = rg ? wr[rg ? n - 6 : 0][0] : wl[n & 1][0], wk = rg ? wr[rg ? n - 6 : 0][1] : wl[n & 1][1], wv = rg ? wr[rg ? n - 6 : 0][2] : wl[n & 1][2];
#pragma unroll
        for (int rb = 0; rb < 5; ++rb) {
          Kt[rb] = mfma16(wk, a[n & 1][rb], Kt[rb]);
          Vn[rb] = mfma16(a[n & 1][rb], wv, Vn[rb]);
          if (rb > 0) Qt[rb - 1] = mfma16(wq, a[n & 1][rb], Qt[rb - 1]);
        }
#pragma unroll
        for (int k = 0; k < 11; ++k) { SGB_MFMA(1); SGB_DSR(1); }
        SGB_MFMA(3);
        SB();
      }
    }
    bar();
    STAMP(8);
    const int tb = cw & 1, kh = cw >> 1;
    {
      u32x2 kp[5], vp[5];
#pragma unroll
      for (int r = 0; r < 5; ++r) { kp[r] = u32x2{pk2(Kt[r][0], Kt[r][1]), pk2(Kt[r][2], Kt[r][3])}; vp[r] = u32x2{pk2(Vn[r][0], Vn[r][1]), pk2(Vn[r][2], Vn[r][3])}; }
      f32x4 m0, m1, m0f;
#pragma unroll
      for (int r = 0; r < 4; ++r) {
        const int d1 = i16 - 4 * g - r, d0 = d1 + 16;
        m0[r] = (d0 <= 14 && !(d0 & 1)) ? 0.f : -1e30f;
        m1[r] = (d1 >= 0 && d1 <= 14 && !(d1 & 1)) ? 0.f : -1e30f;
        m0f[r] = tt == 0 ? -1e30f : m0[r];
      }
      u32x2 op[4];
      const u32x4 ones = u32x4{0x3f803f80u, 0x3f803f80u, 0x3f803f80u, 0x3f803f80u};
#pragma unroll
      for (int qb = 0; qb < 4; ++qb) {
        const u32x2 qp = u32x2{pk2(Qt[qb][0], Qt[qb][1]), pk2(Qt[qb][2], Qt[qb][3])};
        const f32x4 s0 = mfma16k16(kp[qb], qp, qb == 0 ? m0f : m0), s1 = mfma16k16(kp[qb + 1], qp, m1);
        float mx = fmaxf(fmaxf(fmaxf(s0[0], s0[1]), fmaxf(s0[2], s0[3])), fmaxf(fmaxf(s1[0], s1[1]), fmaxf(s1[2], s1[3])));
        mx = swap16_max(mx); mx = swap32_max(mx);
        const float cs = 0.25f * 1.44269504088896f, nm = -mx * cs;
        float e0[4], e1[4];
#pragma unroll
        for (int r = 0; r < 4; ++r) { e0[r] = __builtin_amdgcn_exp2f(fmaf(s0[r], cs, nm)); e1[r] = __builtin_amdgcn_exp2f(fmaf(s1[r], cs, nm)); }
        const u32x4 pf = u32x4{pk2(e0[0], e0[1]), pk2(e0[2], e0[3]), pk2(e1[0], e1[1]), pk2(e1[2], e1[3])};
        const f32x4 z = f32x4{0, 0, 0, 0};
        const f32x4 ot = mfma16(u32x4{vp[qb][0], vp[qb][1], vp[qb + 1][0], vp[qb + 1][1]}, pf, z);
        const f32x4 sm = mfma16(ones, pf, z);
        const float ri = __builtin_amdgcn_rcpf(sm[0]);
        op[qb] = u32x2{pk2(ot[0] * ri, ot[1] * ri), pk2(ot[2] * ri, ot[3] * ri)};
      }
#pragma unroll
      for (int t2 = 0; t2 < 2; ++t2) {
        auto r0 = __builtin_amdgcn_permlane16_swap(op[2 * t2][0], op[2 * t2 + 1][0], false, false);
        auto r1 = __builtin_amdgcn_permlane16_swap(op[2 * t2][1], op[2 * t2 + 1][1], false, false);
        const u32 a0 = r0[0], b0 = r0[1], a1 = r1[0], b1 = r1[1];
        st128_sc1(u32x4{a0, a1, b0, b1}, rws, (u32)(lane * 16), WS_O + (u32)(((2 * tt + t2) * 32 + 4 * hg + cw) * 1024));
      }
    }
    STAMP(9);
    waitv<0>(); bar();
    if (tid == 0) st64_sc1(u32x2{tlo, thi}, rws, 0u, WS_FO + (u32)(tt * 256 + hg * 8));
    STAMP(10);
    wait_flags<8>(rws, WS_FO + (u32)(tt * 256), lane, tlo, thi);
    STAMP(11);
    u32 x1p[8];
    {
      u32x4 of[16];
      const u32 ob = WS_O + (u32)(((2 * tt + tb) * 32 + 16 * kh) * 1024);
#pragma unroll
      for (int s = 0; s < 16; ++s) of[s] = ld128_sc1(rws, (u32)(lane * 16), ob + (u32)(s * 1024));
      SB();
      LAS char* q0 = L + (L_WO + (256 * kh + 8 * h + q4) * 128 + mrow * 32 + (lane & 3) * 8);
      LAS char* qa = q0 + (q4 >> 1) * 64; LAS char* qb = q0 + ((q4 >> 1) ^ 1) * 64;
      f32x16 a0, a1;
#pragma unroll
      for (int r = 0; r < 16; ++r) { a0[r] = 0.f; a1[r] = 0.f; }
      u32x4 wl[2][2];
      wl[0][0] = trd2(qa, 512); wl[0][1] = trd2(qb, 512); SB();
#pragma unroll
      for (int s = 0; s < 16; ++s) {
        if (s < 15) { wl[(s + 1) & 1][0] = trd2(qa + (s + 1) * 2048, 512); wl[(s + 1) & 1][1] = trd2(qb + (s + 1) * 2048, 512); }
        a0 = mfma32(wl[s & 1][0], of[s], a0); a1 = mfma32(wl[s & 1][1], of[s], a1);
        SGB_MFMA(1); SGB_DSR(2); SGB_MFMA(1); SGB_DSR(2);
        SB();
      }
      u32x2 xres[4];
#pragma unroll
      for (int q = 0; q < 4; ++q) xres[q] = *(const LAS u32x2*)(L + L_XRES + (32 * tb + tok) * 128 + (32 * kh + 8 * q + 4 * h) * 2);
      const f32x16 snd = kh ? a0 : a1; f32x16 acc = kh ? a1 : a0;
#pragma unroll
      for (int q = 0; q < 4; ++q) *(LAS f32x4*)(L + L_RED2 + cw * 4096 + q * 1024 + lane * 16) = f32x4{snd[4 * q], snd[4 * q + 1], snd[4 * q + 2], snd[4 * q + 3]};
      STAMP(19);
      bar();
      STAMP(20);
      const float mu1 = ((const LAS float*)(L + L_MU1))[32 * tb + tok];
      float s1 = 0.f, s2 = 0.f; u32 yp[8];
#pragma unroll
      for (int q = 0; q < 4; ++q) {
        const f32x4 o4 = *(const LAS f32x4*)(L + L_RED2 + (cw ^ 2) * 4096 + q * 1024 + lane * 16);
        const u32x2 xq = xres[q];
        const f32x4 ga4 = *(const LAS f32x4*)(L + L_GA + (32 * kh + 8 * q + 4 * h) * 4), g24 = *(const LAS f32x4*)(L + L_G2C + (32 * kh + 8 * q + 4 * h) * 4);
        const float v0 = fmaf(ga4[0], acc[4 * q] + o4[0], bflo(xq[0])), v1 = fmaf(ga4[1], acc[4 * q + 1] + o4[1], bfhi(xq[0]));
        const float v2 = fmaf(ga4[2], acc[4 * q + 2] + o4[2], bflo(xq[1])), v3 = fmaf(ga4[3], acc[4 * q + 3] + o4[3], bfhi(xq[1]));
        x1p[2 * q] = pk2(v0, v1); x1p[2 * q + 1] = pk2(v2, v3);
        const float d0 = bflo(x1p[2 * q]) - mu1, d1 = bfhi(x1p[2 * q]) - mu1, d2 = bflo(x1p[2 * q + 1]) - mu1, d3 = bfhi(x1p[2 * q + 1]) - mu1;
        s1 += (d0 + d1) + (d2 + d3); s2 = fmaf(d0, d0, fmaf(d1, d1, fmaf(d2, d2, fmaf(d3, d3, s2))));
        yp[2 * q] = pk2(d0 * g24[0], d1 * g24[1]); yp[2 * q + 1] = pk2(d2 * g24[2], d3 * g24[3]);
      }
      const u32 yb = WS_Y + (u32)(((2 * tt + tb) * 32 + 4 * cb + 2 * kh) * 1024);
      st128_sc1(u32x4{yp[0], yp[1], yp[2], yp[3]}, rws, (u32)(lane * 16), yb);
      st128_sc1(u32x4{yp[4], yp[5], yp[6], yp[7]}, rws, (u32)(lane * 16), yb + 1024);
      s1 = swap32_add(s1, s1); s2 = swap32_add(s2, s2);
      if (lane < 32) st64_sc1(u32x2{u_(s1), u_(s2)}, rws, (u32)(tok * 8), WS_ST + (u32)(((2 * tt + tb) * 16 + 2 * cb + kh) * 256));
    }
    STAMP(12);
    waitv<0>(); bar();
    if (tid == 0) st64_sc1(u32x2{tlo, thi}, rws, 0u, WS_FY + (u32)(tt * 256 + cb * 8));
    STAMP(13);
    wait_flags<8>(rws, WS_FY + (u32)((2 * T + (cw >> 1)) * 256), lane, tlo, thi);
    STAMP(14);
    const int tb32 = 4 * T + cw;
    u32x2 stv[8];
#pragma unroll
    for (int q = 0; q < 8; ++q) stv[q] = ld64_sc1(rws, (u32)(tok * 8 + h * 2048), WS_ST + (u32)((tb32 * 16 + q) * 256));
    u32x4 yf[32];
#pragma unroll
    for (int s = 0; s < 32; ++s) yf[s] = ld128_sc1(rws, (u32)(lane * 16), WS_Y + (u32)((tb32 * 32 + ((s + 2 * rotg) & 31)) * 1024));
    SB();
    u32 mz, sz; float r2;
    {
      float s1 = 0.f, s2 = 0.f;
#pragma unroll
      for (int q = 0; q < 8; ++q) { s1 += f_(stv[q][0]); s2 += f_(stv[q][1]); }
      s1 = swap32_add(s1, s1); s2 = swap32_add(s2, s2);
      const float md = s1 * (1.f / 512.f);
      const float ve = fmaxf(s2 * (1.f / 512.f) - md * md, 0.f) + 1e-5f;
      const float sg = __builtin_amdgcn_sqrtf(ve); r2 = __builtin_amdgcn_rcpf(ve);
      const u32 hi = pk2(-md, sg), lo = pk2(-md - bflo(hi), sg - bfhi(hi));
      const u32 mm = (hi & 0xffffu) | (lo << 16), ss = (hi >> 16) | (lo & 0xffff0000u);
      mz = h ? 0u : mm; sz = h ? 0u : ss;
      asm volatile("" : "+v"(mz), "+v"(sz), "+v"(r2));
    }
    SB();
    auto act_store = [&](const f32x16& ac, int pp) {
      u32x4 o;
#pragma unroll
      for (int e = 0; e < 4; ++e) o[e] = pk2(fmaxf(ac[2 * e], 0.f) * r2 * ac[8 + 2 * e], fmaxf(ac[2 * e + 1], 0.f) * r2 * ac[9 + 2 * e]);
      st128_sc1(o, rws, (u32)(lane * 16), WS_ACT + (u32)((tb32 * 112 + 7 * jt + pp) * 1024));
    };
    {
      f32x16 acc[4];
#pragma unroll
      for (int q = 0; q < 4; ++q)
#pragma unroll
        for (int r = 0; r < 16; ++r) acc[q][r] = 0.f;
      LAS char* pa[4];
#pragma unroll
      for (int pp = 0; pp < 4; ++pp) pa[pp] = L + (p1base + (((u32)pp ^ p1x) * 32));
      u32x4 wf[2][4];
      auto ldw = [&](int st, int bi) {
#pragma unroll
        for (int pp = 0; pp < 4; ++pp) wf[bi][pp] = trd2(pa[pp] + (st >> 1) * L_P1B + (st & 1) * 2048, 1024);
      };
      ldw(0, 0); SB();
#pragma unroll
      for (int st = 0; st < 32; ++st) {
        if (st == 7 || st == 15 || st == 23) bar();
        if (st + 1 < 32) ldw(st + 1, (st + 1) & 1);
#pragma unroll
        for (int pp = 0; pp < 4; ++pp) acc[pp] = mfma32(wf[st & 1][pp], yf[st], acc[pp]);
#pragma unroll
        for (int pp = 0; pp < 4; ++pp) { SGB_MFMA(1); SGB_DSR(2); }
        SB();
      }
#pragma unroll
      for (int pp = 0; pp < 4; ++pp) acc[pp] = mfma32(*(const LAS u32x4*)(L + L_FIX + pp * 1024 + lane * 16), u32x4{mz, mz, sz, sz}, acc[pp]);
      bar();
      STAMP(15);
#pragma unroll
      for (int pp = 0; pp < 4; ++pp) act_store(acc[pp], p0 + pp);
    }
    u32x4 af[16][2];
    {
      f32x16 acc[3];
#pragma unroll
      for (int q = 0; q < 3; ++q)
#pragma unroll
        for (int r = 0; r < 16; ++r) acc[q][r] = 0.f;
      LAS char* pb = L + p2base;
      u32x4 wf[2][3];
      auto ldw = [&](int st, int bi) {
#pragma unroll
        for (int pp = 0; pp < 3; ++pp) wf[bi][pp] = trd2(pb + (st >> 1) * L_P2B + (st & 1) * 1536 + pp * 32, 768);
      };
      ldw(0, 0); SB();
#pragma unroll
      for (int st = 0; st < 32; ++st) {
        if (st == 7) { waitv<0>(); bar(); if (tid == 0) st64_sc1(u32x2{tlo, thi}, rws, 0u, WS_FA + (u32)(T * 256 + jt * 8)); }
        if (st == 15) bar();
        if (st == 23) bar();
        if (st == 16) { STAMP(23); wait_flags<4>(rws, WS_FA + (u32)(T * 256 + cw * 32), lane, tlo, thi); }
        if (st + 1 < 32) ldw(st + 1, (st + 1) & 1);
        if (st >= 16) {
          const int s = st - 16;
#pragma unroll
          for (int t2 = 0; t2 < 2; ++t2) af[s][t2] = ld128_sc1(rws, (u32)(lane * 16), WS_ACT + (u32)(((2 * tt + t2) * 112 + 28 * cw + 8 * (((s >> 2) + rotd) & 3) + (s & 3)) * 1024));
        }
#pragma unroll
        for (int pp = 0; pp < 3; ++pp) acc[pp] = mfma32(wf[st & 1][pp], yf[st], acc[pp]);
#pragma unroll
        for (int pp = 0; pp < 3; ++pp) { SGB_MFMA(1); SGB_DSR(2); }
        SB();
      }
#pragma unroll
      for (int pp = 0; pp < 3; ++pp) acc[pp] = mfma32(*(const LAS u32x4*)(L + L_FIX + pp * 1024 + lane * 16), u32x4{mz, mz, sz, sz}, acc[pp]);
      bar();
      STAMP(16);
#pragma unroll
      for (int pp = 0; pp < 3; ++pp) act_store(acc[pp], pp >= p0 ? pp + 4 : pp);
    }
    SB();
    {
      f32x16 acc[4];
#pragma unroll
      for (int q = 0; q < 4; ++q)
#pragma unroll
        for (int r = 0; r < 16; ++r) acc[q][r] = 0.f;
      LAS char* d0 = L + (cw * 8192 + (4 * h + q4) * 128 + mrow * 32 + (lane & 3) * 8);
      LAS char* da = d0 + (q4 >> 1) * 64; LAS char* db = d0 + ((q4 >> 1) ^ 1) * 64;
      u32x4 wl[2][2], a2[12][2];
      auto ldw = [&](int s, int bi) { const u32 o = wd_unit_off(s >> 2) + (s & 3) * 2048; wl[bi][0] = trd2(da + o, 1024); wl[bi][1] = trd2(db + o, 1024); };
      ldw(0, 0); SB();
#pragma unroll
      for (int s = 0; s < 28; ++s) {
        if (s == 3) { waitv<0>(); bar(); if (tid == 0) st64_sc1(u32x2{tlo, thi}, rws, 0u, WS_FA2 + (u32)(T * 256 + jt * 8)); }
        if (s == 7 || s == 11 || s == 15 || s == 19 || s == 23) bar();
        if (s == 12) {
          STAMP(17);
          wait_flags<4>(rws, WS_FA2 + (u32)(T * 256 + cw * 32), lane, tlo, thi);
          STAMP(21);
#pragma unroll
          for (int s2 = 0; s2 < 12; ++s2)
#pragma unroll
            for (int t2 = 0; t2 < 2; ++t2) {
              const int jj = (s2 / 3 + rotd) & 3, pp = s2 % 3;
              a2[s2][t2] = ld128_sc1(rws, (u32)(lane * 16), WS_ACT + (u32)(((2 * tt + t2) * 112 + 28 * cw + 7 * jj + (pp >= jj ? pp + 4 : pp)) * 1024));
            }
          SB();
        }
        if (s + 1 < 28) ldw(s + 1, (s + 1) & 1);
        const u32x4 b0 = s < 16 ? af[s < 16 ? s : 0][0] : a2[s < 16 ? 0 : s - 16][0], b1 = s < 16 ? af[s < 16 ? s : 0][1] : a2[s < 16 ? 0 : s - 16][1];
        acc[0] = mfma32(wl[s & 1][0], b0, acc[0]); acc[2] = mfma32(wl[s & 1][1], b0, acc[2]);
        acc[1] = mfma32(wl[s & 1][0], b1, acc[1]); acc[3] = mfma32(wl[s & 1][1], b1, acc[3]);
        SGB_MFMA(1); SGB_DSR(2); SGB_MFMA(1); SGB_DSR(2); SGB_MFMA(2);
        SB();
      }
      STAMP(22);
#pragma unroll
      for (int o = 0; o < 4; ++o) {
        if (o != cw) {
          const int sl = ((cw - o) & 3) - 1;
#pragma unroll
          for (int q = 0; q < 4; ++q) *(LAS f32x4*)(L + L_RED4 + (o * 3 + sl) * 4096 + q * 1024 + lane * 16) = f32x4{acc[o][4 * q], acc[o][4 * q + 1], acc[o][4 * q + 2], acc[o][4 * q + 3]};
        }
      }
      f32x16 mine = cw == 0 ? acc[0] : cw == 1 ? acc[1] : cw == 2 ? acc[2] : acc[3];
      bar();
      u32 ob[8];
#pragma unroll
      for (int q = 0; q < 4; ++q) {
        const LAS char* rp = L + L_RED4 + cw * 3 * 4096 + q * 1024 + lane * 16;
        const f32x4 r0 = *(const LAS f32x4*)rp, r1 = *(const LAS f32x4*)(rp + 4096), r2_ = *(const LAS f32x4*)(rp + 8192);
        const f32x4 gb4 = *(const LAS f32x4*)(L + L_GBV + (32 * kh + 8 * q + 4 * h) * 4);
        float v[4];
#pragma unroll
        for (int e = 0; e < 4; ++e) v[e] = (mine[4 * q + e] + r0[e]) + (r1[e] + r2_[e]);
        ob[2 * q] = pk2(fmaf(gb4[0], v[0], bflo(x1p[2 * q])), fmaf(gb4[1], v[1], bfhi(x1p[2 * q])));
        ob[2 * q + 1] = pk2(fmaf(gb4[2], v[2], bflo(x1p[2 * q + 1])), fmaf(gb4[3], v[3], bfhi(x1p[2 * q + 1])));
      }
#pragma unroll
      for (int m = 0; m < 2; ++m) {
        auto r0 = __builtin_amdgcn_permlane32_swap(ob[4 * m], ob[4 * m + 2], false, false);
        auto r1 = __builtin_amdgcn_permlane32_swap(ob[4 * m + 1], ob[4 * m + 3], false, false);
        const u32 a0 = r0[0], b0 = r0[1], a1 = r1[0], b1 = r1[1];
        __builtin_amdgcn_raw_buffer_store_b128(u32x4{a0, a1, b0, b1}, rout, (u32)((64 * tt + 32 * tb + tok) * 1024 + (64 * cb + 32 * kh + 16 * m + 8 * h) * 2), 0, 0);
      }
    }
    STAMP(18);
    if (b == 0 && cw == 0) {
      const u64 t0 = __builtin_amdgcn_s_memrealtime();
      for (;;) {
        bool ok = true;
#pragma unroll
        for (int k = 0; k < 4; ++k) { const int f = 64 * k + lane; const u32x2 v = ld64_sc1(rws, (u32)((f >> 3) * 256 + (f & 7) * 8), WS_FO); ok = ok && v[0] == tlo && v[1] == thi; }
        if (__all(ok)) break;
        if (spin_out(t0)) break;
      }
      if (lane == 0) st64_sc1(u32x2{tlo, thi}, rws, 0u, WS_E);
    }
  }
}

extern "C" void kernel_launch(void* const* d_in, const int* in_sizes, int n_in, void* d_out, int out_size, void* d_ws, size_t ws_size, hipStream_t stream) {
  Params p;
  p.x = d_in[0]; p.wqkv = d_in[1]; p.wo = d_in[2]; p.wg = d_in[4]; p.wu = d_in[5]; p.wd = d_in[6]; p.c = d_in[7]; p.wmod = d_in[8];
  p.out = d_out; p.ws = d_ws;
  (void)hipFuncSetAttribute((const void*)fused_block, hipFuncAttributeMaxDynamicSharedMemorySize, LDS_BYTES);
  fused_block<<<256, 512, LDS_BYTES, stream>>>(p);
}
```

```cpp
#include <hip/hip_runtime.h>
#include <stdint.h>

typedef unsigned short u16;
typedef unsigned u32;
typedef unsigned long long u64;
typedef u32 u32x2 __attribute__((ext_vector_type(2)));
typedef u32 u32x4 __attribute__((ext_vector_type(4)));
typedef short s16x4 __attribute__((ext_vector_type(4)));
typedef short s16x8 __attribute__((ext_vector_type(8)));
typedef __bf16 bf16x2 __attribute__((ext_vector_type(2)));
typedef __bf16 bf16x8 __attribute__((ext_vector_type(8)));
typedef float f32x2 __attribute__((ext_vector_type(2)));
typedef float f32x4 __attribute__((ext_vector_type(4)));
typedef float f32x16 __attribute__((ext_vector_type(16)));
typedef __amdgpu_buffer_rsrc_t rsrc_t;
#define LAS __attribute__((address_space(3)))
#define DI __device__ __forceinline__

#define EXPW 0
#define TOUCH 6
constexpr unsigned CODE_BYTES = 50000;
#define DIAG 0
#define XMAP 2

constexpr int S_ = 2048, D_ = 512, F_ = 1792;
constexpr u32 WS_E = 0, WS_MODG = 4096, WS_FO = 32768, WS_FY = 40960, WS_FA = 49152, WS_FA2 = 53248, WS_DBG = 65536;
constexpr u32 WS_O = 1u << 20, WS_Y = 3u << 20, WS_ST = 5u << 20, WS_ACT = 6u << 20, WS_BYTES = 13u << 20;
constexpr u32 L_AIMG = 0, L_RW = 81920, L_W1S = 12288;
constexpr u32 L_LNP = 73728, L_PRED = L_LNP + 6400, L_RSNB = L_PRED + 1088;
constexpr u32 L_XRES = 65536, L_WO = 73728, L_RED2 = 139264;
constexpr u32 L_P1B = 8192, L_P2B = 6400, L_P2M = 3200;
constexpr u32 L_RED4 = 65536;
constexpr u32 L_MISC = 155648, L_G1 = L_MISC, L_S1 = L_MISC + 2048, L_FIX = L_MISC;
constexpr u32 L_MU1 = L_MISC + 4096, L_GA = L_MU1 + 256, L_G2C = L_GA + 256, L_GBV = L_G2C + 256, L_GS = L_MISC + 5120;
constexpr u32 L_CP = L_MISC + 7168, L_HP = L_CP + 16, L_RX = L_CP + 32, L_CNT = L_CP + 48;
constexpr u32 LDS_BYTES = 163840;
__host__ __device__ constexpr u32 wd_unit_off(int u) { return u == 0 || u == 4 ? 102400u : (u == 1 || u == 5 ? 0u : (u == 2 || u == 6 ? 32768u : 65536u)); }

template <int N> struct ic { static constexpr int value = N; };
struct Params { const void *x, *wqkv, *wo, *wg, *wu, *wd, *c, *wmod; void *out; void *ws; };

DI rsrc_t mk(const void* p, u32 bytes) { return __builtin_amdgcn_make_buffer_rsrc((void*)p, 0, bytes, 0x00020000); }
DI float bflo(u32 v) { return __builtin_bit_cast(float, v << 16); }
DI float bfhi(u32 v) { return __builtin_bit_cast(float, v & 0xffff0000u); }
DI u32 pk2(float a, float b) { bf16x2 r; r[0] = (__bf16)a; r[1] = (__bf16)b; return __builtin_bit_cast(u32, r); }
DI float rbf(float a) { return bflo(pk2(a, a)); }
DI f32x4 mfma16(u32x4 a, u32x4 b, f32x4 c) { return __builtin_amdgcn_mfma_f32_16x16x32_bf16(__builtin_bit_cast(bf16x8, a), __builtin_bit_cast(bf16x8, b), c, 0, 0, 0); }
DI f32x4 mfma16k16(u32x2 a, u32x2 b, f32x4 c) { return __builtin_amdgcn_mfma_f32_16x16x16bf16_1k(__builtin_bit_cast(s16x4, a), __builtin_bit_cast(s16x4, b), c, 0, 0, 0); }
DI f32x16 mfma32(u32x4 a, u32x4 b, f32x16 c) { return __builtin_amdgcn_mfma_f32_32x32x16_bf16(__builtin_bit_cast(bf16x8, a), __builtin_bit_cast(bf16x8, b), c, 0, 0, 0); }
DI u32x2 trd(LAS char* p) { return __builtin_bit_cast(u32x2, __builtin_amdgcn_ds_read_tr16_b64_v4i16((LAS s16x4*)p)); }
DI u32x4 trd2(LAS char* p, int d) { u32x2 a = trd(p), b = trd(p + d); return u32x4{a[0], a[1], b[0], b[1]}; }
DI void dma16(rsrc_t rs, u32 voff, u32 soff, u32 lds) {
  asm volatile("s_mov_b32 m0, %0\n\ts_nop 0\n\tbuffer_load_dwordx4 %1, %2, %3 offen lds" :: "s"(lds), "v"(voff), "s"(rs), "s"(soff) : "m0", "memory");
}
template <int OFF> DI void ald128(u32x4& d, rsrc_t rs, u32 voff, u32 soff) {
  asm volatile("buffer_load_dwordx4 %0, %1, %2, %3 offen offset:%c4" : "=v"(d) : "v"(voff), "s"(rs), "s"(soff), "i"(OFF) : "memory");
}
template <int OFF> DI void ald128_sc1(u32x4& d, rsrc_t rs, u32 voff, u32 soff) {
  asm volatile("buffer_load_dwordx4 %0, %1, %2, %3 offen offset:%c4 sc1" : "=v"(d) : "v"(voff), "s"(rs), "s"(soff), "i"(OFF) : "memory");
}
DI void ald64_sc1(u32x2& d, rsrc_t rs, u32 voff, u32 soff) {
  asm volatile("buffer_load_dwordx2 %0, %1, %2, %3 offen sc1" : "=v"(d) : "v"(voff), "s"(rs), "s"(soff) : "memory");
}
DI void ald64(u32x2& d, rsrc_t rs, u32 voff, u32 soff) {
  asm volatile("buffer_load_dwordx2 %0, %1, %2, %3 offen" : "=v"(d) : "v"(voff), "s"(rs), "s"(soff) : "memory");
}
DI void warm(u32 lds, rsrc_t rs, u32 voff, u32 soff) {
  asm volatile("s_mov_b32 m0, %0\n\ts_nop 0\n\tbuffer_load_dword %1, %2, %3 offen lds" :: "s"(lds), "v"(voff), "s"(rs), "s"(soff) : "m0", "memory");
}
template <int N> DI void waitv() { asm volatile("s_waitcnt vmcnt(%c0)" :: "i"(N) : "memory"); }
template <int N> DI void waitv4(u32x4& a, u32x4& b, u32x4& c, u32x4& d) { asm volatile("s_waitcnt vmcnt(%c4)" : "+v"(a), "+v"(b), "+v"(c), "+v"(d) : "i"(N) : "memory"); }
template <int N> DI void waitv1(u32x4& a) { asm volatile("s_waitcnt vmcnt(%c1)" : "+v"(a) : "i"(N) : "memory"); }
DI void bar() { asm volatile("s_waitcnt lgkmcnt(0)\n\ts_barrier" ::: "memory"); }
DI u32x4 ld128_sc1(rsrc_t rs, u32 voff, u32 soff) { return __builtin_amdgcn_raw_buffer_load_b128(rs, voff, soff, 16); }
DI u32x2 ld64_sc1(rsrc_t rs, u32 voff, u32 soff) { return __builtin_amdgcn_raw_buffer_load_b64(rs, voff, soff, 16); }
DI void st128_sc1(u32x4 v, rsrc_t rs, u32 voff, u32 soff) { __builtin_amdgcn_raw_buffer_store_b128(v, rs, voff, soff, 16); }
DI void st64_sc1(u32x2 v, rsrc_t rs, u32 voff, u32 soff) { __builtin_amdgcn_raw_buffer_store_b64(v, rs, voff, soff, 16); }
DI float f_(u32 v) { return __builtin_bit_cast(float, v); }
DI u32 u_(float v) { return __builtin_bit_cast(u32, v); }
DI float swap16_add(float a, float b) { auto r = __builtin_amdgcn_permlane16_swap(u_(a), u_(b), false, false); u32 r0 = r[0], r1 = r[1]; return f_(r0) + f_(r1); }
DI float swap32_add(float a, float b) { auto r = __builtin_amdgcn_permlane32_swap(u_(a), u_(b), false, false); u32 r0 = r[0], r1 = r[1]; return f_(r0) + f_(r1); }
DI float swap16_max(float a) { auto r = __builtin_amdgcn_permlane16_swap(u_(a), u_(a), false, false); u32 r0 = r[0], r1 = r[1]; return fmaxf(f_(r0), f_(r1)); }
DI float swap32_max(float a) { auto r = __builtin_amdgcn_permlane32_swap(u_(a), u_(a), false, false); u32 r0 = r[0], r1 = r[1]; return fmaxf(f_(r0), f_(r1)); }
template <int CTRL> DI float dpp_add(float a) { return a + f_(__builtin_amdgcn_update_dpp(0, u_(a), CTRL, 0xf, 0xf, true)); }
DI float dot2(u32 a, u32 b, float c) { return __builtin_amdgcn_fdot2_f32_bf16(__builtin_bit_cast(bf16x2, a), __builtin_bit_cast(bf16x2, b), c, false); }

constexpr u64 SPIN_TICKS = 20000;
DI bool spin_out(u64 t0) { __builtin_amdgcn_s_sleep(1); return (__builtin_amdgcn_s_memrealtime() - t0) > SPIN_TICKS; }

#define SB() __builtin_amdgcn_sched_barrier(0)
#define SGB_MFMA(n) __builtin_amdgcn_sched_group_barrier(0x8, n, 0)
#define SGB_DSR(n) __builtin_amdgcn_sched_group_barrier(0x100, n, 0)
#define SGB_DSW(n) __builtin_amdgcn_sched_group_barrier(0x200, n, 0)
#define STAMP(i) do {} while (0)
#define WACC(i, code) do { code; } while (0)

DI void lsig(LAS char* byte, u32 v, int lane) { asm volatile("" ::: "memory"); if (lane == 0) *(volatile LAS unsigned char*)byte = (unsigned char)v; asm volatile("" ::: "memory"); }
DI u32 lpeek(LAS char* word) { return *(volatile LAS u32*)word; }
DI bool lge(u32 v, u32 want) { return (((__builtin_amdgcn_readfirstlane(v) | 0x80808080u) - want * 0x01010101u) & 0x80808080u) == 0x80808080u; }
template <bool SLEEP> DI void lwait_from(u32 seen, LAS char* word, u32 want) {
  if (!lge(seen, want)) {
    for (u32 sp = 0; sp < (1u << 18); ++sp) {
      if (SLEEP) __builtin_amdgcn_s_sleep(1);
      if (lge(lpeek(word), want)) break;
    }
  }
  asm volatile("" ::: "memory");
}
template <bool SLEEP> DI void lwait(LAS char* word, u32 want) { lwait_from<SLEEP>(lpeek(word), word, want); }
template <int YOUNGER> DI void publish(LAS char* cnt, rsrc_t rws, u32 flag_off, u32 tlo, u32 thi, int lane) {
  waitv<YOUNGER>();
  if (lane == 0) {
    const u32 old = __hip_atomic_fetch_add((LAS u32*)cnt, 1u, __ATOMIC_RELAXED, __HIP_MEMORY_SCOPE_WORKGROUP);
    if (old == 3u) st64_sc1(u32x2{tlo, thi}, rws, 0u, flag_off);
  }
  asm volatile("" ::: "memory");
}
template <int NF> DI void wait_flags(rsrc_t rws, u32 off, int lane, u32 tlo, u32 thi) {
  const u32 vo = (u32)(lane & (NF - 1)) * 8u;
  const u64 t0 = __builtin_amdgcn_s_memrealtime();
  for (;;) {
    u32x2 f = ld64_sc1(rws, vo, off);
    if (__all(f[0] == tlo && f[1] == thi)) break;
    if (spin_out(t0)) break;
  }
  __builtin_amdgcn_fence(__ATOMIC_ACQUIRE, "wavefront");
}
DI bool gran_ok(u32x4 g, u32 glo, u32 ghi) { return (g[0] >> 16) == glo && g[1] == ghi && (g[2] >> 16) == glo && g[3] == ghi; }

#define STAMP_PARAM
#define STAMP_ARG
DI void front(const Params& p, LAS char* L, const int tid, const int w, const int b, u32x4 (&wr)[18], u32& tlo, u32& thi STAMP_PARAM) {
  const bool isC = w < 4;
  f32x2 xh[10][4];
  const u32 Lb = (u32)(size_t)L;
  const int lane = tid & 63, cw = w & 3;
  const int xcd = b & 7, idx = b >> 3;
  const int tt = 8 * (xcd >> 1) + (idx >> 2), hgl = idx & 3, hg = 4 * (xcd & 1) + hgl;
  const int jt = 8 * (xcd & 1) + 4 * (tt & 1) + hgl;
  const int T = tt >> 1, cb = hg;
  STAMP(0);
  if (tid < 32) *(LAS u32*)(L + L_CP + tid * 4) = 0u;
  const rsrc_t rx = mk(p.x, S_ * D_ * 2), rw1 = mk(p.wqkv, D_ * 3 * D_ * 2), rwo = mk(p.wo, D_ * D_ * 2);
  const rsrc_t rwg = mk(p.wg, D_ * F_ * 2), rwu = mk(p.wu, D_ * F_ * 2), rwd = mk(p.wd, F_ * D_ * 2);
  const rsrc_t rc = mk(p.c, D_ * 2), rwm = mk(p.wmod, D_ * 6 * D_ * 2), rout = mk(p.out, S_ * D_ * 2), rws = mk(p.ws, WS_BYTES);
  const int g = lane >> 4, i16 = lane & 15, h = lane >> 5, tok = lane & 31, t4 = tid & 255;

  const int nrot = 2 * hgl, rot = 2 * (tt & 7);
  const u32 w1v = (u32)((lane >> 3) * 3072 + (((((lane >> 1) & 3) ^ ((lane >> 4) & 1)) ^ ((cw & 1) << 1)) * 32) + (lane & 1) * 16);
  auto w1slot = [](int n) { return n < 6 ? L_RW + n * L_W1S : (n < 12 ? (n - 6) * L_W1S : L_RW + (n - 12) * L_W1S); };
  {
    u64 pc; asm volatile("s_getpc_b64 %0" : "=s"(pc));
    const rsrc_t rcode = mk((const void*)(pc & ~(u64)255), CODE_BYTES);
    warm(Lb + L_CP + 256, rcode, (u32)((idx * 8 + w) * 2 + (lane & 1)) * 128u, 0u);
  }
  u32x2 ev; ald64_sc1(ev, rws, 0u, WS_E);
  const bool prod1 = isC && idx < 8, prod2 = isC && idx >= 8 && idx < 24;
  const int cbk = idx < 8 ? xcd * 8 + idx : 64 + xcd * 16 + (idx - 8);
  auto mod_issue = [&](u32x4 (&wm)[4], u32x2& cv) {
#pragma unroll
    for (int i = 0; i < 4; ++i) ald128<0>(wm[i], rwm, (u32)((t4 >> 1) * 4 * 6144 + (t4 & 1) * 16), (u32)(i * 6144 + cbk * 32));
    ald64(cv, rc, (u32)((t4 >> 1) * 8), 0u);
  };
  auto dma_w1 = [&](int n0) {
#pragma unroll
    for (int n = n0; n < n0 + 4; ++n) {
      const int ks = (n + rot) & 15;
#pragma unroll
      for (int pp = 0; pp < 3; ++pp) dma16(rw1, w1v, (u32)((32 * ks + 8 * cw) * 3072 + pp * 1024 + hg * 128), Lb + (u32)(w1slot(n) + pp * 4096 + cw * 1024));
    }
  };
  auto mod_finish = [&](u32 pred, const u32x4 (&wm)[4], const u32x2& cv, u32 tlo, u32 thi) {
    float a8[8];
#pragma unroll
    for (int e = 0; e < 8; ++e) a8[e] = 0.f;
#pragma unroll
    for (int i = 0; i < 4; ++i) {
      const float cf = (i & 1) ? bfhi(cv[i >> 1]) : bflo(cv[i >> 1]);
      const float sv = rbf(cf * __builtin_amdgcn_rcpf(1.f + __expf(-cf)));
#pragma unroll
      for (int e = 0; e < 4; ++e) { a8[2 * e] = fmaf(sv, bflo(wm[i][e]), a8[2 * e]); a8[2 * e + 1] = fmaf(sv, bfhi(wm[i][e]), a8[2 * e + 1]); }
    }
#pragma unroll
    for (int e = 0; e < 8; ++e) { a8[e] = dpp_add<0x122>(a8[e]); a8[e] = dpp_add<0x124>(a8[e]); a8[e] = dpp_add<0x128>(a8[e]); }
    if (i16 < 2) {
      LAS f32x4* d = (LAS f32x4*)(L + pred + (cw * 4 + g) * 64 + i16 * 32);
      d[0] = f32x4{a8[0], a8[1], a8[2], a8[3]}; d[1] = f32x4{a8[4], a8[5], a8[6], a8[7]};
    }
    asm volatile("s_waitcnt lgkmcnt(0)" ::: "memory");
    if (lane == 0) *(volatile LAS u32x2*)(L + pred + 1024 + cw * 8) = u32x2{tlo, thi};
    if (cw == 0) {
      const u64 t0 = __builtin_amdgcn_s_memrealtime();
      for (;;) {
        const u32x2 tg = *(volatile LAS u32x2*)(L + pred + 1024 + (lane & 3) * 8);
        if (__all(tg[0] == tlo && tg[1] == thi)) break;
        if ((__builtin_amdgcn_s_memrealtime() - t0) > SPIN_TICKS) break;
      }
      asm volatile("" ::: "memory");
      LAS float* s = (LAS float*)(L + pred) + g * 64 + i16;
      float v = (s[0] + s[16]) + (s[32] + s[48]);
      v = swap16_add(v, v); v = swap32_add(v, v);
      const u32 mb = pk2(v, v) & 0xffffu;
      if (lane < 16) st64_sc1(u32x2{mb | (tlo << 16), (tlo >> 16) | (thi << 16)}, rws, (u32)(lane * 8), WS_MODG + (u32)cbk * 128u);
    }
  };
  u32x2 cv; u32x4 wm[4];
  if (prod1) mod_issue(wm, cv);
  u32x4 xr[10];
#pragma unroll
  for (int n = 0; n < 10; ++n) {
    int r8 = n + nrot; r8 = r8 >= 10 ? r8 - 10 : r8;
    int row = 64 * tt - 16 + 8 * r8 + (lane >> 3); row = row < 0 ? 0 : row;
    ald128<0>(xr[n], rx, (u32)(row * 1024 + (lane & 7) * 16), (u32)(w * 128));
  }
  if (prod2) mod_issue(wm, cv);
  if (!isC) dma_w1(0);
  STAMP(1);
  if (prod1) {
    asm volatile("s_waitcnt vmcnt(10)" : "+v"(wm[0]), "+v"(wm[1]), "+v"(wm[2]), "+v"(wm[3]), "+v"(cv), "+v"(ev) :: "memory");
    const u64 e1 = (((u64)__builtin_amdgcn_readfirstlane(ev[1]) << 32) | (u64)__builtin_amdgcn_readfirstlane(ev[0])) + 1ull;
    mod_finish(L_PRED, wm, cv, (u32)e1, (u32)(e1 >> 32));
  }
  STAMP(2);
  if (isC) { if (prod2) waitv<5>(); else if (prod1 && w == 0) waitv<1>(); else waitv<0>(); } else waitv<12>();
#pragma unroll
  for (int n = 0; n < 10; n += 2) asm volatile("" : "+v"(xr[n]), "+v"(xr[n + 1]));
  asm volatile("" : "+v"(ev));
  {
    const u64 e1 = (((u64)__builtin_amdgcn_readfirstlane(ev[1]) << 32) | (u64)__builtin_amdgcn_readfirstlane(ev[0])) + 1ull;
    tlo = (u32)e1; thi = (u32)(e1 >> 32);
  }
  STAMP(3);
  {
    const int c8 = lane & 7, r8l = lane >> 3;
    float pv[10];
#pragma unroll
    for (int n = 0; n < 10; ++n) {
#pragma unroll
      for (int e = 0; e < 4; ++e) xh[n][e] = f32x2{bflo(xr[n][e]), bfhi(xr[n][e])};
      const f32x2 a1 = (xh[n][0] + xh[n][1]) + (xh[n][2] + xh[n][3]);
      const f32x2 a2 = __builtin_elementwise_fma(xh[n][0], xh[n][0], xh[n][1] * xh[n][1]) + __builtin_elementwise_fma(xh[n][2], xh[n][2], xh[n][3] * xh[n][3]);
      const float s1 = a1[0] + a1[1], s2 = a2[0] + a2[1];
      const float keep = (c8 & 1) ? s2 : s1, give = (c8 & 1) ? s1 : s2;
      float v = keep + f_(__builtin_amdgcn_update_dpp(0, u_(give), 0xB1, 0xf, 0xf, true));
      v = dpp_add<0x4E>(v);
      pv[n] = v + f_(__builtin_amdgcn_update_dpp(0, u_(v), 0x104, 0xf, 0xf, true));
    }
    if (c8 < 2) {
#pragma unroll
      for (int n = 0; n < 10; ++n) *(LAS float*)(L + L_LNP + (n * 8 + r8l) * 80 + (w * 2 + c8) * 4) = pv[n];
    }
    bar();
    if (!isC) dma_w1(4);
#pragma unroll
    for (int k = 0; k < 2; ++k) {
      const int rho = k ? 64 + i16 : lane;
      const LAS f32x4* q = (const LAS f32x4*)(L + L_LNP + rho * 80);
      const f32x4 a = (q[0] + q[1]) + (q[2] + q[3]);
      const float mu = (a[0] + a[2]) * (1.f / 512.f);
      const float var = fmaxf((a[1] + a[3]) * (1.f / 512.f) - mu * mu, 0.f);
      const float rs = __builtin_amdgcn_rsqf(var + 1e-5f);
      *(LAS f32x2*)(L + L_RSNB + rho * 8) = f32x2{rs, -mu * rs};
      int r = rho + 8 * nrot; r = r >= 80 ? r - 80 : r;
      if (r >= 16 && w == 0) ((LAS float*)(L + L_MU1))[r - 16] = mu;
    }
    asm volatile("s_waitcnt lgkmcnt(0)" ::: "memory");
#pragma unroll
    for (int n = 0; n < 10; ++n) {
      const f32x2 rn = *(const LAS f32x2*)(L + L_RSNB + (n * 8 + r8l) * 8);
#pragma unroll
      for (int e = 0; e < 4; ++e) xh[n][e] = __builtin_elementwise_fma(xh[n][e], f32x2{rn[0], rn[0]}, f32x2{rn[1], rn[1]});
    }
  }
#pragma unroll
  for (int n = 0; n < 10; ++n) asm volatile("" : "+v"(xh[n][0]), "+v"(xh[n][1]), "+v"(xh[n][2]), "+v"(xh[n][3]));
  STAMP(4);
  if (!isC) { dma_w1(8); waitv<0>(); }
  if (prod2) {
    asm volatile("s_waitcnt vmcnt(0)" : "+v"(wm[0]), "+v"(wm[1]), "+v"(wm[2]), "+v"(wm[3]), "+v"(cv) :: "memory");
    mod_finish(L_PRED, wm, cv, tlo, thi);
  }
  bar();
  if (isC) {
    LAS char* w1rd = L + ((8 * g + (i16 >> 2)) * 128 + ((cw ^ (((i16 >> 3) & 1) | ((g & 1) << 1))) * 32) + (lane & 3) * 8);
    const u32 glo = tlo & 0xffffu, ghi = (tlo >> 16) | (thi << 16);
    __builtin_amdgcn_s_setprio(2);
#pragma unroll
    for (int n = 0; n < 6; ++n)
#pragma unroll
      for (int pp = 0; pp < 3; ++pp) wr[3 * n + pp] = trd2(w1rd + n * L_W1S + pp * 4096, 512);
    STAMP(5);
    {
      u32x4 ga_, gc_;
      const u64 t0 = __builtin_amdgcn_s_memrealtime();
      for (;;) {
        ga_ = ld128_sc1(rws, (u32)(tid * 16), WS_MODG); gc_ = ld128_sc1(rws, (u32)(tid * 16), WS_MODG + 4096);
        if (__all(gran_ok(ga_, glo, ghi) && gran_ok(gc_, glo, ghi))) break;
        if (spin_out(t0)) break;
      }
      *(LAS f32x2*)(L + L_S1 + tid * 8) = f32x2{bflo(ga_[0]), bflo(ga_[2])};
      *(LAS f32x2*)(L + L_G1 + tid * 8) = f32x2{rbf(1.f + bflo(gc_[0])), rbf(1.f + bflo(gc_[2]))};
    }
    STAMP(6);
  }
  bar();
  if (isC) {
    {
      const u32 lo = (u32)(b + 256 * lane) * 128u, o64 = (u32)lane * 65536u;
      if (cw == 0) { warm(Lb + L_CP + 256, rwg, lo, 0u); warm(Lb + L_CP + 256, rws, o64 * 4u, WS_O); }
      else if (cw == 1) { warm(Lb + L_CP + 256, rwu, lo, 0u); warm(Lb + L_CP + 256, rout, o64 * 4u, 0u); }
      else if (cw == 2) warm(Lb + L_CP + 256, rwd, lo, 0u);
    }
  }
  {
    const int c8 = lane & 7, r8l = lane >> 3;
    const LAS f32x4* gp = (const LAS f32x4*)(L + L_G1 + (w * 64 + c8 * 8) * 4);
    const LAS f32x4* sp = (const LAS f32x4*)(L + L_S1 + (w * 64 + c8 * 8) * 4);
    const f32x4 g0 = gp[0], g1 = gp[1], s0 = sp[0], s1 = sp[1];
    LAS char* ab = L + L_AIMG + (2 * w + (c8 >> 2)) * 1024 + (c8 & 3) * 256 + ((r8l ^ c8) * 16);
#pragma unroll
    for (int n = 0; n < 10; ++n) {
      const f32x2 h0 = __builtin_elementwise_fma(xh[n][0], f32x2{g0[0], g0[1]}, f32x2{s0[0], s0[1]}), h1 = __builtin_elementwise_fma(xh[n][1], f32x2{g0[2], g0[3]}, f32x2{s0[2], s0[3]});
      const f32x2 h2 = __builtin_elementwise_fma(xh[n][2], f32x2{g1[0], g1[1]}, f32x2{s1[0], s1[1]}), h3 = __builtin_elementwise_fma(xh[n][3], f32x2{g1[2], g1[3]}, f32x2{s1[2], s1[3]});
      const u32x4 o = u32x4{pk2(h0[0], h0[1]), pk2(h1[0], h1[1]), pk2(h2[0], h2[1]), pk2(h3[0], h3[1])};
      int r8 = n + nrot; r8 = r8 >= 10 ? r8 - 10 : r8;
      *(LAS u32x4*)(ab + (r8 >> 1) * 16384 + (r8 & 1) * 128) = o;
    }
  }
  bar();
  STAMP(7);
}

template <bool isC> DI void role_body(const Params& p, LAS char* L, const int tid, const int w, const int b, u32x4 (&wr)[18], const u32 tlo, const u32 thi STAMP_PARAM) {
  const u32 Lb = (u32)(size_t)L;
  const int lane = tid & 63, cw = w & 3;
  const int xcd = b & 7, idx = b >> 3;
  const int tt = 8 * (xcd >> 1) + (idx >> 2), hgl = idx & 3, hg = 4 * (xcd & 1) + hgl;
  const int jt = 8 * (xcd & 1) + 4 * (tt & 1) + hgl;
  const int T = tt >> 1, cb = hg;
  const rsrc_t rx = mk(p.x, S_ * D_ * 2), rw1 = mk(p.wqkv, D_ * 3 * D_ * 2), rwo = mk(p.wo, D_ * D_ * 2);
  const rsrc_t rwg = mk(p.wg, D_ * F_ * 2), rwu = mk(p.wu, D_ * F_ * 2), rwd = mk(p.wd, F_ * D_ * 2);
  const rsrc_t rc = mk(p.c, D_ * 2), rwm = mk(p.wmod, D_ * 6 * D_ * 2), rout = mk(p.out, S_ * D_ * 2), rws = mk(p.ws, WS_BYTES);
  const int g = lane >> 4, i16 = lane & 15, h = lane >> 5, tok = lane & 31, t4 = tid & 255;

  const int nrot = 2 * hgl, rot = 2 * (tt & 7);
  const u32 w1v = (u32)((lane >> 3) * 3072 + (((((lane >> 1) & 3) ^ ((lane >> 4) & 1)) ^ ((cw & 1) << 1)) * 32) + (lane & 1) * 16);
  auto w1slot = [](int n) { return n < 6 ? L_RW + n * L_W1S : (n < 12 ? (n - 6) * L_W1S : L_RW + (n - 12) * L_W1S); };
  LAS char* w1rd = L + ((8 * g + (i16 >> 2)) * 128 + ((cw ^ (((i16 >> 3) & 1) | ((g & 1) << 1))) * 32) + (lane & 3) * 8);
  const u32 glo = tlo & 0xffffu, ghi = (tlo >> 16) | (thi << 16);
  const int p0 = jt & 3, rotg = 4 * (T & 3), rotd = tt & 3;
  const u32 wov = (u32)((lane >> 3) * 1024 + ((((lane >> 2) & 1) ^ ((lane >> 4) & 1)) * 64) + (lane & 3) * 16);
  const int mrow = (lane >> 4) & 1, q4 = i16 >> 2;
  const u32 p1base = (u32)(mrow * 4096 + (4 * h + q4) * 128 + (lane & 3) * 8), p1x = (u32)((q4 >> 1) | (mrow << 1));
  const u32 p2base = (u32)(mrow * L_P2M + (4 * h + q4) * 96 + (lane & 3) * 8);

  if constexpr (!isC) {
    LAS char* const CP = L + L_CP; LAS char* const HP = L + L_HP; LAS char* const myHP = HP + cw;
    u32x4 w1t[12];
#pragma unroll
    for (int n = 12; n < 16; ++n)
#pragma unroll
      for (int pp = 0; pp < 3; ++pp) ald128<0>(w1t[3 * (n - 12) + pp], rw1, w1v, (u32)((32 * ((n + rot) & 15) + 8 * cw) * 3072 + pp * 1024 + hg * 128));
    u32x4 woh[18], p2h[8][3];
#pragma unroll
    for (int q = 0; q < 16; ++q) ald128<0>(woh[q], rwo, wov, (u32)((128 * cw + 8 * ((q + rot) & 15)) * 1024 + cb * 128));
#pragma unroll
    for (int q = 0; q < 2; ++q) ald128<0>(woh[16 + q], rx, (u32)((lane >> 3) * 1024 + (lane & 7) * 16), (u32)((64 * tt + 16 * cw + 8 * q) * 1024 + cb * 128));
    lwait<true>(CP, 1);
    waitv<18>();
#pragma unroll
    for (int n = 0; n < 12; n += 4) asm volatile("" : "+v"(w1t[n]), "+v"(w1t[n + 1]), "+v"(w1t[n + 2]), "+v"(w1t[n + 3]));
#pragma unroll
    for (int n = 0; n < 12; ++n) *(LAS u32x4*)(L + L_RW + (n / 3) * L_W1S + (n % 3) * 4096 + cw * 1024 + lane * 16) = w1t[n];
    lsig(myHP, 1, lane);
    STAMP(8);
    {
      u32x4 gs_, gc_, gg_;
      const u64 t0 = __builtin_amdgcn_s_memrealtime();
      for (;;) {
        gs_ = ld128_sc1(rws, (u32)(t4 * 16), WS_MODG + 3 * 4096); gc_ = ld128_sc1(rws, (u32)(t4 * 16), WS_MODG + 4 * 4096);
        gg_ = ld128_sc1(rws, (u32)(tok * 16 + h * 3 * 4096), WS_MODG + 2 * 4096 + (u32)cb * 512u);
        if (__all(gran_ok(gs_, glo, ghi) && gran_ok(gc_, glo, ghi) && gran_ok(gg_, glo, ghi))) break;
        if (spin_out(t0)) break;
      }
      const float g2a = rbf(1.f + bflo(gc_[0])), g2b = rbf(1.f + bflo(gc_[2]));
      const int kk = (2 * t4) & 31;
      LAS char* gd = L + L_GS + (t4 >> 4) * 128 + ((kk >> 2) & 3) * 32 + ((((kk >> 4) << 2) | (kk & 3)) * 2);
      *(LAS u32*)gd = pk2(g2a, g2b);
      *(LAS u32*)(gd + 16) = (gs_[0] & 0xffffu) | (gs_[2] << 16);
      if ((t4 >> 5) == cb) *(LAS f32x2*)(L + L_G2C + tok * 8) = f32x2{g2a, g2b};
      if (cw == 0) *(LAS f32x2*)(L + (h ? L_GBV : L_GA) + tok * 8) = f32x2{bflo(gg_[0]), bflo(gg_[2])};
    }
    u32 p2v[3];
#pragma unroll
    for (int i = 0; i < 3; ++i) { const int n = 64 * i + lane, row = n / 6, c = n - row * 6; p2v[i] = (u32)(row * 3584 + c * 16 + ((c >> 1) >= p0 ? 128 : 0)); }
    const rsrc_t rgu = (cw & 1) ? rwu : rwg;
#pragma unroll
    for (int k = 0; k < 8; ++k)
#pragma unroll
      for (int i = 0; i < 3; ++i) ald128<0>(p2h[k][i], rgu, p2v[i], (u32)(((2 * k + (cw >> 1) + rotg) & 15) * 32 * 3584 + jt * 224));
    STAMP(9);
    lwait<true>(CP, 2);
    waitv<24>();
#pragma unroll
    for (int q = 0; q < 18; q += 3) asm volatile("" : "+v"(woh[q]), "+v"(woh[q + 1]), "+v"(woh[q + 2]));
#pragma unroll
    for (int q = 0; q < 16; ++q) *(LAS u32x4*)(L + L_WO + (16 * cw + ((q + rot) & 15)) * 1024 + lane * 16) = woh[q];
#pragma unroll
    for (int q = 0; q < 2; ++q) *(LAS u32x4*)(L + L_XRES + (2 * cw + q) * 1024 + lane * 16) = woh[16 + q];
    lsig(myHP, 2, lane); STAMP(10);
    const u32 p1v = (u32)((lane >> 3) * 3584 + ((((lane >> 1) & 3) ^ ((lane >> 4) & 1)) * 32) + (lane & 1) * 16);
    auto dma_p1 = [&](int k) {
      const u32 so = (u32)((32 * ((k + rotg) & 15) + 8 * cw) * 3584 + jt * 224 + p0 * 32);
      dma16(rwg, p1v, so, Lb + (u32)(k * L_P1B + cw * 1024));
      dma16(rwu, p1v ^ 64u, so, Lb + (u32)(k * L_P1B + 4096 + cw * 1024));
    };
#pragma unroll
    for (int k = 0; k < 8; ++k) dma_p1(k);
    waitv<0>(); lsig(myHP, 4, lane); STAMP(11);
#pragma unroll
    for (int k = 0; k < 8; ++k) asm volatile("" : "+v"(p2h[k][0]), "+v"(p2h[k][1]), "+v"(p2h[k][2]));
    lwait<true>(CP, 3);
#pragma unroll
    for (int k = 8; k < 16; ++k) dma_p1(k);
    auto wd_src = [&](int u, int q) {
      const int n = 8 * (u - 4) + q;
      const int jj = ((u < 4 ? u : n / 6) + rotd) & 3, r6 = n % 6;
      const int row = u < 4 ? 112 * jj + 16 * jj + 8 * q : 112 * jj + 8 * r6 + ((r6 >> 1) >= jj ? 64 : 0);
      return (u32)((448 * cw + row) * 1024 + cb * 128);
    };
    u32x4 wdh[3][8];
#pragma unroll
    for (int u = 0; u < 3; ++u)
#pragma unroll
      for (int q = 0; q < 8; ++q) ald128<0>(wdh[u][q], rwd, wov, wd_src(u, q));
    lwait<true>(HP, 4);
    f32x4 ua, ub;
    auto ut_blocks = [&](LAS char* ba, LAS char* bb, int bstride, int tstride, int k0, int k1) {
      u32x4 fa[2], fb[2], gv[2];
      auto ld = [&](int k, int bi) {
        fa[bi] = trd2(ba + k * bstride, tstride); fb[bi] = trd2(bb + k * bstride, tstride);
        gv[bi] = *(const LAS u32x4*)(L + L_GS + ((k + rotg) & 15) * 128 + g * 32 + (lane & 1) * 16);
      };
      ld(k0, 0);
#pragma unroll
      for (int k = k0; k < k1; ++k) {
        if (k + 1 < k1) ld(k + 1, (k + 1 - k0) & 1);
        SB();
        ua = mfma16(gv[(k - k0) & 1], fa[(k - k0) & 1], ua); ub = mfma16(gv[(k - k0) & 1], fb[(k - k0) & 1], ub);
        SB();
      }
      asm volatile("" : "+v"(ua), "+v"(ub));
    };
    auto put_fix = [&](const f32x4& v, int slot, int m) {
      const u32 hi = pk2(v[0], v[1]), lo = pk2(v[0] - bflo(hi), v[1] - bfhi(hi));
      const u32 u1 = hi & 0xffffu, t1 = hi >> 16, u2 = lo & 0xffffu, t2 = lo >> 16;
      const u32x4 o = u32x4{u1 | (u1 << 16), u2 | (u2 << 16), t1 | (t1 << 16), t2 | (t2 << 16)};
      if (lane < 16) { LAS char* d = L + L_FIX + slot * 1024 + (16 * m + lane) * 16; *(LAS u32x4*)d = o; *(LAS u32x4*)(d + 512) = o; }
    };
    LAS char* u1a = L + ((4 * g + q4) * 128 + (lane & 3) * 8 + ((cw ^ (q4 >> 1)) * 32));
    LAS char* u1b = L + (4096 + (4 * g + q4) * 128 + (lane & 3) * 8 + ((cw ^ (q4 >> 1) ^ 2) * 32));
    ua = f32x4{0, 0, 0, 0}; ub = f32x4{0, 0, 0, 0};
    ut_blocks(u1a, u1b, L_P1B, 2048, 0, 8);
    waitv<24>(); lsig(myHP, 5, lane); STAMP(12);
    lwait<true>(HP, 5);
    ut_blocks(u1a, u1b, L_P1B, 2048, 8, 16);
    put_fix(ua, cw, 0); put_fix(ub, cw, 1);
    lsig(myHP, 6, lane); STAMP(13);
    auto put_p2 = [&](int k0, int k1) {
#pragma unroll
      for (int k = k0; k < k1; ++k)
#pragma unroll
        for (int i = 0; i < 3; ++i) *(LAS u32x4*)(L + (2 * k + (cw >> 1)) * L_P2B + (cw & 1) * L_P2M + i * 1024 + lane * 16) = p2h[k][i];
    };
    LAS char* u2a = L + ((cw & 1) * L_P2M + (4 * g + q4) * 96 + (lane & 3) * 8 + (cw >> 1) * 32);
    LAS char* u2b = L + ((cw & 1) * L_P2M + (4 * g + q4) * 96 + (lane & 3) * 8 + 64);
    ua = f32x4{0, 0, 0, 0}; ub = f32x4{0, 0, 0, 0};
    lwait<true>(CP, 4);
    put_p2(0, 2);
    lwait<true>(CP, 5);
    put_p2(2, 5); lsig(myHP, 7, lane);
    STAMP(14);
    lwait<true>(HP, 7);
    ut_blocks(u2a, u2b, L_P2B, 1536, 0, 10);
    lwait<true>(CP, 6);
    put_p2(5, 7);
    lwait<true>(CP, 7);
    put_p2(7, 8); lsig(myHP, 9, lane);
    STAMP(15);
    waitv<0>();
#pragma unroll
    for (int u = 0; u < 3; ++u) { asm volatile("" : "+v"(wdh[u][0]), "+v"(wdh[u][1]), "+v"(wdh[u][2]), "+v"(wdh[u][3])); asm volatile("" : "+v"(wdh[u][4]), "+v"(wdh[u][5]), "+v"(wdh[u][6]), "+v"(wdh[u][7])); }
    auto put_wd = [&](int u) {
#pragma unroll
      for (int q = 0; q < 8; ++q) *(LAS u32x4*)(L + wd_unit_off(u) + cw * 8192 + q * 1024 + lane * 16) = wdh[u][q];
    };
    auto dma_wd = [&](int u) {
#pragma unroll
      for (int q = 0; q < 8; ++q) dma16(rwd, wov, wd_src(u, q), Lb + (u32)(wd_unit_off(u) + cw * 8192 + q * 1024));
    };
    put_wd(0);
    lwait<true>(HP, 9);
    ut_blocks(u2a, u2b, L_P2B, 1536, 10, 16);
    put_fix(ua, cw >> 1, cw & 1); if (cw < 2) put_fix(ub, 2, cw & 1);
    lsig(myHP, 11, lane); STAMP(16);
    lwait<true>(CP, 9);
    put_wd(1); lsig(myHP, 12, lane);
    lwait<true>(CP, 10); lwait<true>(HP, 11);
    put_wd(2); lsig(myHP, 13, lane);
    lwait<true>(CP, 11);
    dma_wd(3); STAMP(17);
    lwait<true>(CP, 12);
    dma_wd(4);
    waitv<8>(); lsig(myHP, 14, lane);
    lwait<true>(CP, 13);
    dma_wd(5);
    waitv<8>(); lsig(myHP, 15, lane);
    lwait<true>(CP, 14);
    dma_wd(6);
    waitv<8>(); lsig(myHP, 16, lane); STAMP(18);
    waitv<0>(); lsig(myHP, 17, lane);
  } else {
    LAS char* const CP = L + L_CP; LAS char* const HP = L + L_HP; LAS char* const myCP = CP + cw;
    u32 hv;
    f32x4 Kt[5], Qt[4], Vn[5];
#pragma unroll
    for (int r = 0; r < 5; ++r) { Kt[r] = f32x4{0, 0, 0, 0}; Vn[r] = f32x4{0, 0, 0, 0}; if (r < 4) Qt[r] = f32x4{0, 0, 0, 0}; }
    {
      u32x4 a[2][5], wl[2][3];
      auto ld = [&](int n, int bi) {
        LAS char* ab = L + L_AIMG + ((n + rot) & 15) * 1024 + g * 256 + ((i16 ^ (((n & 1) << 2) | g)) * 16);
#pragma unroll
        for (int rb = 0; rb < 5; ++rb) a[bi][rb] = *(const LAS u32x4*)(ab + rb * 16384);
        if (n < 6 || n >= 12) {
#pragma unroll
          for (int pp = 0; pp < 3; ++pp) wl[bi][pp] = trd2(w1rd + w1slot(n) + pp * 4096, 512);
        }
      };
      ld(0, 0); SB();
#pragma unroll
      for (int n = 0; n < 16; ++n) {
        if (n == 3) lsig(myCP, 1, lane);
        if (n == 11) WACC(24, lwait_from<false>(hv, HP, 1));
        if (n == 10) hv = lpeek(HP);
        if (n < 15) ld(n + 1, (n + 1) & 1);
        const bool rg = n >= 6 && n < 12;
        const u32x4 wq = rg ? wr[rg ? 3 * (n - 6) : 0] : wl[n & 1][0], wk = rg ? wr[rg ? 3 * (n - 6) + 1 : 0] : wl[n & 1][1], wv = rg ? wr[rg ? 3 * (n - 6) + 2 : 0] : wl[n & 1][2];
#pragma unroll
        for (int rb = 0; rb < 5; ++rb) {
          Kt[rb] = mfma16(wk, a[n & 1][rb], Kt[rb]);
          Vn[rb] = mfma16(a[n & 1][rb], wv, Vn[rb]);
          if (rb > 0) Qt[rb - 1] = mfma16(wq, a[n & 1][rb], Qt[rb - 1]);
        }
#pragma unroll
        for (int k = 0; k < 11; ++k) { SGB_MFMA(1); SGB_DSR(1); }
        SGB_MFMA(3);
        SB();
      }
    }
    lsig(myCP, 2, lane);
    STAMP(8);
    const int tb = cw & 1, kh = cw >> 1;
    {
      u32x2 kp[5], vp[5];
#pragma unroll
      for (int r = 0; r < 5; ++r) { kp[r] = u32x2{pk2(Kt[r][0], Kt[r][1]), pk2(Kt[r][2], Kt[r][3])}; vp[r] = u32x2{pk2(Vn[r][0], Vn[r][1]), pk2(Vn[r][2], Vn[r][3])}; }
      f32x4 m0, m1, m0f;
#pragma unroll
      for (int r = 0; r < 4; ++r) {
        const int d1 = i16 - 4 * g - r, d0 = d1 + 16;
        m0[r] = (d0 <= 14 && !(d0 & 1)) ? 0.f : -1e30f;
        m1[r] = (d1 >= 0 && d1 <= 14 && !(d1 & 1)) ? 0.f : -1e30f;
        m0f[r] = tt == 0 ? -1e30f : m0[r];
      }
      u32x2 op[4];
      const u32x4 ones = u32x4{0x3f803f80u, 0x3f803f80u, 0x3f803f80u, 0x3f803f80u};
#pragma unroll
      for (int qb = 0; qb < 4; ++qb) {
        const u32x2 qp = u32x2{pk2(Qt[qb][0], Qt[qb][1]), pk2(Qt[qb][2], Qt[qb][3])};
        const f32x4 s0 = mfma16k16(kp[qb], qp, qb == 0 ? m0f : m0), s1 = mfma16k16(kp[qb + 1], qp, m1);
        float mx = fmaxf(fmaxf(fmaxf(s0[0], s0[1]), fmaxf(s0[2], s0[3])), fmaxf(fmaxf(s1[0], s1[1]), fmaxf(s1[2], s1[3])));
        mx = swap16_max(mx); mx = swap32_max(mx);
        const float cs = 0.25f * 1.44269504088896f, nm = -mx * cs;
        float e0[4], e1[4];
#pragma unroll
        for (int r = 0; r < 4; ++r) { e0[r] = __builtin_amdgcn_exp2f(fmaf(s0[r], cs, nm)); e1[r] = __builtin_amdgcn_exp2f(fmaf(s1[r], cs, nm)); }
        const u32x4 pf = u32x4{pk2(e0[0], e0[1]), pk2(e0[2], e0[3]), pk2(e1[0], e1[1]), pk2(e1[2], e1[3])};
        const f32x4 z = f32x4{0, 0, 0, 0};
        const f32x4 ot = mfma16(u32x4{vp[qb][0], vp[qb][1], vp[qb + 1][0], vp[qb + 1][1]}, pf, z);
        const f32x4 sm = mfma16(ones, pf, z);
        const float ri = __builtin_amdgcn_rcpf(sm[0]);
        op[qb] = u32x2{pk2(ot[0] * ri, ot[1] * ri), pk2(ot[2] * ri, ot[3] * ri)};
      }
#pragma unroll
      for (int t2 = 0; t2 < 2; ++t2) {
        auto r0 = __builtin_amdgcn_permlane16_swap(op[2 * t2][0], op[2 * t2 + 1][0], false, false);
        auto r1 = __builtin_amdgcn_permlane16_swap(op[2 * t2][1], op[2 * t2 + 1][1], false, false);
        const u32 a0 = r0[0], b0 = r0[1], a1 = r1[0], b1 = r1[1];
        st128_sc1(u32x4{a0, a1, b0, b1}, rws, (u32)(lane * 16), WS_O + (u32)(((2 * tt + t2) * 32 + 4 * hg + cw) * 1024));
      }
    }
    STAMP(9);
    publish<0>(L + L_CNT, rws, WS_FO + (u32)(tt * 256 + hg * 8), tlo, thi, lane);
    lwait<false>(HP, 2);
    STAMP(10);
    wait_flags<8>(rws, WS_FO + (u32)(tt * 256), lane, tlo, thi);
    STAMP(11);
    u32 x1p[8];
    {
      u32x4 of[16];
      const u32 ob = WS_O + (u32)(((2 * tt + tb) * 32 + 16 * kh) * 1024);
#pragma unroll
      for (int s = 0; s < 16; ++s) of[s] = ld128_sc1(rws, (u32)(lane * 16), ob + (u32)(s * 1024));
      SB();
      LAS char* q0 = L + (L_WO + (256 * kh + 8 * h + q4) * 128 + mrow * 32 + (lane & 3) * 8);
      LAS char* qa = q0 + ((q4 >> 1) ^ kh) * 64; LAS char* qb = q0 + ((q4 >> 1) ^ kh ^ 1) * 64;
      f32x16 a0, a1;
#pragma unroll
      for (int r = 0; r < 16; ++r) { a0[r] = 0.f; a1[r] = 0.f; }
      u32x4 wl[2][2];
      wl[0][0] = trd2(qa, 512); wl[0][1] = trd2(qb, 512); SB();
#pragma unroll
      for (int s = 0; s < 16; ++s) {
        if (s < 15) { wl[(s + 1) & 1][0] = trd2(qa + (s + 1) * 2048, 512); wl[(s + 1) & 1][1] = trd2(qb + (s + 1) * 2048, 512); }
        a0 = mfma32(wl[s & 1][0], of[s], a0); a1 = mfma32(wl[s & 1][1], of[s], a1);
        SGB_MFMA(1); SGB_DSR(2); SGB_MFMA(1); SGB_DSR(2);
        SB();
      }
      u32x2 xres[4];
#pragma unroll
      for (int q = 0; q < 4; ++q) xres[q] = *(const LAS u32x2*)(L + L_XRES + (32 * tb + tok) * 128 + (32 * kh + 8 * q + 4 * h) * 2);
      const f32x16 snd = a1, acc = a0;
#pragma unroll
      for (int q = 0; q < 4; ++q) *(LAS f32x4*)(L + L_RED2 + cw * 4096 + q * 1024 + lane * 16) = f32x4{snd[4 * q], snd[4 * q + 1], snd[4 * q + 2], snd[4 * q + 3]};
      lsig(L + L_RX + cw, 1, lane); lsig(myCP, 3, lane);
      STAMP(19);
      lwait<false>(L + L_RX, 1);
      STAMP(20);
      const float mu1 = ((const LAS float*)(L + L_MU1))[32 * tb + tok];
      float s1 = 0.f, s2 = 0.f; u32 yp[8];
#pragma unroll
      for (int q = 0; q < 4; ++q) {
        const f32x4 o4 = *(const LAS f32x4*)(L + L_RED2 + (cw ^ 2) * 4096 + q * 1024 + lane * 16);
        const u32x2 xq = xres[q];
        const f32x4 ga4 = *(const LAS f32x4*)(L + L_GA + (32 * kh + 8 * q + 4 * h) * 4), g24 = *(const LAS f32x4*)(L + L_G2C + (32 * kh + 8 * q + 4 * h) * 4);
        const float v0 = fmaf(ga4[0], acc[4 * q] + o4[0], bflo(xq[0])), v1 = fmaf(ga4[1], acc[4 * q + 1] + o4[1], bfhi(xq[0]));
        const float v2 = fmaf(ga4[2], acc[4 * q + 2] + o4[2], bflo(xq[1])), v3 = fmaf(ga4[3], acc[4 * q + 3] + o4[3], bfhi(xq[1]));
        x1p[2 * q] = pk2(v0, v1); x1p[2 * q + 1] = pk2(v2, v3);
        const float d0 = bflo(x1p[2 * q]) - mu1, d1 = bfhi(x1p[2 * q]) - mu1, d2 = bflo(x1p[2 * q + 1]) - mu1, d3 = bfhi(x1p[2 * q + 1]) - mu1;
        s1 += (d0 + d1) + (d2 + d3); s2 = fmaf(d0, d0, fmaf(d1, d1, fmaf(d2, d2, fmaf(d3, d3, s2))));
        yp[2 * q] = pk2(d0 * g24[0], d1 * g24[1]); yp[2 * q + 1] = pk2(d2 * g24[2], d3 * g24[3]);
      }
      const u32 yb = WS_Y + (u32)(((2 * tt + tb) * 32 + 4 * cb + 2 * kh) * 1024);
      st128_sc1(u32x4{yp[0], yp[1], yp[2], yp[3]}, rws, (u32)(lane * 16), yb);
      st128_sc1(u32x4{yp[4], yp[5], yp[6], yp[7]}, rws, (u32)(lane * 16), yb + 1024);
      s1 = swap32_add(s1, s1); s2 = swap32_add(s2, s2);
      if (lane < 32) st64_sc1(u32x2{u_(s1), u_(s2)}, rws, (u32)(tok * 8), WS_ST + (u32)(((2 * tt + tb) * 16 + 2 * cb + kh) * 256));
    }
#pragma unroll
    for (int q = 0; q < 8; q += 4) asm volatile("" : "+v"(x1p[q]), "+v"(x1p[q + 1]), "+v"(x1p[q + 2]), "+v"(x1p[q + 3]));
    STAMP(12);
    publish<0>(L + L_CNT + 4, rws, WS_FY + (u32)(tt * 256 + cb * 8), tlo, thi, lane);
    STAMP(13);
    lwait<false>(HP, 4);
    wait_flags<8>(rws, WS_FY + (u32)((2 * T + (cw >> 1)) * 256), lane, tlo, thi);
    STAMP(14);
    const int tb32 = 4 * T + cw;
    u32x2 stv[8];
#pragma unroll
    for (int q = 0; q < 8; ++q) stv[q] = ld64_sc1(rws, (u32)(tok * 8 + h * 2048), WS_ST + (u32)((tb32 * 16 + q) * 256));
    u32x4 yf[32];
    auto ldy = [&](int s) { yf[s] = ld128_sc1(rws, (u32)(lane * 16), WS_Y + (u32)((tb32 * 32 + ((s + 2 * rotg) & 31)) * 1024)); };
#pragma unroll
    for (int s = 0; s < (EXPW ? 32 : 10); ++s) ldy(s);
    SB();
    u32 mz, sz; float r2;
    {
      float s1 = 0.f, s2 = 0.f;
#pragma unroll
      for (int q = 0; q < 8; ++q) { s1 += f_(stv[q][0]); s2 += f_(stv[q][1]); }
      s1 = swap32_add(s1, s1); s2 = swap32_add(s2, s2);
      const float md = s1 * (1.f / 512.f);
      const float ve = fmaxf(s2 * (1.f / 512.f) - md * md, 0.f) + 1e-5f;
      const float sg = __builtin_amdgcn_sqrtf(ve); r2 = __builtin_amdgcn_rcpf(ve);
      const u32 hi = pk2(-md, sg), lo = pk2(-md - bflo(hi), sg - bfhi(hi));
      const u32 mm = (hi & 0xffffu) | (lo << 16), ss = (hi >> 16) | (lo & 0xffff0000u);
      mz = h ? 0u : mm; sz = h ? 0u : ss;
      asm volatile("" : "+v"(mz), "+v"(sz), "+v"(r2));
    }
    SB();
    auto act_store = [&](const f32x16& ac, int pp) {
      u32x4 o;
#pragma unroll
      for (int e = 0; e < 4; ++e) o[e] = pk2(fmaxf(ac[2 * e], 0.f) * r2 * ac[8 + 2 * e], fmaxf(ac[2 * e + 1], 0.f) * r2 * ac[9 + 2 * e]);
      st128_sc1(o, rws, (u32)(lane * 16), WS_ACT + (u32)((tb32 * 112 + 7 * jt + pp) * 1024));
    };
    {
      f32x16 acc[4];
#pragma unroll
      for (int q = 0; q < 4; ++q)
#pragma unroll
        for (int r = 0; r < 16; ++r) acc[q][r] = 0.f;
      LAS char* pa[4];
#pragma unroll
      for (int pp = 0; pp < 4; ++pp) pa[pp] = L + (p1base + (((u32)pp ^ p1x) * 32));
      u32x4 wf[2][4];
      auto ldw = [&](int st, int bi) {
#pragma unroll
        for (int pp = 0; pp < 4; ++pp) wf[bi][pp] = trd2(pa[pp] + (st >> 1) * L_P1B + (st & 1) * 2048, 1024);
      };
      ldw(0, 0); SB();
#pragma unroll
      for (int st = 0; st < 32; ++st) {
        if (st == 7 || st == 15 || st == 23) lsig(myCP, 4 + st / 8, lane);
        if (st == 15) WACC(25, lwait_from<false>(hv, HP, 5));
        if (st == 14 || st == 31) hv = lpeek(HP);
        if (st + 1 < 32) ldw(st + 1, (st + 1) & 1);
        if (!EXPW && st + 10 < 32) ldy(st + 10);
#pragma unroll
        for (int pp = 0; pp < 4; ++pp) acc[pp] = mfma32(wf[st & 1][pp], yf[st], acc[pp]);
#pragma unroll
        for (int pp = 0; pp < 4; ++pp) { SGB_MFMA(1); SGB_DSR(2); }
        SB();
      }
      WACC(26, lwait_from<false>(hv, HP, 7));
#pragma unroll
      for (int pp = 0; pp < 4; ++pp) acc[pp] = mfma32(*(const LAS u32x4*)(L + L_FIX + pp * 1024 + lane * 16), u32x4{mz, mz, sz, sz}, acc[pp]);
      lsig(myCP, 7, lane);
      STAMP(15);
#pragma unroll
      for (int pp = 0; pp < 4; ++pp) act_store(acc[pp], p0 + pp);
    }
    u32x4 af[16][2];
    auto lda = [&](int n) { const int s = n >> 1, t2 = n & 1; af[s][t2] = ld128_sc1(rws, (u32)(lane * 16), WS_ACT + (u32)(((2 * tt + (t2 ^ tb)) * 112 + 28 * cw + 8 * (((s >> 2) + rotd) & 3) + (s & 3)) * 1024)); };
    {
      f32x16 acc[3];
#pragma unroll
      for (int q = 0; q < 3; ++q)
#pragma unroll
        for (int r = 0; r < 16; ++r) acc[q][r] = 0.f;
      LAS char* pb = L + p2base;
      u32x4 wf[2][3];
      auto ldw = [&](int st, int bi) {
#pragma unroll
        for (int pp = 0; pp < 3; ++pp) wf[bi][pp] = trd2(pb + (st >> 1) * L_P2B + (st & 1) * 1536 + pp * 32, 768);
      };
      ldw(0, 0); SB();
#pragma unroll
      for (int st = 0; st < 32; ++st) {
        if (st == 4) WACC(30, publish<0>(L + L_CNT + 8, rws, WS_FA + (u32)(T * 256 + jt * 8), tlo, thi, lane));
        if (st == 15 || st == 23) lsig(myCP, 8 + st / 8, lane);
        if (st == 19) WACC(27, lwait_from<false>(hv, HP, 9));
        if (st == 18 || st == 31) hv = lpeek(HP);
        if (st == 12) { STAMP(23); WACC(28, wait_flags<4>(rws, WS_FA + (u32)(T * 256 + cw * 32), lane, tlo, thi)); }
        if (st + 1 < 32) ldw(st + 1, (st + 1) & 1);
        if (st >= 12) lda(st - 12);
#pragma unroll
        for (int pp = 0; pp < 3; ++pp) acc[pp] = mfma32(wf[st & 1][pp], yf[st], acc[pp]);
#pragma unroll
        for (int pp = 0; pp < 3; ++pp) { SGB_MFMA(1); SGB_DSR(2); }
        SB();
      }
      WACC(27, lwait_from<false>(hv, HP, 11));
#pragma unroll
      for (int pp = 0; pp < 3; ++pp) acc[pp] = mfma32(*(const LAS u32x4*)(L + L_FIX + pp * 1024 + lane * 16), u32x4{mz, mz, sz, sz}, acc[pp]);
      lsig(myCP, 11, lane);
      STAMP(16);
#pragma unroll
      for (int pp = 0; pp < 3; ++pp) act_store(acc[pp], pp >= p0 ? pp + 4 : pp);
#pragma unroll
      for (int n = 20; n < 24; ++n) lda(n);
    }
    SB();
    {
      f32x16 acc[4];
#pragma unroll
      for (int q = 0; q < 4; ++q)
#pragma unroll
        for (int r = 0; r < 16; ++r) acc[q][r] = 0.f;
      LAS char* d0 = L + (cw * 8192 + (4 * h + q4) * 128 + mrow * 32 + (lane & 3) * 8);
      LAS char* da = d0 + ((q4 >> 1) ^ kh) * 64; LAS char* db = d0 + ((q4 >> 1) ^ kh ^ 1) * 64;
      u32x4 wl[2][2], a2[12][2];
      auto lda2 = [&](int n) {
        const int s2 = n >> 1, t2 = n & 1, jj = (s2 / 3 + rotd) & 3, pp = s2 % 3;
        a2[s2][t2] = ld128_sc1(rws, (u32)(lane * 16), WS_ACT + (u32)(((2 * tt + (t2 ^ tb)) * 112 + 28 * cw + 7 * jj + (pp >= jj ? pp + 4 : pp)) * 1024));
      };
      auto ldw = [&](int s, int bi) { const u32 o = wd_unit_off(s >> 2) + (s & 3) * 2048; wl[bi][0] = trd2(da + o, 1024); wl[bi][1] = trd2(db + o, 1024); };
      ldw(0, 0); SB();
#pragma unroll
      for (int s = 0; s < 28; ++s) {
        if (s == 3) WACC(30, publish<7>(L + L_CNT + 12, rws, WS_FA2 + (u32)(T * 256 + jt * 8), tlo, thi, lane));
        if (s == 3 || s == 7 || s == 11 || s == 19) lsig(myCP, s == 19 ? 15 : 12 + s / 4, lane);
        if ((s & 3) == 3 && s < 24) WACC(29, lwait_from<false>(hv, HP, 12 + s / 4));
        if ((s & 3) == 2 && s < 24) hv = lpeek(HP);
        if (s == 27) hv = lpeek(CP);
        if (s == 12) {
          STAMP(17);
          wait_flags<4>(rws, WS_FA2 + (u32)(T * 256 + cw * 32), lane, tlo, thi);
          STAMP(21);
#pragma unroll
          for (int n = 0; n < 12; ++n) lda2(n);
          SB();
        }
        if (s < 8) lda(24 + s);
        if (s >= 13 && s < 19) { lda2(12 + 2 * (s - 13)); lda2(13 + 2 * (s - 13)); }
        if (s + 1 < 28) ldw(s + 1, (s + 1) & 1);
        const u32x4 b0 = s < 16 ? af[s < 16 ? s : 0][0] : a2[s < 16 ? 0 : s - 16][0], b1 = s < 16 ? af[s < 16 ? s : 0][1] : a2[s < 16 ? 0 : s - 16][1];
        acc[0] = mfma32(wl[s & 1][0], b0, acc[0]); acc[2] = mfma32(wl[s & 1][1], b0, acc[2]);
        acc[1] = mfma32(wl[s & 1][0], b1, acc[1]); acc[3] = mfma32(wl[s & 1][1], b1, acc[3]);
        SGB_MFMA(1); SGB_DSR(2); SGB_MFMA(1); SGB_DSR(2); SGB_MFMA(2);
        SB();
      }
      STAMP(22);
      lwait_from<false>(hv, CP, 15);
      int ln2 = lane; asm volatile("" : "+v"(ln2));
#pragma unroll
      for (int i = 1; i < 4; ++i)
#pragma unroll
        for (int q = 0; q < 4; ++q) *(LAS f32x4*)(L + L_RED4 + ((i ^ cw) * 3 + i - 1) * 4096 + q * 1024 + ln2 * 16) = f32x4{acc[i][4 * q], acc[i][4 * q + 1], acc[i][4 * q + 2], acc[i][4 * q + 3]};
      const f32x16 mine = acc[0];
      lsig(L + L_RX + cw, 2, lane);
      lwait<false>(L + L_RX, 2);
      u32 ob[8];
#pragma unroll
      for (int q = 0; q < 4; ++q) {
        const LAS char* rp = L + L_RED4 + cw * 3 * 4096 + q * 1024 + ln2 * 16;
        const f32x4 r0 = *(const LAS f32x4*)rp, r1 = *(const LAS f32x4*)(rp + 4096), r2_ = *(const LAS f32x4*)(rp + 8192);
        const f32x4 gb4 = *(const LAS f32x4*)(L + L_GBV + (32 * kh + 8 * q + 4 * (ln2 >> 5)) * 4);
        float v[4];
#pragma unroll
        for (int e = 0; e < 4; ++e) v[e] = (mine[4 * q + e] + r0[e]) + (r1[e] + r2_[e]);
        ob[2 * q] = pk2(fmaf(gb4[0], v[0], bflo(x1p[2 * q])), fmaf(gb4[1], v[1], bfhi(x1p[2 * q])));
        ob[2 * q + 1] = pk2(fmaf(gb4[2], v[2], bflo(x1p[2 * q + 1])), fmaf(gb4[3], v[3], bfhi(x1p[2 * q + 1])));
      }
#pragma unroll
      for (int m = 0; m < 2; ++m) {
        auto r0 = __builtin_amdgcn_permlane32_swap(ob[4 * m], ob[4 * m + 2], false, false);
        auto r1 = __builtin_amdgcn_permlane32_swap(ob[4 * m + 1], ob[4 * m + 3], false, false);
        const u32 a0 = r0[0], b0 = r0[1], a1 = r1[0], b1 = r1[1];
        __builtin_amdgcn_raw_buffer_store_b128(u32x4{a0, a1, b0, b1}, rout, (u32)((64 * tt + 32 * tb + (ln2 & 31)) * 1024 + (64 * cb + 32 * kh + 16 * m + 8 * (ln2 >> 5)) * 2), 0, 0);
      }
    }
    STAMP(18);
    if (b == 0 && cw == 0) {
      const u64 t0 = __builtin_amdgcn_s_memrealtime();
      for (;;) {
        bool ok = true;
#pragma unroll
        for (int k = 0; k < 4; ++k) { const int f = 64 * k + lane; const u32x2 v = ld64_sc1(rws, (u32)((f >> 3) * 256 + (f & 7) * 8), WS_FO); ok = ok && v[0] == tlo && v[1] == thi; }
        if (__all(ok)) break;
        if (spin_out(t0)) break;
      }
      if (lane == 0) st64_sc1(u32x2{tlo, thi}, rws, 0u, WS_E);
    }
  }
}

__global__ __launch_bounds__(512, 2) void fused_block(Params p) {
  extern __shared__ __attribute__((aligned(16))) char smem_[];
  asm volatile("" :: "s"(p.x), "s"(p.wqkv), "s"(p.wo), "s"(p.wg), "s"(p.wu), "s"(p.wd), "s"(p.c), "s"(p.wmod), "s"(p.out), "s"(p.ws));
  int w = __builtin_amdgcn_readfirstlane(threadIdx.x >> 6);
  u32x4 wr[18]; u32 tlo, thi;
  front(p, (LAS char*)smem_, threadIdx.x, w, blockIdx.x, wr, tlo, thi STAMP_ARG);
  if (w >= 4) {
    int b = blockIdx.x, tid = threadIdx.x; asm volatile("; H role" : "+s"(b), "+s"(w)); asm volatile("; H role" : "+v"(tid));
    role_body<false>(p, (LAS char*)smem_, tid, w, b, wr, tlo, thi STAMP_ARG);
  } else {
    int b = blockIdx.x, tid = threadIdx.x; asm volatile("; C role" : "+s"(b), "+s"(w)); asm volatile("; C role" : "+v"(tid));
    role_body<true>(p, (LAS char*)smem_, tid, w, b, wr, tlo, thi STAMP_ARG);
  }
}

extern "C" void kernel_launch(void* const* d_in, const int* in_sizes, int n_in, void* d_out, int out_size, void* d_ws, size_t ws_size, hipStream_t stream) {
  Params p;
  p.x = d_in[0]; p.wqkv = d_in[1]; p.wo = d_in[2]; p.wg = d_in[4]; p.wu = d_in[5]; p.wd = d_in[6]; p.c = d_in[7]; p.wmod = d_in[8];
  p.out = d_out; p.ws = d_ws;
  (void)hipFuncSetAttribute((const void*)fused_block, hipFuncAttributeMaxDynamicSharedMemorySize, LDS_BYTES);
  fused_block<<<256, 512, LDS_BYTES, stream>>>(p);
}
```

```cpp
#include <hip/hip_runtime.h>
#include <stdint.h>

typedef unsigned short u16;
typedef unsigned u32;
typedef unsigned long long u64;
typedef u32 u32x2 __attribute__((ext_vector_type(2)));
typedef u32 u32x4 __attribute__((ext_vector_type(4)));
typedef short s16x4 __attribute__((ext_vector_type(4)));
typedef short s16x8 __attribute__((ext_vector_type(8)));
typedef __bf16 bf16x2 __attribute__((ext_vector_type(2)));
typedef __bf16 bf16x8 __attribute__((ext_vector_type(8)));
typedef float f32x2 __attribute__((ext_vector_type(2)));
typedef float f32x4 __attribute__((ext_vector_type(4)));
typedef float f32x16 __attribute__((ext_vector_type(16)));
typedef __amdgpu_buffer_rsrc_t rsrc_t;
#define LAS __attribute__((address_space(3)))
#define DI __device__ __forceinline__

#define EXPW 0
#define TOUCH 30
#define THR 4
constexpr unsigned CODE_BYTES = 50000;
#define NTX 1
#define FA1P 16
#define DIAG 0
#define XMAP 2

constexpr int S_ = 2048, D_ = 512, F_ = 1792;
constexpr u32 WS_E = 0, WS_MODG = 393216, WS_FO = 32768, WS_FY = 40960, WS_FA = 49152, WS_FA2 = 53248, WS_DBG = 65536;
constexpr u32 WS_O = 1u << 20, WS_Y = 3u << 20, WS_ST = 5u << 20, WS_ACT = 6u << 20, WS_BYTES = 13u << 20;
constexpr u32 L_AIMG = 0, L_RW = 81920, L_W1S = 12288;
constexpr u32 L_LNP = 73728, L_PRED = L_LNP + 6400, L_RSNB = L_PRED + 1088;
constexpr u32 L_XRES = 65536, L_WO = 73728, L_RED2 = 139264;
constexpr u32 L_P1B = 8192, L_P2B = 6400, L_P2M = 3200;
constexpr u32 L_RED4 = 65536;
constexpr u32 L_MISC = 155648, L_G1 = L_MISC, L_S1 = L_MISC + 2048, L_FIX = L_MISC;
constexpr u32 L_MU1 = L_MISC + 7168 + 576, L_GA = L_MISC + 4096, L_G2C = L_GA + 256, L_GBV = L_G2C + 256, L_GS = L_MISC + 5120;
constexpr u32 L_CP = L_MISC + 7168, L_HP = L_CP + 16, L_RX = L_CP + 32, L_CNT = L_CP + 48;
constexpr u32 LDS_BYTES = 163840;
__host__ __device__ constexpr u32 wd_unit_off(int u) { return u == 0 || u == 4 ? 102400u : (u == 1 || u == 5 ? 0u : (u == 2 || u == 6 ? 32768u : 65536u)); }

template <int N> struct ic { static constexpr int value = N; };
struct Params { const void *x, *wqkv, *wo, *wg, *wu, *wd, *c, *wmod; void *out; void *ws; };

DI rsrc_t mk(const void* p, u32 bytes) { return __builtin_amdgcn_make_buffer_rsrc((void*)p, 0, bytes, 0x00020000); }
DI float bflo(u32 v) { return __builtin_bit_cast(float, v << 16); }
DI float bfhi(u32 v) { return __builtin_bit_cast(float, v & 0xffff0000u); }
DI u32 pk2(float a, float b) { bf16x2 r; r[0] = (__bf16)a; r[1] = (__bf16)b; return __builtin_bit_cast(u32, r); }
DI float rbf(float a) { return bflo(pk2(a, a)); }
DI f32x4 mfma16(u32x4 a, u32x4 b, f32x4 c) { return __builtin_amdgcn_mfma_f32_16x16x32_bf16(__builtin_bit_cast(bf16x8, a), __builtin_bit_cast(bf16x8, b), c, 0, 0, 0); }
DI f32x4 mfma16k16(u32x2 a, u32x2 b, f32x4 c) { return __builtin_amdgcn_mfma_f32_16x16x16bf16_1k(__builtin_bit_cast(s16x4, a), __builtin_bit_cast(s16x4, b), c, 0, 0, 0); }
DI f32x16 mfma32(u32x4 a, u32x4 b, f32x16 c) { return __builtin_amdgcn_mfma_f32_32x32x16_bf16(__builtin_bit_cast(bf16x8, a), __builtin_bit_cast(bf16x8, b), c, 0, 0, 0); }
DI u32x2 trd(LAS char* p) { return __builtin_bit_cast(u32x2, __builtin_amdgcn_ds_read_tr16_b64_v4i16((LAS s16x4*)p)); }
DI u32x4 trd2(LAS char* p, int d) { u32x2 a = trd(p), b = trd(p + d); return u32x4{a[0], a[1], b[0], b[1]}; }
DI void dma16(rsrc_t rs, u32 voff, u32 soff, u32 lds) {
  asm volatile("s_mov_b32 m0, %0\n\ts_nop 0\n\tbuffer_load_dwordx4 %1, %2, %3 offen lds" :: "s"(lds), "v"(voff), "s"(rs), "s"(soff) : "m0", "memory");
}
template <int OFF> DI void ald128(u32x4& d, rsrc_t rs, u32 voff, u32 soff) {
  asm volatile("buffer_load_dwordx4 %0, %1, %2, %3 offen offset:%c4" : "=v"(d) : "v"(voff), "s"(rs), "s"(soff), "i"(OFF) : "memory");
}
DI void ald128_nt(u32x4& d, rsrc_t rs, u32 voff, u32 soff) {
  asm volatile("buffer_load_dwordx4 %0, %1, %2, %3 offen nt" : "=v"(d) : "v"(voff), "s"(rs), "s"(soff) : "memory");
}
template <int OFF> DI void ald128_sc1(u32x4& d, rsrc_t rs, u32 voff, u32 soff) {
  asm volatile("buffer_load_dwordx4 %0, %1, %2, %3 offen offset:%c4 sc1" : "=v"(d) : "v"(voff), "s"(rs), "s"(soff), "i"(OFF) : "memory");
}
DI void ald64_sc1(u32x2& d, rsrc_t rs, u32 voff, u32 soff) {
  asm volatile("buffer_load_dwordx2 %0, %1, %2, %3 offen sc1" : "=v"(d) : "v"(voff), "s"(rs), "s"(soff) : "memory");
}
DI void ald64(u32x2& d, rsrc_t rs, u32 voff, u32 soff) {
  asm volatile("buffer_load_dwordx2 %0, %1, %2, %3 offen" : "=v"(d) : "v"(voff), "s"(rs), "s"(soff) : "memory");
}
DI void warm(u32 lds, rsrc_t rs, u32 voff, u32 soff) {
  asm volatile("s_mov_b32 m0, %0\n\ts_nop 0\n\tbuffer_load_dword %1, %2, %3 offen lds" :: "s"(lds), "v"(voff), "s"(rs), "s"(soff) : "m0", "memory");
}
template <int N> DI void waitv() { asm volatile("s_waitcnt vmcnt(%c0)" :: "i"(N) : "memory"); }
template <int N> DI void waitv4(u32x4& a, u32x4& b, u32x4& c, u32x4& d) { asm volatile("s_waitcnt vmcnt(%c4)" : "+v"(a), "+v"(b), "+v"(c), "+v"(d) : "i"(N) : "memory"); }
template <int N> DI void waitv1(u32x4& a) { asm volatile("s_waitcnt vmcnt(%c1)" : "+v"(a) : "i"(N) : "memory"); }
DI void bar() { asm volatile("s_waitcnt lgkmcnt(0)\n\ts_barrier" ::: "memory"); }
DI u32x4 ld128_sc1(rsrc_t rs, u32 voff, u32 soff) { return __builtin_amdgcn_raw_buffer_load_b128(rs, voff, soff, 16); }
DI u32x2 ld64_sc1(rsrc_t rs, u32 voff, u32 soff) { return __builtin_amdgcn_raw_buffer_load_b64(rs, voff, soff, 16); }
DI void st128_sc1(u32x4 v, rsrc_t rs, u32 voff, u32 soff) { __builtin_amdgcn_raw_buffer_store_b128(v, rs, voff, soff, 16); }
DI void st64_sc1(u32x2 v, rsrc_t rs, u32 voff, u32 soff) { __builtin_amdgcn_raw_buffer_store_b64(v, rs, voff, soff, 16); }
DI float f_(u32 v) { return __builtin_bit_cast(float, v); }
DI u32 u_(float v) { return __builtin_bit_cast(u32, v); }
DI float swap16_add(float a, float b) { auto r = __builtin_amdgcn_permlane16_swap(u_(a), u_(b), false, false); u32 r0 = r[0], r1 = r[1]; return f_(r0) + f_(r1); }
DI float swap32_add(float a, float b) { auto r = __builtin_amdgcn_permlane32_swap(u_(a), u_(b), false, false); u32 r0 = r[0], r1 = r[1]; return f_(r0) + f_(r1); }
DI float swap16_max(float a) { auto r = __builtin_amdgcn_permlane16_swap(u_(a), u_(a), false, false); u32 r0 = r[0], r1 = r[1]; return fmaxf(f_(r0), f_(r1)); }
DI float swap32_max(float a) { auto r = __builtin_amdgcn_permlane32_swap(u_(a), u_(a), false, false); u32 r0 = r[0], r1 = r[1]; return fmaxf(f_(r0), f_(r1)); }
template <int CTRL> DI float dpp_add(float a) { return a + f_(__builtin_amdgcn_update_dpp(0, u_(a), CTRL, 0xf, 0xf, true)); }
DI float dot2(u32 a, u32 b, float c) { return __builtin_amdgcn_fdot2_f32_bf16(__builtin_bit_cast(bf16x2, a), __builtin_bit_cast(bf16x2, b), c, false); }

constexpr u64 SPIN_TICKS = 20000;
DI bool spin_out(u64 t0) { __builtin_amdgcn_s_sleep(1); return (__builtin_amdgcn_s_memrealtime() - t0) > SPIN_TICKS; }

#define SB() __builtin_amdgcn_sched_barrier(0)
#define SGB_MFMA(n) __builtin_amdgcn_sched_group_barrier(0x8, n, 0)
#define SGB_DSR(n) __builtin_amdgcn_sched_group_barrier(0x100, n, 0)
#define SGB_DSW(n) __builtin_amdgcn_sched_group_barrier(0x200, n, 0)
#define STAMP(i) do {} while (0)
#define WACC(i, code) do { code; } while (0)

DI void lsig(LAS char* byte, u32 v, int lane) { asm volatile("" ::: "memory"); if (lane == 0) *(volatile LAS unsigned char*)byte = (unsigned char)v; asm volatile("" ::: "memory"); }
DI u32 lpeek(LAS char* word) { return *(volatile LAS u32*)word; }
DI bool lge(u32 v, u32 want) { return (((__builtin_amdgcn_readfirstlane(v) | 0x80808080u) - want * 0x01010101u) & 0x80808080u) == 0x80808080u; }
template <bool SLEEP> DI void lwait_from(u32 seen, LAS char* word, u32 want) {
  if (!lge(seen, want)) {
    for (u32 sp = 0; sp < (1u << 18); ++sp) {
      if (SLEEP) __builtin_amdgcn_s_sleep(1);
      if (lge(lpeek(word), want)) break;
    }
  }
  asm volatile("" ::: "memory");
}
template <bool SLEEP> DI void lwait(LAS char* word, u32 want) { lwait_from<SLEEP>(lpeek(word), word, want); }
template <int YOUNGER> DI void pubbar(rsrc_t rws, u32 flag_off, u32 tlo, u32 thi, int tid) {
  waitv<YOUNGER>(); bar();
  if (tid == 0) st64_sc1(u32x2{tlo, thi}, rws, 0u, flag_off);
}
template <int YOUNGER> DI void publish(LAS char* cnt, rsrc_t rws, u32 flag_off, u32 tlo, u32 thi, int lane) {
  waitv<YOUNGER>();
  if (lane == 0) {
    const u32 old = __hip_atomic_fetch_add((LAS u32*)cnt, 1u, __ATOMIC_RELAXED, __HIP_MEMORY_SCOPE_WORKGROUP);
    if (old == 3u) st64_sc1(u32x2{tlo, thi}, rws, 0u, flag_off);
  }
  asm volatile("" ::: "memory");
}
template <int NF> DI u32x2 peek_flags(rsrc_t rws, u32 off, int lane) { return ld64_sc1(rws, (u32)(lane & (NF - 1)) * 8u, off); }
template <int NF> DI void wait_flags(rsrc_t rws, u32 off, int lane, u32 tlo, u32 thi) {
  const u32 vo = (u32)(lane & (NF - 1)) * 8u;
  const u64 t0 = __builtin_amdgcn_s_memrealtime();
  for (;;) {
    u32x2 f = ld64_sc1(rws, vo, off);
    if (__all(f[0] == tlo && f[1] == thi)) break;
    if (spin_out(t0)) break;
  }
  __builtin_amdgcn_fence(__ATOMIC_ACQUIRE, "wavefront");
}
DI bool gran_ok(u32x4 g, u32 glo, u32 ghi) { return (g[0] & 0xffu) == glo && g[1] == ghi && (g[2] & 0xffu) == glo && g[3] == ghi; }

#define STAMP_PARAM
#define STAMP_ARG
template <int NF> DI void wait_flags_from(u32x2 seen, rsrc_t rws, u32 off, int lane, u32 tlo, u32 thi) {
  if (!__all(seen[0] == tlo && seen[1] == thi)) wait_flags<NF>(rws, off, lane, tlo, thi);
  __builtin_amdgcn_fence(__ATOMIC_ACQUIRE, "wavefront");
}
struct Gath { u32x4 q[4]; };
DI void gath_load(Gath& G, rsrc_t rws, u32 voff, u32 soff) {
#pragma unroll
  for (int k = 0; k < 4; ++k) G.q[k] = ld128_sc1(rws, voff, WS_MODG + soff + (u32)k * 24576u);
}
DI bool gath_ok(const Gath& G, u32 glo, u32 ghi) { return gran_ok(G.q[0], glo, ghi) && gran_ok(G.q[1], glo, ghi) && gran_ok(G.q[2], glo, ghi) && gran_ok(G.q[3], glo, ghi); }
DI u32 gath_val(const Gath& G) {
  const float a = (f_(G.q[0][0] & 0xffffff00u) + f_(G.q[1][0] & 0xffffff00u)) + (f_(G.q[2][0] & 0xffffff00u) + f_(G.q[3][0] & 0xffffff00u));
  const float b = (f_(G.q[0][2] & 0xffffff00u) + f_(G.q[1][2] & 0xffffff00u)) + (f_(G.q[2][2] & 0xffffff00u) + f_(G.q[3][2] & 0xffffff00u));
  return pk2(a, b);
}
DI void front(const Params& p, LAS char* L, const int tid, const int w, const int b, u32x4 (&wr)[18], u32& tlo, u32& thi STAMP_PARAM) {
  const bool isC = w < 4;
  f32x2 xh[10][4];
  const u32 Lb = (u32)(size_t)L;
  const int lane = tid & 63, cw = w & 3;
  const int xcd = b & 7, idx = b >> 3;
  const int tt = 8 * (xcd >> 1) + (idx >> 2), hgl = idx & 3, hg = 4 * (xcd & 1) + hgl;
  const int jt = 8 * (xcd & 1) + 4 * (tt & 1) + hgl;
  const int T = tt >> 1, cb = hg;
  STAMP(0);
  if (tid < 32) *(LAS u32*)(L + L_CP + tid * 4) = 0u;
  const rsrc_t rx = mk(p.x, S_ * D_ * 2), rw1 = mk(p.wqkv, D_ * 3 * D_ * 2), rwo = mk(p.wo, D_ * D_ * 2);
  const rsrc_t rwg = mk(p.wg, D_ * F_ * 2), rwu = mk(p.wu, D_ * F_ * 2), rwd = mk(p.wd, F_ * D_ * 2);
  const rsrc_t rc = mk(p.c, D_ * 2), rwm = mk(p.wmod, D_ * 6 * D_ * 2), rout = mk(p.out, S_ * D_ * 2), rws = mk(p.ws, WS_BYTES);
  const int g = lane >> 4, i16 = lane & 15, h = lane >> 5, tok = lane & 31, t4 = tid & 255;

  const int nrot = 2 * hgl, rot = 2 * (tt & 7);
  const u32 w1v = (u32)((lane >> 3) * 3072 + (((((lane >> 1) & 3) ^ ((lane >> 4) & 1)) ^ ((cw & 1) << 1)) * 32) + (lane & 1) * 16);
  auto w1slot = [](int n) { return n < 6 ? L_RW + n * L_W1S : (n < 12 ? (n - 6) * L_W1S : L_RW + (n - 12) * L_W1S); };
  {
    u64 pc; asm volatile("s_getpc_b64 %0" : "=s"(pc));
    const rsrc_t rcode = mk((const void*)(pc & ~(u64)255), CODE_BYTES);
    warm(Lb + L_CP + 256, rcode, (u32)((idx * 8 + w) * 2 + (lane & 1)) * 128u, 0u);
  }
  u32x2 ev; ald64_sc1(ev, rws, 0u, WS_E);
  const bool prod = isC && idx < 8, prod2 = isC && idx >= 8 && idx < 24;
  const int mq = idx & 3, mblk = idx < 8 ? xcd * 2 + (idx >> 2) : 16 + xcd * 4 + ((idx - 8) >> 2);
  auto mod_issue = [&](u32x4 (&wm)[4], u32x2& cv) {
    const int r0 = 128 * mq + 32 * cw;
#pragma unroll
    for (int i = 0; i < 4; ++i) ald128_nt(wm[i], rwm, (u32)((lane >> 3) * 4 * 6144 + (lane & 7) * 16), (u32)((r0 + i) * 6144 + mblk * 128));
    ald64(cv, rc, (u32)((lane >> 3) * 8), (u32)(r0 * 2));
  };
  auto dma_w1 = [&](int i) {
    const int n = (i < 9 ? 6 : 0) + 2 * ((i % 9) / 3) + (w >> 2), pp = i % 3;
    dma16(rw1, w1v, (u32)((32 * ((n + rot) & 15) + 8 * cw) * 3072 + pp * 1024 + hg * 128), Lb + (u32)((i < 9 ? (n - 6) * L_W1S : L_RW + n * L_W1S) + pp * 4096 + cw * 1024));
  };
  auto mod_finish = [&](u32 pred, const u32x4 (&wm)[4], const u32x2& cv, u32 tlo, u32 thi) {
    float a8[8];
#pragma unroll
    for (int e = 0; e < 8; ++e) a8[e] = 0.f;
#pragma unroll
    for (int i = 0; i < 4; ++i) {
      const float cf = (i & 1) ? bfhi(cv[i >> 1]) : bflo(cv[i >> 1]);
      const float sv = rbf(cf * __builtin_amdgcn_rcpf(1.f + __expf(-cf)));
#pragma unroll
      for (int e = 0; e < 4; ++e) { a8[2 * e] = fmaf(sv, bflo(wm[i][e]), a8[2 * e]); a8[2 * e + 1] = fmaf(sv, bfhi(wm[i][e]), a8[2 * e + 1]); }
    }
#pragma unroll
    for (int e = 0; e < 8; ++e) a8[e] = dpp_add<0x128>(a8[e]);
    float b4[4], b2[2];
#pragma unroll
    for (int e = 0; e < 4; ++e) b4[e] = swap16_add(a8[e], a8[e + 4]);
#pragma unroll
    for (int e = 0; e < 2; ++e) b2[e] = swap32_add(b4[e], b4[e + 2]);
    if (i16 < 8) *(LAS f32x2*)(L + pred + cw * 256 + (i16 * 8 + (g & 1) * 4 + (g >> 1) * 2) * 4) = f32x2{b2[0], b2[1]};
    asm volatile("s_waitcnt lgkmcnt(0)" ::: "memory");
    if (lane == 0) *(volatile LAS u32x2*)(L + pred + 1024 + cw * 8) = u32x2{tlo, thi};
    if (cw == 0) {
      const u64 t0 = __builtin_amdgcn_s_memrealtime();
      for (;;) {
        const u32x2 tg = *(volatile LAS u32x2*)(L + pred + 1024 + (lane & 3) * 8);
        if (__all(tg[0] == tlo && tg[1] == thi)) break;
        if ((__builtin_amdgcn_s_memrealtime() - t0) > SPIN_TICKS) break;
      }
      asm volatile("" ::: "memory");
      LAS float* s = (LAS float*)(L + pred) + lane;
      const float v = (s[0] + s[64]) + (s[128] + s[192]);
      st64_sc1(u32x2{((u_(v) + 0x80u) & 0xffffff00u) | (tlo & 0xffu), (tlo >> 8) | (thi << 24)}, rws, (u32)(lane * 8), WS_MODG + (u32)(mq * 24576 + mblk * 512));
    }
  };
  u32x2 cv; u32x4 wm[4];
  if (prod) mod_issue(wm, cv);
  u32x4 xr[10];
#pragma unroll
  for (int n = 0; n < 10; ++n) {
    int r8 = n + nrot; r8 = r8 >= 10 ? r8 - 10 : r8;
    int row = 64 * tt - 16 + 8 * r8 + (lane >> 3); row = row < 0 ? 0 : row;
    ald128_nt(xr[n], rx, (u32)(row * 1024 + (lane & 7) * 16), (u32)(w * 128));
  }
  {
    const rsrc_t rt = w == 0 ? rwo : w == 1 ? rwg : w == 2 ? rwu : w == 3 ? rwd : w == 4 ? rout : rws;
    warm(Lb + L_CP + 256, rt, (u32)lane << 20, 0u);
  }
  dma_w1(0); dma_w1(1); dma_w1(2);
  constexpr int Y0 = 3 + ((TOUCH & 4) ? 1 : 0);
  STAMP(1);
  if (prod) {
    asm volatile("s_waitcnt vmcnt(%c6)" : "+v"(wm[0]), "+v"(wm[1]), "+v"(wm[2]), "+v"(wm[3]), "+v"(cv), "+v"(ev) : "i"(10 + Y0) : "memory");
    const u64 e1 = (((u64)__builtin_amdgcn_readfirstlane(ev[1]) << 32) | (u64)__builtin_amdgcn_readfirstlane(ev[0])) + 1ull;
    mod_finish(L_PRED, wm, cv, (u32)e1, (u32)(e1 >> 32));
  }
  STAMP(2);
  waitv<Y0>();
#pragma unroll
  for (int n = 0; n < 10; n += 2) asm volatile("" : "+v"(xr[n]), "+v"(xr[n + 1]));
  asm volatile("" : "+v"(ev));
  {
    const u64 e1 = (((u64)__builtin_amdgcn_readfirstlane(ev[1]) << 32) | (u64)__builtin_amdgcn_readfirstlane(ev[0])) + 1ull;
    tlo = (u32)e1; thi = (u32)(e1 >> 32);
  }
  if (prod2) mod_issue(wm, cv);
  STAMP(3);
  {
    const int c8 = lane & 7, r8l = lane >> 3;
    float pv[10];
#pragma unroll
    for (int n = 0; n < 10; ++n) {
#pragma unroll
      for (int e = 0; e < 4; ++e) xh[n][e] = f32x2{bflo(xr[n][e]), bfhi(xr[n][e])};
      const f32x2 a1 = (xh[n][0] + xh[n][1]) + (xh[n][2] + xh[n][3]);
      const f32x2 a2 = __builtin_elementwise_fma(xh[n][0], xh[n][0], xh[n][1] * xh[n][1]) + __builtin_elementwise_fma(xh[n][2], xh[n][2], xh[n][3] * xh[n][3]);
      const float s1 = a1[0] + a1[1], s2 = a2[0] + a2[1];
      const float keep = (c8 & 1) ? s2 : s1, give = (c8 & 1) ? s1 : s2;
      float v = keep + f_(__builtin_amdgcn_update_dpp(0, u_(give), 0xB1, 0xf, 0xf, true));
      v = dpp_add<0x4E>(v);
      pv[n] = v + f_(__builtin_amdgcn_update_dpp(0, u_(v), 0x104, 0xf, 0xf, true));
      if (n < 6) { asm volatile("" : "+v"(pv[n])); dma_w1(3 + n); SB(); }
    }
    if (c8 < 2) {
#pragma unroll
      for (int n = 0; n < 10; ++n) *(LAS float*)(L + L_LNP + (n * 8 + r8l) * 80 + (w * 2 + c8) * 4) = pv[n];
    }
    bar();
    f32x4 qa[2][4];
#pragma unroll
    for (int k = 0; k < 2; ++k)
#pragma unroll
      for (int e = 0; e < 4; ++e) qa[k][e] = *(const LAS f32x4*)(L + L_LNP + (k ? 64 + i16 : lane) * 80 + e * 16);
    dma_w1(9); dma_w1(10);
    float mus[2];
#pragma unroll
    for (int k = 0; k < 2; ++k) {
      const f32x4 a = (qa[k][0] + qa[k][1]) + (qa[k][2] + qa[k][3]);
      const float mu = (a[0] + a[2]) * (1.f / 512.f);
      const float var = fmaxf((a[1] + a[3]) * (1.f / 512.f) - mu * mu, 0.f);
      const float rs = __builtin_amdgcn_rsqf(var + 1e-5f);
      *(LAS f32x2*)(L + L_RSNB + (k ? 64 + i16 : lane) * 8) = f32x2{rs, -mu * rs};
      mus[k] = mu;
    }
    asm volatile("" ::: "memory");
    f32x2 rn[10];
#pragma unroll
    for (int n = 0; n < 10; ++n) rn[n] = *(const LAS f32x2*)(L + L_RSNB + (n * 8 + r8l) * 8);
    if (w == 0) {
#pragma unroll
      for (int k = 0; k < 2; ++k) { int r = (k ? 64 + i16 : lane) + 8 * nrot; r = r >= 80 ? r - 80 : r; ((LAS float*)(L + L_MU1 - 64))[r] = mus[k]; }
    }
#pragma unroll
    for (int n = 0; n < 10; ++n) {
#pragma unroll
      for (int e = 0; e < 4; ++e) xh[n][e] = __builtin_elementwise_fma(xh[n][e], f32x2{rn[n][0], rn[n][0]}, f32x2{rn[n][1], rn[n][1]});
      if (n < 7) { asm volatile("" : "+v"(xh[n][0]), "+v"(xh[n][1]), "+v"(xh[n][2]), "+v"(xh[n][3])); dma_w1(11 + n); SB(); }
    }
  }
#pragma unroll
  for (int n = 0; n < 10; ++n) asm volatile("" : "+v"(xh[n][0]), "+v"(xh[n][1]), "+v"(xh[n][2]), "+v"(xh[n][3]));
  STAMP(4);
  const u32 glo = tlo & 0xffu, ghi = (tlo >> 8) | (thi << 24);
  const u32 gvo = (u32)(w * 512 + tok * 16 + h * 4096);
  Gath g1_;
#pragma unroll
  for (int k = 0; k < 4; ++k) ald128_sc1<0>(g1_.q[k], rws, gvo, WS_MODG + (u32)k * 24576u);
  if (prod2) {
    asm volatile("s_waitcnt vmcnt(19)" : "+v"(wm[0]), "+v"(wm[1]), "+v"(wm[2]), "+v"(wm[3]), "+v"(cv) :: "memory");
    mod_finish(L_PRED, wm, cv, tlo, thi);
  }
  waitv<13>();
  bar();
  if (isC) {
    LAS char* w1rd = L + ((8 * g + (i16 >> 2)) * 128 + ((cw ^ (((i16 >> 3) & 1) | ((g & 1) << 1))) * 32) + (lane & 3) * 8);
    __builtin_amdgcn_s_setprio(2);
#pragma unroll
    for (int n = 0; n < 6; ++n)
#pragma unroll
      for (int pp = 0; pp < 3; ++pp) wr[3 * n + pp] = trd2(w1rd + n * L_W1S + pp * 4096, 512);
  }
  STAMP(5);
  waitv4<0>(g1_.q[0], g1_.q[1], g1_.q[2], g1_.q[3]);
  if (!__all(gath_ok(g1_, glo, ghi))) {
    const u64 t0 = __builtin_amdgcn_s_memrealtime();
    for (;;) {
      gath_load(g1_, rws, gvo, 0u);
      if (__all(gath_ok(g1_, glo, ghi))) break;
      if (spin_out(t0)) break;
    }
  }
  {
    const u32 v2 = gath_val(g1_);
    const float a = bflo(v2), c = bfhi(v2);
    *(LAS f32x2*)(L + (h ? L_G1 : L_S1) + (w * 32 + tok) * 8) = h ? f32x2{rbf(1.f + a), rbf(1.f + c)} : f32x2{a, c};
  }
  STAMP(6);
  bar();
  if (w < 3) warm(Lb + L_CP + 256, w == 0 ? rwg : w == 1 ? rwu : rwd, (u32)(b + 256 * lane) * 128u, 0u);
  {
    const int c8 = lane & 7, r8l = lane >> 3;
    const LAS f32x4* gp = (const LAS f32x4*)(L + L_G1 + (w * 64 + c8 * 8) * 4);
    const LAS f32x4* sp = (const LAS f32x4*)(L + L_S1 + (w * 64 + c8 * 8) * 4);
    const f32x4 g0 = gp[0], g1 = gp[1], s0 = sp[0], s1 = sp[1];
    LAS char* ab = L + L_AIMG + (2 * w + (c8 >> 2)) * 1024 + (c8 & 3) * 256 + ((r8l ^ c8) * 16);
#pragma unroll
    for (int n = 0; n < 10; ++n) {
      const f32x2 h0 = __builtin_elementwise_fma(xh[n][0], f32x2{g0[0], g0[1]}, f32x2{s0[0], s0[1]}), h1 = __builtin_elementwise_fma(xh[n][1], f32x2{g0[2], g0[3]}, f32x2{s0[2], s0[3]});
      const f32x2 h2 = __builtin_elementwise_fma(xh[n][2], f32x2{g1[0], g1[1]}, f32x2{s1[0], s1[1]}), h3 = __builtin_elementwise_fma(xh[n][3], f32x2{g1[2], g1[3]}, f32x2{s1[2], s1[3]});
      const u32x4 o = u32x4{pk2(h0[0], h0[1]), pk2(h1[0], h1[1]), pk2(h2[0], h2[1]), pk2(h3[0], h3[1])};
      int r8 = n + nrot; r8 = r8 >= 10 ? r8 - 10 : r8;
      *(LAS u32x4*)(ab + (r8 >> 1) * 16384 + (r8 & 1) * 128) = o;
    }
  }
  bar();
  STAMP(7);
}

template <bool isC> DI void role_body(const Params& p, LAS char* L, const int tid, const int w, const int b, u32x4 (&wr)[18], const u32 tlo, const u32 thi STAMP_PARAM) {
  const u32 Lb = (u32)(size_t)L;
  const int lane = tid & 63, cw = w & 3;
  const int xcd = b & 7, idx = b >> 3;
  const int tt = 8 * (xcd >> 1) + (idx >> 2), hgl = idx & 3, hg = 4 * (xcd & 1) + hgl;
  const int jt = 8 * (xcd & 1) + 4 * (tt & 1) + hgl;
  const int T = tt >> 1, cb = hg;
  const rsrc_t rx = mk(p.x, S_ * D_ * 2), rw1 = mk(p.wqkv, D_ * 3 * D_ * 2), rwo = mk(p.wo, D_ * D_ * 2);
  const rsrc_t rwg = mk(p.wg, D_ * F_ * 2), rwu = mk(p.wu, D_ * F_ * 2), rwd = mk(p.wd, F_ * D_ * 2);
  const rsrc_t rc = mk(p.c, D_ * 2), rwm = mk(p.wmod, D_ * 6 * D_ * 2), rout = mk(p.out, S_ * D_ * 2), rws = mk(p.ws, WS_BYTES);
  const int g = lane >> 4, i16 = lane & 15, h = lane >> 5, tok = lane & 31, t4 = tid & 255;

  const int nrot = 2 * hgl, rot = 2 * (tt & 7);
  const u32 w1v = (u32)((lane >> 3) * 3072 + (((((lane >> 1) & 3) ^ ((lane >> 4) & 1)) ^ ((cw & 1) << 1)) * 32) + (lane & 1) * 16);
  auto w1slot = [](int n) { return n < 6 ? L_RW + n * L_W1S : (n < 12 ? (n - 6) * L_W1S : L_RW + (n - 12) * L_W1S); };
  LAS char* w1rd = L + ((8 * g + (i16 >> 2)) * 128 + ((cw ^ (((i16 >> 3) & 1) | ((g & 1) << 1))) * 32) + (lane & 3) * 8);
  const u32 glo = tlo & 0xffu, ghi = (tlo >> 8) | (thi << 24);
  const int p0 = jt & 3, rotg = (4 * (T & 3) + 2 * (jt & 7)) & 15, rotd = (tt + hgl) & 3;
  const u32 wov = (u32)((lane >> 3) * 1024 + ((((lane >> 2) & 1) ^ ((lane >> 4) & 1)) * 64) + (lane & 3) * 16);
  const int mrow = (lane >> 4) & 1, q4 = i16 >> 2;
  const u32 p1base = (u32)(mrow * 4096 + (4 * h + q4) * 128 + (lane & 3) * 8), p1x = (u32)((q4 >> 1) | (mrow << 1));
  const u32 p2base = (u32)(mrow * L_P2M + (4 * h + q4) * 96 + (lane & 3) * 8);

  if constexpr (!isC) {
    LAS char* const CP = L + L_CP; LAS char* const HP = L + L_HP; LAS char* const myHP = HP + cw;
    Gath gs_, gc_, gg_;
#pragma unroll
    for (int k = 0; k < 4; ++k) {
      ald128_sc1<0>(gs_.q[k], rws, (u32)(t4 * 16), WS_MODG + 3 * 4096u + (u32)k * 24576u); ald128_sc1<0>(gc_.q[k], rws, (u32)(t4 * 16), WS_MODG + 4 * 4096u + (u32)k * 24576u);
      ald128_sc1<0>(gg_.q[k], rws, (u32)(tok * 16 + h * 3 * 4096), WS_MODG + 2 * 4096u + (u32)cb * 512u + (u32)k * 24576u);
    }
    bar();
#pragma unroll 1
    for (int n = 0; n < 4; ++n)
#pragma unroll
      for (int pp = 0; pp < 3; ++pp) dma16(rw1, w1v, (u32)((32 * ((12 + n + rot) & 15) + 8 * cw) * 3072 + pp * 1024 + hg * 128), Lb + (u32)(L_RW + n * L_W1S + pp * 4096 + cw * 1024));
    waitv<0>(); bar();
    STAMP(8);
    warm(Lb + L_CP + 256, rwo, (u32)((64 * (tt & 7) + 16 * cw + (lane & 15)) * 1024), (u32)(cb * 128));
    STAMP(9);
    bar();
#pragma unroll 1
    for (int q = 0; q < 16; ++q) { const int qq = (q + rot) & 15; dma16(rwo, wov, (u32)((128 * cw + 8 * qq) * 1024 + cb * 128), Lb + (u32)(L_WO + (16 * cw + qq) * 1024)); }
#pragma unroll
    for (int q = 0; q < 2; ++q) dma16(rx, (u32)((lane >> 3) * 1024 + (lane & 7) * 16), (u32)((64 * tt + 16 * cw + 8 * q) * 1024 + cb * 128), Lb + (u32)(L_XRES + (2 * cw + q) * 1024));
    const u32 p1v = (u32)((lane >> 3) * 3584 + ((((lane >> 1) & 3) ^ ((lane >> 4) & 1)) * 32) + (lane & 1) * 16);
    auto dma_p1 = [&](int k) {
      const u32 so = (u32)((32 * ((k + rotg) & 15) + 8 * cw) * 3584 + jt * 224 + p0 * 32);
      dma16(rwg, p1v, so, Lb + (u32)(k * L_P1B + cw * 1024));
      dma16(rwu, p1v ^ 64u, so, Lb + (u32)(k * L_P1B + 4096 + cw * 1024));
    };
    dma_p1(0); dma_p1(1);
    waitv<4>(); STAMP(10); bar();
#pragma unroll 1
    for (int k = 2; k < 8; ++k) dma_p1(k);
    {
      asm volatile("" : "+v"(gs_.q[0]), "+v"(gs_.q[1]), "+v"(gs_.q[2]), "+v"(gs_.q[3]), "+v"(gc_.q[0]), "+v"(gc_.q[1]), "+v"(gc_.q[2]), "+v"(gc_.q[3]));
      asm volatile("" : "+v"(gg_.q[0]), "+v"(gg_.q[1]), "+v"(gg_.q[2]), "+v"(gg_.q[3]));
      if (!__all(gath_ok(gs_, glo, ghi) && gath_ok(gc_, glo, ghi) && gath_ok(gg_, glo, ghi))) {
        const u64 t0 = __builtin_amdgcn_s_memrealtime();
        for (;;) {
          gath_load(gs_, rws, (u32)(t4 * 16), 3 * 4096u); gath_load(gc_, rws, (u32)(t4 * 16), 4 * 4096u);
          gath_load(gg_, rws, (u32)(tok * 16 + h * 3 * 4096), 2 * 4096u + (u32)cb * 512u);
          if (__all(gath_ok(gs_, glo, ghi) && gath_ok(gc_, glo, ghi) && gath_ok(gg_, glo, ghi))) break;
          if (spin_out(t0)) break;
        }
      }
      const u32 sb2 = gath_val(gs_), cb2 = gath_val(gc_), gg2 = gath_val(gg_);
      const float g2a = rbf(1.f + bflo(cb2)), g2b = rbf(1.f + bfhi(cb2));
      const int kk = (2 * t4) & 31;
      LAS char* gd = L + L_GS + (t4 >> 4) * 128 + ((kk >> 2) & 3) * 32 + ((((kk >> 4) << 2) | (kk & 3)) * 2);
      *(LAS u32*)gd = pk2(g2a, g2b);
      *(LAS u32*)(gd + 16) = sb2;
      if ((t4 >> 5) == cb) *(LAS f32x2*)(L + L_G2C + tok * 8) = f32x2{g2a, g2b};
      if (cw == 0) *(LAS f32x2*)(L + (h ? L_GBV : L_GA) + tok * 8) = f32x2{bflo(gg2), bfhi(gg2)};
    }
    waitv<0>(); STAMP(11); bar();
#pragma unroll 1
    for (int k = 8; k < 16; ++k) dma_p1(k);
    bar();
    u32 p2v[3];
#pragma unroll
    for (int i = 0; i < 3; ++i) { const int n = 64 * i + lane, row = n / 6, c = n - row * 6; p2v[i] = (u32)(row * 3584 + c * 16 + ((c >> 1) >= p0 ? 128 : 0)); }
    const rsrc_t rgu = (cw & 1) ? rwu : rwg;
    auto dma_p2 = [&](int k0, int k1) {
#pragma unroll 1
      for (int k = k0; k < k1; ++k) {
        const int pos = 2 * k + (cw >> 1);
#pragma unroll
        for (int i = 0; i < 3; ++i) dma16(rgu, p2v[i], (u32)(((pos + rotg) & 15) * 32 * 3584 + jt * 224), Lb + (u32)(pos * L_P2B + (cw & 1) * L_P2M + i * 1024));
      }
    };
    auto dma_wd = [&](int u) {
#pragma unroll
      for (int q = 0; q < 8; ++q) {
        const int n = 8 * (u - 4) + q;
        const int jj = ((u < 4 ? u : n / 6) + rotd) & 3, r6 = n % 6;
        const int row = u < 4 ? 112 * jj + 16 * jj + 8 * q : 112 * jj + 8 * r6 + ((r6 >> 1) >= jj ? 64 : 0);
        dma16(rwd, wov, (u32)((448 * cw + row) * 1024 + cb * 128), Lb + (u32)(wd_unit_off(u) + cw * 8192 + q * 1024));
      }
    };
    f32x4 ua, ub;
    auto ut_blocks = [&](LAS char* ba, LAS char* bb, int bstride, int tstride, int k0, int k1) {
      u32x4 fa[8], fb[8], gv[8];
#pragma unroll
      for (int k = k0; k < k1; ++k) {
        fa[k - k0] = trd2(ba + k * bstride, tstride); fb[k - k0] = trd2(bb + k * bstride, tstride);
        gv[k - k0] = *(const LAS u32x4*)(L + L_GS + ((k + rotg) & 15) * 128 + g * 32 + (lane & 1) * 16);
      }
      SB();
#pragma unroll
      for (int k = k0; k < k1; ++k) { ua = mfma16(gv[k - k0], fa[k - k0], ua); ub = mfma16(gv[k - k0], fb[k - k0], ub); }
      asm volatile("" : "+v"(ua), "+v"(ub));
    };
    auto put_fix = [&](const f32x4& v, int slot, int m) {
      const u32 hi = pk2(v[0], v[1]), lo = pk2(v[0] - bflo(hi), v[1] - bfhi(hi));
      const u32 u1 = hi & 0xffffu, t1 = hi >> 16, u2 = lo & 0xffffu, t2 = lo >> 16;
      const u32x4 o = u32x4{u1 | (u1 << 16), u2 | (u2 << 16), t1 | (t1 << 16), t2 | (t2 << 16)};
      if (lane < 16) { LAS char* d = L + L_FIX + slot * 1024 + (16 * m + lane) * 16; *(LAS u32x4*)d = o; *(LAS u32x4*)(d + 512) = o; }
    };
    LAS char* u1a = L + ((4 * g + q4) * 128 + (lane & 3) * 8 + ((cw ^ (q4 >> 1)) * 32));
    LAS char* u1b = L + (4096 + (4 * g + q4) * 128 + (lane & 3) * 8 + ((cw ^ (q4 >> 1) ^ 2) * 32));
    ua = f32x4{0, 0, 0, 0}; ub = f32x4{0, 0, 0, 0};
    ut_blocks(u1a, u1b, L_P1B, 2048, 0, 8);
    STAMP(12); bar();
    dma_p2(0, 2);
    waitv<6>(); STAMP(13); bar();
    dma_p2(2, 5);
    ut_blocks(u1a, u1b, L_P1B, 2048, 8, 12);
    STAMP(14); bar();
    dma_p2(5, 7);
    ut_blocks(u1a, u1b, L_P1B, 2048, 12, 16);
    put_fix(ua, cw, 0); put_fix(ub, cw, 1);
    LAS char* u2a = L + ((cw & 1) * L_P2M + (4 * g + q4) * 96 + (lane & 3) * 8 + (cw >> 1) * 32);
    LAS char* u2b = L + ((cw & 1) * L_P2M + (4 * g + q4) * 96 + (lane & 3) * 8 + 64);
    ua = f32x4{0, 0, 0, 0}; ub = f32x4{0, 0, 0, 0};
    waitv<15>(); STAMP(15); bar();
    dma_p2(7, 8);
    dma_wd(0);
    bar();
    ut_blocks(u2a, u2b, L_P2B, 1536, 0, 4);
    waitv<17>(); STAMP(16); bar();
    ut_blocks(u2a, u2b, L_P2B, 1536, 4, 10);
    waitv<8>(); bar();
    dma_wd(1);
    ut_blocks(u2a, u2b, L_P2B, 1536, 10, 13);
    bar();
    dma_wd(2);
    ut_blocks(u2a, u2b, L_P2B, 1536, 13, 16);
    put_fix(ua, cw >> 1, cw & 1); if (cw < 2) put_fix(ub, 2, cw & 1);
    waitv<16>(); bar();
    dma_wd(3);
    waitv<16>(); STAMP(17); bar();
    dma_wd(4);
    waitv<16>(); bar();
    dma_wd(5);
    waitv<16>(); bar();
    dma_wd(6);
    waitv<16>(); STAMP(18); bar();
    waitv<8>(); bar();
    waitv<0>(); bar();
    bar();
  } else {
    LAS char* const CP = L + L_CP; LAS char* const HP = L + L_HP; LAS char* const myCP = CP + cw;
    u32 hv;
    f32x4 Kt[5], Qt[4], Vn[5];
#pragma unroll
    for (int r = 0; r < 5; ++r) { Kt[r] = f32x4{0, 0, 0, 0}; Vn[r] = f32x4{0, 0, 0, 0}; if (r < 4) Qt[r] = f32x4{0, 0, 0, 0}; }
    {
      u32x4 a[2][5], wl[2][3];
      auto ld = [&](int n, int bi) {
        LAS char* ab = L + L_AIMG + ((n + rot) & 15) * 1024 + g * 256 + ((i16 ^ (((n & 1) << 2) | g)) * 16);
#pragma unroll
        for (int rb = 0; rb < 5; ++rb) a[bi][rb] = *(const LAS u32x4*)(ab + rb * 16384);
        if (n < 6 || n >= 12) {
#pragma unroll
          for (int pp = 0; pp < 3; ++pp) wl[bi][pp] = trd2(w1rd + w1slot(n) + pp * 4096, 512);
        }
      };
      ld(0, 0); SB();
#pragma unroll
      for (int n = 0; n < 16; ++n) {
        if (n == 3) bar();
        if (n == 11) WACC(24, bar());
        if (n < 15) ld(n + 1, (n + 1) & 1);
        const bool rg = n >= 6 && n < 12;
        const u32x4 wq = rg ? wr[rg ? 3 * (n - 6) : 0] : wl[n & 1][0], wk = rg ? wr[rg ? 3 * (n - 6) + 1 : 0] : wl[n & 1][1], wv = rg ? wr[rg ? 3 * (n - 6) + 2 : 0] : wl[n & 1][2];
#pragma unroll
        for (int rb = 0; rb < 5; ++rb) {
          Kt[rb] = mfma16(wk, a[n & 1][rb], Kt[rb]);
          Vn[rb] = mfma16(a[n & 1][rb], wv, Vn[rb]);
          if (rb > 0) Qt[rb - 1] = mfma16(wq, a[n & 1][rb], Qt[rb - 1]);
        }
#pragma unroll
        for (int k = 0; k < 11; ++k) { SGB_MFMA(1); SGB_DSR(1); }
        SGB_MFMA(3);
        SB();
      }
    }
    bar();
    STAMP(8);
    const int tb = cw & 1, kh = cw >> 1;
    {
      u32x2 kp[5], vp[5];
#pragma unroll
      for (int r = 0; r < 5; ++r) { kp[r] = u32x2{pk2(Kt[r][0], Kt[r][1]), pk2(Kt[r][2], Kt[r][3])}; vp[r] = u32x2{pk2(Vn[r][0], Vn[r][1]), pk2(Vn[r][2], Vn[r][3])}; }
      f32x4 m0, m1, m0f;
#pragma unroll
      for (int r = 0; r < 4; ++r) {
        const int d1 = i16 - 4 * g - r, d0 = d1 + 16;
        m0[r] = (d0 <= 14 && !(d0 & 1)) ? 0.f : -1e30f;
        m1[r] = (d1 >= 0 && d1 <= 14 && !(d1 & 1)) ? 0.f : -1e30f;
        m0f[r] = tt == 0 ? -1e30f : m0[r];
      }
      u32x2 op[4];
      const u32x4 ones = u32x4{0x3f803f80u, 0x3f803f80u, 0x3f803f80u, 0x3f803f80u};
#pragma unroll
      for (int qb = 0; qb < 4; ++qb) {
        const u32x2 qp = u32x2{pk2(Qt[qb][0], Qt[qb][1]), pk2(Qt[qb][2], Qt[qb][3])};
        const f32x4 s0 = mfma16k16(kp[qb], qp, qb == 0 ? m0f : m0), s1 = mfma16k16(kp[qb + 1], qp, m1);
        float mx = fmaxf(fmaxf(fmaxf(s0[0], s0[1]), fmaxf(s0[2], s0[3])), fmaxf(fmaxf(s1[0], s1[1]), fmaxf(s1[2], s1[3])));
        mx = swap16_max(mx); mx = swap32_max(mx);
        const float cs = 0.25f * 1.44269504088896f, nm = -mx * cs;
        float e0[4], e1[4];
#pragma unroll
        for (int r = 0; r < 4; ++r) { e0[r] = __builtin_amdgcn_exp2f(fmaf(s0[r], cs, nm)); e1[r] = __builtin_amdgcn_exp2f(fmaf(s1[r], cs, nm)); }
        const u32x4 pf = u32x4{pk2(e0[0], e0[1]), pk2(e0[2], e0[3]), pk2(e1[0], e1[1]), pk2(e1[2], e1[3])};
        const f32x4 z = f32x4{0, 0, 0, 0};
        const f32x4 ot = mfma16(u32x4{vp[qb][0], vp[qb][1], vp[qb + 1][0], vp[qb + 1][1]}, pf, z);
        const f32x4 sm = mfma16(ones, pf, z);
        const float ri = __builtin_amdgcn_rcpf(sm[0]);
        op[qb] = u32x2{pk2(ot[0] * ri, ot[1] * ri), pk2(ot[2] * ri, ot[3] * ri)};
      }
#pragma unroll
      for (int t2 = 0; t2 < 2; ++t2) {
        auto r0 = __builtin_amdgcn_permlane16_swap(op[2 * t2][0], op[2 * t2 + 1][0], false, false);
        auto r1 = __builtin_amdgcn_permlane16_swap(op[2 * t2][1], op[2 * t2 + 1][1], false, false);
        const u32 a0 = r0[0], b0 = r0[1], a1 = r1[0], b1 = r1[1];
        st128_sc1(u32x4{a0, a1, b0, b1}, rws, (u32)(lane * 16), WS_O + (u32)(((2 * tt + t2) * 32 + 4 * hg + cw) * 1024));
      }
    }
    STAMP(9);
    pubbar<0>(rws, WS_FO + (u32)(tt * 256 + hg * 8), tlo, thi, tid);
    STAMP(10);
    wait_flags<8>(rws, WS_FO + (u32)(tt * 256), lane, tlo, thi);
    STAMP(11);
    u32 x1p[8];
    {
      u32x4 of[16];
      const u32 ob = WS_O + (u32)(((2 * tt + tb) * 32 + 16 * kh) * 1024);
#pragma unroll
      for (int s = 0; s < 16; ++s) { of[s] = ld128_sc1(rws, (u32)(lane * 16), ob + (u32)(((s + 4 * hgl) & 15) * 1024)); SB(); }
      SB();
      LAS char* q0 = L + (L_WO + (256 * kh + 8 * h + q4) * 128 + mrow * 32 + (lane & 3) * 8);
      LAS char* qa = q0 + ((q4 >> 1) ^ kh) * 64; LAS char* qb = q0 + ((q4 >> 1) ^ kh ^ 1) * 64;
      f32x16 a0, a1;
#pragma unroll
      for (int r = 0; r < 16; ++r) { a0[r] = 0.f; a1[r] = 0.f; }
      u32x4 wl[2][2];
      wl[0][0] = trd2(qa + hgl * 8192, 512); wl[0][1] = trd2(qb + hgl * 8192, 512); SB();
#pragma unroll
      for (int s = 0; s < 16; ++s) {
        if (s < 15) { const int o2 = ((((s + 1) >> 2) + hgl) & 3) * 8192 + ((s + 1) & 3) * 2048; wl[(s + 1) & 1][0] = trd2(qa + o2, 512); wl[(s + 1) & 1][1] = trd2(qb + o2, 512); }
        a0 = mfma32(wl[s & 1][0], of[s], a0); a1 = mfma32(wl[s & 1][1], of[s], a1);
        SGB_MFMA(1); SGB_DSR(2); SGB_MFMA(1); SGB_DSR(2);
        SB();
      }
      u32x2 xres[4];
#pragma unroll
      for (int q = 0; q < 4; ++q) xres[q] = *(const LAS u32x2*)(L + L_XRES + (32 * tb + tok) * 128 + (32 * kh + 8 * q + 4 * h) * 2);
      const f32x16 snd = a1, acc = a0;
#pragma unroll
      for (int q = 0; q < 4; ++q) *(LAS f32x4*)(L + L_RED2 + cw * 4096 + q * 1024 + lane * 16) = f32x4{snd[4 * q], snd[4 * q + 1], snd[4 * q + 2], snd[4 * q + 3]};
      STAMP(19);
      bar();
      STAMP(20);
      const float mu1 = ((const LAS float*)(L + L_MU1))[32 * tb + tok];
      float s1 = 0.f, s2 = 0.f; u32 yp[8];
#pragma unroll
      for (int q = 0; q < 4; ++q) {
        const f32x4 o4 = *(const LAS f32x4*)(L + L_RED2 + (cw ^ 2) * 4096 + q * 1024 + lane * 16);
        const u32x2 xq = xres[q];
        const f32x4 ga4 = *(const LAS f32x4*)(L + L_GA + (32 * kh + 8 * q + 4 * h) * 4), g24 = *(const LAS f32x4*)(L + L_G2C + (32 * kh + 8 * q + 4 * h) * 4);
        const float v0 = fmaf(ga4[0], acc[4 * q] + o4[0], bflo(xq[0])), v1 = fmaf(ga4[1], acc[4 * q + 1] + o4[1], bfhi(xq[0]));
        const float v2 = fmaf(ga4[2], acc[4 * q + 2] + o4[2], bflo(xq[1])), v3 = fmaf(ga4[3], acc[4 * q + 3] + o4[3], bfhi(xq[1]));
        x1p[2 * q] = pk2(v0, v1); x1p[2 * q + 1] = pk2(v2, v3);
        const float d0 = bflo(x1p[2 * q]) - mu1, d1 = bfhi(x1p[2 * q]) - mu1, d2 = bflo(x1p[2 * q + 1]) - mu1, d3 = bfhi(x1p[2 * q + 1]) - mu1;
        s1 += (d0 + d1) + (d2 + d3); s2 = fmaf(d0, d0, fmaf(d1, d1, fmaf(d2, d2, fmaf(d3, d3, s2))));
        yp[2 * q] = pk2(d0 * g24[0], d1 * g24[1]); yp[2 * q + 1] = pk2(d2 * g24[2], d3 * g24[3]);
      }
      const u32 yb = WS_Y + (u32)(((2 * tt + tb) * 32 + 4 * cb + 2 * kh) * 1024);
      st128_sc1(u32x4{yp[0], yp[1], yp[2], yp[3]}, rws, (u32)(lane * 16), yb);
      st128_sc1(u32x4{yp[4], yp[5], yp[6], yp[7]}, rws, (u32)(lane * 16), yb + 1024);
      s1 = swap32_add(s1, s1); s2 = swap32_add(s2, s2);
      if (lane < 32) st64_sc1(u32x2{u_(s1), u_(s2)}, rws, (u32)(tok * 8), WS_ST + (u32)(((2 * tt + tb) * 16 + 2 * cb + kh) * 256));
    }
#pragma unroll
    for (int q = 0; q < 8; q += 4) asm volatile("" : "+v"(x1p[q]), "+v"(x1p[q + 1]), "+v"(x1p[q + 2]), "+v"(x1p[q + 3]));
    STAMP(12);
    pubbar<0>(rws, WS_FY + (u32)(tt * 256 + cb * 8), tlo, thi, tid);
    STAMP(13);
    wait_flags<8>(rws, WS_FY + (u32)((2 * T + (cw >> 1)) * 256), lane, tlo, thi);
    STAMP(14);
    const int tb32 = 4 * T + cw;
    u32x2 stv[8];
#pragma unroll
    for (int q = 0; q < 8; ++q) stv[q] = ld64_sc1(rws, (u32)(tok * 8 + h * 2048), WS_ST + (u32)((tb32 * 16 + q) * 256));
    u32x4 yf[32];
    auto ldy = [&](int s) { yf[s] = ld128_sc1(rws, (u32)(lane * 16), WS_Y + (u32)((tb32 * 32 + ((s + 2 * rotg) & 31)) * 1024)); };
#pragma unroll
    for (int s = 0; s < (EXPW ? 32 : 10); ++s) { ldy(s); SB(); }
    SB();
    u32 mz, sz; float r2;
    {
      float s1 = 0.f, s2 = 0.f;
#pragma unroll
      for (int q = 0; q < 8; ++q) { s1 += f_(stv[q][0]); s2 += f_(stv[q][1]); }
      s1 = swap32_add(s1, s1); s2 = swap32_add(s2, s2);
      const float md = s1 * (1.f / 512.f);
      const float ve = fmaxf(s2 * (1.f / 512.f) - md * md, 0.f) + 1e-5f;
      const float sg = __builtin_amdgcn_sqrtf(ve); r2 = __builtin_amdgcn_rcpf(ve);
      const u32 hi = pk2(-md, sg), lo = pk2(-md - bflo(hi), sg - bfhi(hi));
      const u32 mm = (hi & 0xffffu) | (lo << 16), ss = (hi >> 16) | (lo & 0xffff0000u);
      mz = h ? 0u : mm; sz = h ? 0u : ss;
      asm volatile("" : "+v"(mz), "+v"(sz), "+v"(r2));
    }
    SB();
    auto act_store = [&](const f32x16& ac, int pp) {
      u32x4 o;
#pragma unroll
      for (int e = 0; e < 4; ++e) o[e] = pk2(fmaxf(ac[2 * e], 0.f) * r2 * ac[8 + 2 * e], fmaxf(ac[2 * e + 1], 0.f) * r2 * ac[9 + 2 * e]);
      st128_sc1(o, rws, (u32)(lane * 16), WS_ACT + (u32)((tb32 * 112 + 7 * jt + pp) * 1024));
    };
    {
      f32x16 acc[4];
#pragma unroll
      for (int q = 0; q < 4; ++q)
#pragma unroll
        for (int r = 0; r < 16; ++r) acc[q][r] = 0.f;
      LAS char* pa[4];
#pragma unroll
      for (int pp = 0; pp < 4; ++pp) pa[pp] = L + (p1base + (((u32)pp ^ p1x) * 32));
      u32x4 wf[2][4];
      auto ldw = [&](int st, int bi) {
#pragma unroll
        for (int pp = 0; pp < 4; ++pp) wf[bi][pp] = trd2(pa[pp] + (st >> 1) * L_P1B + (st & 1) * 2048, 1024);
      };
      ldw(0, 0); SB();
#pragma unroll
      for (int st = 0; st < 32; ++st) {
        if (st == 7 || st == 15 || st == 23) WACC(25, bar());
        if (st + 1 < 32) ldw(st + 1, (st + 1) & 1);
        if (!EXPW && st + 10 < 32) ldy(st + 10);
#pragma unroll
        for (int pp = 0; pp < 4; ++pp) acc[pp] = mfma32(wf[st & 1][pp], yf[st], acc[pp]);
#pragma unroll
        for (int pp = 0; pp < 4; ++pp) { SGB_MFMA(1); SGB_DSR(2); }
        SB();
      }
      WACC(26, bar());
#pragma unroll
      for (int pp = 0; pp < 4; ++pp) acc[pp] = mfma32(*(const LAS u32x4*)(L + L_FIX + pp * 1024 + lane * 16), u32x4{mz, mz, sz, sz}, acc[pp]);
      STAMP(15);
#pragma unroll
      for (int pp = 0; pp < 4; ++pp) act_store(acc[pp], p0 + pp);
    }
    u32x4 af[16][2]; u32x2 fl;
    auto lda = [&](int n) { const int s = n >> 1, t2 = n & 1; af[s][t2] = ld128_sc1(rws, (u32)(lane * 16), WS_ACT + (u32)(((2 * tt + (t2 ^ tb)) * 112 + 28 * cw + 8 * (((s >> 2) + rotd) & 3) + (s & 3)) * 1024)); };
    {
      f32x16 acc[3];
#pragma unroll
      for (int q = 0; q < 3; ++q)
#pragma unroll
        for (int r = 0; r < 16; ++r) acc[q][r] = 0.f;
      LAS char* pb = L + p2base;
      u32x4 wf[2][3];
      auto ldw = [&](int st, int bi) {
#pragma unroll
        for (int pp = 0; pp < 3; ++pp) wf[bi][pp] = trd2(pb + (st >> 1) * L_P2B + (st & 1) * 1536 + pp * 32, 768);
      };
      ldw(0, 0); SB();
#pragma unroll
      for (int st = 0; st < 32; ++st) {
        if (st == 3) WACC(30, pubbar<0>(rws, WS_FA + (u32)(T * 256 + jt * 8), tlo, thi, tid));
        if (st == 7 || st == 15 || st == 23) WACC(27, bar());
        if (st == FA1P) fl = peek_flags<4>(rws, WS_FA + (u32)(T * 256 + cw * 32), lane);
        if (st == FA1P + 4) { STAMP(23); WACC(28, wait_flags_from<4>(fl, rws, WS_FA + (u32)(T * 256 + cw * 32), lane, tlo, thi)); }
        if (st + 1 < 32) ldw(st + 1, (st + 1) & 1);
        if (st >= FA1P + 4) lda(st - FA1P - 4);
#pragma unroll
        for (int pp = 0; pp < 3; ++pp) acc[pp] = mfma32(wf[st & 1][pp], yf[st], acc[pp]);
#pragma unroll
        for (int pp = 0; pp < 3; ++pp) { SGB_MFMA(1); SGB_DSR(2); }
        SB();
      }
      WACC(27, bar());
#pragma unroll
      for (int pp = 0; pp < 3; ++pp) acc[pp] = mfma32(*(const LAS u32x4*)(L + L_FIX + pp * 1024 + lane * 16), u32x4{mz, mz, sz, sz}, acc[pp]);
      STAMP(16);
#pragma unroll
      for (int pp = 0; pp < 3; ++pp) act_store(acc[pp], pp >= p0 ? pp + 4 : pp);
#pragma unroll
      for (int n = 28 - FA1P; n < 24; ++n) { lda(n); SB(); }
    }
    SB();
    {
      f32x16 acc[4];
#pragma unroll
      for (int q = 0; q < 4; ++q)
#pragma unroll
        for (int r = 0; r < 16; ++r) acc[q][r] = 0.f;
      LAS char* d0 = L + (cw * 8192 + (4 * h + q4) * 128 + mrow * 32 + (lane & 3) * 8);
      LAS char* da = d0 + ((q4 >> 1) ^ kh) * 64; LAS char* db = d0 + ((q4 >> 1) ^ kh ^ 1) * 64;
      u32x4 wl[2][2], a2[12][2];
      auto lda2 = [&](int n) {
        const int s2 = n >> 1, t2 = n & 1, jj = (s2 / 3 + rotd) & 3, pp = s2 % 3;
        a2[s2][t2] = ld128_sc1(rws, (u32)(lane * 16), WS_ACT + (u32)(((2 * tt + (t2 ^ tb)) * 112 + 28 * cw + 7 * jj + (pp >= jj ? pp + 4 : pp)) * 1024));
      };
      auto ldw = [&](int s, int bi) { const u32 o = wd_unit_off(s >> 2) + (s & 3) * 2048; wl[bi][0] = trd2(da + o, 1024); wl[bi][1] = trd2(db + o, 1024); };
      ldw(0, 0); SB();
#pragma unroll
      for (int s = 0; s < 28; ++s) {
        if (s == 3) WACC(30, pubbar<FA1P - 4 + 3>(rws, WS_FA2 + (u32)(T * 256 + jt * 8), tlo, thi, tid));
        if ((s & 3) == 3 && s > 3 && s < 24) WACC(29, bar());
        if (s == 12) {
          STAMP(17);
          wait_flags_from<4>(fl, rws, WS_FA2 + (u32)(T * 256 + cw * 32), lane, tlo, thi);
          STAMP(21);
#pragma unroll
          for (int n = 0; n < 12; ++n) { lda2(n); SB(); }
          SB();
        }
        if (s < 8) lda(24 + s);
        if (s == 8) fl = peek_flags<4>(rws, WS_FA2 + (u32)(T * 256 + cw * 32), lane);
        if (s >= 13 && s < 19) { lda2(12 + 2 * (s - 13)); SB(); lda2(13 + 2 * (s - 13)); }
        if (s + 1 < 28) ldw(s + 1, (s + 1) & 1);
        const u32x4 b0 = s < 16 ? af[s < 16 ? s : 0][0] : a2[s < 16 ? 0 : s - 16][0], b1 = s < 16 ? af[s < 16 ? s : 0][1] : a2[s < 16 ? 0 : s - 16][1];
        acc[0] = mfma32(wl[s & 1][0], b0, acc[0]); acc[2] = mfma32(wl[s & 1][1], b0, acc[2]);
        acc[1] = mfma32(wl[s & 1][0], b1, acc[1]); acc[3] = mfma32(wl[s & 1][1], b1, acc[3]);
        SGB_MFMA(1); SGB_DSR(2); SGB_MFMA(1); SGB_DSR(2); SGB_MFMA(2);
        SB();
      }
      STAMP(22);
      int ln2 = lane; asm volatile("" : "+v"(ln2));
#pragma unroll
      for (int i = 1; i < 4; ++i)
#pragma unroll
        for (int q = 0; q < 4; ++q) *(LAS f32x4*)(L + L_RED4 + ((i ^ cw) * 3 + i - 1) * 4096 + q * 1024 + ln2 * 16) = f32x4{acc[i][4 * q], acc[i][4 * q + 1], acc[i][4 * q + 2], acc[i][4 * q + 3]};
      const f32x16 mine = acc[0];
      bar();
      u32 ob[8];
#pragma unroll
      for (int q = 0; q < 4; ++q) {
        const LAS char* rp = L + L_RED4 + cw * 3 * 4096 + q * 1024 + ln2 * 16;
        const f32x4 r0 = *(const LAS f32x4*)rp, r1 = *(const LAS f32x4*)(rp + 4096), r2_ = *(const LAS f32x4*)(rp + 8192);
        const f32x4 gb4 = *(const LAS f32x4*)(L + L_GBV + (32 * kh + 8 * q + 4 * (ln2 >> 5)) * 4);
        float v[4];
#pragma unroll
        for (int e = 0; e < 4; ++e) v[e] = (mine[4 * q + e] + r0[e]) + (r1[e] + r2_[e]);
        ob[2 * q] = pk2(fmaf(gb4[0], v[0], bflo(x1p[2 * q])), fmaf(gb4[1], v[1], bfhi(x1p[2 * q])));
        ob[2 * q + 1] = pk2(fmaf(gb4[2], v[2], bflo(x1p[2 * q + 1])), fmaf(gb4[3], v[3], bfhi(x1p[2 * q + 1])));
      }
#pragma unroll
      for (int m = 0; m < 2; ++m) {
        auto r0 = __builtin_amdgcn_permlane32_swap(ob[4 * m], ob[4 * m + 2], false, false);
        auto r1 = __builtin_amdgcn_permlane32_swap(ob[4 * m + 1], ob[4 * m + 3], false, false);
        const u32 a0 = r0[0], b0 = r0[1], a1 = r1[0], b1 = r1[1];
        __builtin_amdgcn_raw_buffer_store_b128(u32x4{a0, a1, b0, b1}, rout, (u32)((64 * tt + 32 * tb + (ln2 & 31)) * 1024 + (64 * cb + 32 * kh + 16 * m + 8 * (ln2 >> 5)) * 2), 0, 0);
      }
    }
    STAMP(18);
    if (b == 0 && cw == 0) {
      const u64 t0 = __builtin_amdgcn_s_memrealtime();
      for (;;) {
        bool ok = true;
#pragma unroll
        for (int k = 0; k < 4; ++k) { const int f = 64 * k + lane; const u32x2 v = ld64_sc1(rws, (u32)((f >> 3) * 256 + (f & 7) * 8), WS_FO); ok = ok && v[0] == tlo && v[1] == thi; }
        if (__all(ok)) break;
        if (spin_out(t0)) break;
      }
      if (lane == 0) st64_sc1(u32x2{tlo, thi}, rws, 0u, WS_E);
    }
  }
}

__global__ __launch_bounds__(512, 2) void fused_block(Params p) {
  extern __shared__ __attribute__((aligned(16))) char smem_[];
  asm volatile("" :: "s"(p.x), "s"(p.wqkv), "s"(p.wo), "s"(p.wg), "s"(p.wu), "s"(p.wd), "s"(p.c), "s"(p.wmod), "s"(p.out), "s"(p.ws));
  int w = __builtin_amdgcn_readfirstlane(threadIdx.x >> 6);
  u32x4 wr[18]; u32 tlo, thi;
  front(p, (LAS char*)smem_, threadIdx.x, w, blockIdx.x, wr, tlo, thi STAMP_ARG);
  if (w >= 4) {
    int b = blockIdx.x, tid = threadIdx.x; asm volatile("; H role" : "+s"(b), "+s"(w)); asm volatile("; H role" : "+v"(tid));
    role_body<false>(p, (LAS char*)smem_, tid, w, b, wr, tlo, thi STAMP_ARG);
  } else {
    int b = blockIdx.x, tid = threadIdx.x; asm volatile("; C role" : "+s"(b), "+s"(w)); asm volatile("; C role" : "+v"(tid));
    role_body<true>(p, (LAS char*)smem_, tid, w, b, wr, tlo, thi STAMP_ARG);
  }
}

extern "C" void kernel_launch(void* const* d_in, const int* in_sizes, int n_in, void* d_out, int out_size, void* d_ws, size_t ws_size, hipStream_t stream) {
  Params p;
  p.x = d_in[0]; p.wqkv = d_in[1]; p.wo = d_in[2]; p.wg = d_in[4]; p.wu = d_in[5]; p.wd = d_in[6]; p.c = d_in[7]; p.wmod = d_in[8];
  p.out = d_out; p.ws = d_ws;
  (void)hipFuncSetAttribute((const void*)fused_block, hipFuncAttributeMaxDynamicSharedMemorySize, LDS_BYTES);
  fused_block<<<256, 512, LDS_BYTES, stream>>>(p);
}
```

```cpp
#define DIAG 1
#define ENC 0
#define ENCB 100
#include <hip/hip_runtime.h>
#include <stdint.h>

typedef unsigned short u16;
typedef unsigned u32;
typedef unsigned long long u64;
typedef u32 u32x2 __attribute__((ext_vector_type(2)));
typedef u32 u32x4 __attribute__((ext_vector_type(4)));
typedef short s16x4 __attribute__((ext_vector_type(4)));
typedef short s16x8 __attribute__((ext_vector_type(8)));
typedef __bf16 bf16x2 __attribute__((ext_vector_type(2)));
typedef __bf16 bf16x8 __attribute__((ext_vector_type(8)));
typedef float f32x2 __attribute__((ext_vector_type(2)));
typedef float f32x4 __attribute__((ext_vector_type(4)));
typedef float f32x16 __attribute__((ext_vector_type(16)));
typedef __amdgpu_buffer_rsrc_t rsrc_t;
#define LAS __attribute__((address_space(3)))
#define DI __device__ __forceinline__

#define EXPW 0
#define TOUCH 30
#define THR 4
constexpr unsigned CODE_BYTES = 50000;
#define NTX 1
#define FA1P 16
#define XMAP 2

constexpr int S_ = 2048, D_ = 512, F_ = 1792;
constexpr u32 WS_E = 0, WS_MODG = 393216, WS_FO = 32768, WS_FY = 40960, WS_FA = 49152, WS_FA2 = 53248, WS_DBG = 65536;
constexpr u32 WS_O = 1u << 20, WS_Y = 3u << 20, WS_ST = 5u << 20, WS_ACT = 6u << 20, WS_BYTES = 13u << 20;
constexpr u32 L_AIMG = 0, L_RW = 81920, L_W1S = 12288;
constexpr u32 L_LNP = 73728, L_PRED = L_LNP + 6400, L_RSNB = L_PRED + 1088;
constexpr u32 L_XRES = 65536, L_WO = 73728, L_RED2 = 139264;
constexpr u32 L_P1B = 8192, L_P2B = 6400, L_P2M = 3200;
constexpr u32 L_RED4 = 65536;
constexpr u32 L_MISC = 155648, L_G1 = L_MISC, L_S1 = L_MISC + 2048, L_FIX = L_MISC;
constexpr u32 L_MU1 = L_MISC + 7168 + 576, L_GA = L_MISC + 4096, L_G2C = L_GA + 256, L_GBV = L_G2C + 256, L_GS = L_MISC + 5120;
constexpr u32 L_CP = L_MISC + 7168, L_HP = L_CP + 16, L_RX = L_CP + 32, L_CNT = L_CP + 48;
constexpr u32 LDS_BYTES = 163840;
__host__ __device__ constexpr u32 wd_unit_off(int u) { return u == 0 || u == 4 ? 102400u : (u == 1 || u == 5 ? 0u : (u == 2 || u == 6 ? 32768u : 65536u)); }

template <int N> struct ic { static constexpr int value = N; };
struct Params { const void *x, *wqkv, *wo, *wg, *wu, *wd, *c, *wmod; void *out; void *ws; };

DI rsrc_t mk(const void* p, u32 bytes) { return __builtin_amdgcn_make_buffer_rsrc((void*)p, 0, bytes, 0x00020000); }
DI float bflo(u32 v) { return __builtin_bit_cast(float, v << 16); }
DI float bfhi(u32 v) { return __builtin_bit_cast(float, v & 0xffff0000u); }
DI u32 pk2(float a, float b) { bf16x2 r; r[0] = (__bf16)a; r[1] = (__bf16)b; return __builtin_bit_cast(u32, r); }
DI float rbf(float a) { return bflo(pk2(a, a)); }
DI f32x4 mfma16(u32x4 a, u32x4 b, f32x4 c) { return __builtin_amdgcn_mfma_f32_16x16x32_bf16(__builtin_bit_cast(bf16x8, a), __builtin_bit_cast(bf16x8, b), c, 0, 0, 0); }
DI f32x4 mfma16k16(u32x2 a, u32x2 b, f32x4 c) { return __builtin_amdgcn_mfma_f32_16x16x16bf16_1k(__builtin_bit_cast(s16x4, a), __builtin_bit_cast(s16x4, b), c, 0, 0, 0); }
DI f32x16 mfma32(u32x4 a, u32x4 b, f32x16 c) { return __builtin_amdgcn_mfma_f32_32x32x16_bf16(__builtin_bit_cast(bf16x8, a), __builtin_bit_cast(bf16x8, b), c, 0, 0, 0); }
DI u32x2 trd(LAS char* p) { return __builtin_bit_cast(u32x2, __builtin_amdgcn_ds_read_tr16_b64_v4i16((LAS s16x4*)p)); }
DI u32x4 trd2(LAS char* p, int d) { u32x2 a = trd(p), b = trd(p + d); return u32x4{a[0], a[1], b[0], b[1]}; }
DI LAS char* opq(LAS char* p) { asm volatile("" : "+v"(p)); return p; }
DI void dma16(rsrc_t rs, u32 voff, u32 soff, u32 lds) {
  asm volatile("s_mov_b32 m0, %0\n\ts_nop 0\n\tbuffer_load_dwordx4 %1, %2, %3 offen lds" :: "s"(lds), "v"(voff), "s"(rs), "s"(soff) : "m0", "memory");
}
template <int OFF> DI void ald128(u32x4& d, rsrc_t rs, u32 voff, u32 soff) {
  asm volatile("buffer_load_dwordx4 %0, %1, %2, %3 offen offset:%c4" : "=v"(d) : "v"(voff), "s"(rs), "s"(soff), "i"(OFF) : "memory");
}
DI void ald128_nt(u32x4& d, rsrc_t rs, u32 voff, u32 soff) {
  asm volatile("buffer_load_dwordx4 %0, %1, %2, %3 offen nt" : "=v"(d) : "v"(voff), "s"(rs), "s"(soff) : "memory");
}
template <int OFF> DI void ald128_sc1(u32x4& d, rsrc_t rs, u32 voff, u32 soff) {
  asm volatile("buffer_load_dwordx4 %0, %1, %2, %3 offen offset:%c4 sc1" : "=v"(d) : "v"(voff), "s"(rs), "s"(soff), "i"(OFF) : "memory");
}
DI void ald64_sc1(u32x2& d, rsrc_t rs, u32 voff, u32 soff) {
  asm volatile("buffer_load_dwordx2 %0, %1, %2, %3 offen sc1" : "=v"(d) : "v"(voff), "s"(rs), "s"(soff) : "memory");
}
DI void ald64(u32x2& d, rsrc_t rs, u32 voff, u32 soff) {
  asm volatile("buffer_load_dwordx2 %0, %1, %2, %3 offen" : "=v"(d) : "v"(voff), "s"(rs), "s"(soff) : "memory");
}
DI void warm(u32 lds, rsrc_t rs, u32 voff, u32 soff) {
  asm volatile("s_mov_b32 m0, %0\n\ts_nop 0\n\tbuffer_load_dword %1, %2, %3 offen lds" :: "s"(lds), "v"(voff), "s"(rs), "s"(soff) : "m0", "memory");
}
template <int N> DI void waitv() { asm volatile("s_waitcnt vmcnt(%c0)" :: "i"(N) : "memory"); }
template <int N> DI void waitv4(u32x4& a, u32x4& b, u32x4& c, u32x4& d) { asm volatile("s_waitcnt vmcnt(%c4)" : "+v"(a), "+v"(b), "+v"(c), "+v"(d) : "i"(N) : "memory"); }
template <int N> DI void waitv1(u32x4& a) { asm volatile("s_waitcnt vmcnt(%c1)" : "+v"(a) : "i"(N) : "memory"); }
DI void bar() { asm volatile("s_waitcnt lgkmcnt(0)\n\ts_barrier" ::: "memory"); }
DI u32x4 ld128_sc1(rsrc_t rs, u32 voff, u32 soff) { return __builtin_amdgcn_raw_buffer_load_b128(rs, voff, soff, 16); }
DI u32x2 ld64_sc1(rsrc_t rs, u32 voff, u32 soff) { return __builtin_amdgcn_raw_buffer_load_b64(rs, voff, soff, 16); }
DI void st128_sc1(u32x4 v, rsrc_t rs, u32 voff, u32 soff) { __builtin_amdgcn_raw_buffer_store_b128(v, rs, voff, soff, 16); }
DI void st64_sc1(u32x2 v, rsrc_t rs, u32 voff, u32 soff) { __builtin_amdgcn_raw_buffer_store_b64(v, rs, voff, soff, 16); }
DI float f_(u32 v) { return __builtin_bit_cast(float, v); }
DI u32 u_(float v) { return __builtin_bit_cast(u32, v); }
DI float swap16_add(float a, float b) { auto r = __builtin_amdgcn_permlane16_swap(u_(a), u_(b), false, false); u32 r0 = r[0], r1 = r[1]; return f_(r0) + f_(r1); }
DI float swap32_add(float a, float b) { auto r = __builtin_amdgcn_permlane32_swap(u_(a), u_(b), false, false); u32 r0 = r[0], r1 = r[1]; return f_(r0) + f_(r1); }
DI float swap16_max(float a) { auto r = __builtin_amdgcn_permlane16_swap(u_(a), u_(a), false, false); u32 r0 = r[0], r1 = r[1]; return fmaxf(f_(r0), f_(r1)); }
DI float swap32_max(float a) { auto r = __builtin_amdgcn_permlane32_swap(u_(a), u_(a), false, false); u32 r0 = r[0], r1 = r[1]; return fmaxf(f_(r0), f_(r1)); }
template <int CTRL> DI float dpp_add(float a) { return a + f_(__builtin_amdgcn_update_dpp(0, u_(a), CTRL, 0xf, 0xf, true)); }
DI float dot2(u32 a, u32 b, float c) { return __builtin_amdgcn_fdot2_f32_bf16(__builtin_bit_cast(bf16x2, a), __builtin_bit_cast(bf16x2, b), c, false); }

constexpr u64 SPIN_TICKS = 20000;
DI bool spin_out(u64 t0) { __builtin_amdgcn_s_sleep(1); return (__builtin_amdgcn_s_memrealtime() - t0) > SPIN_TICKS; }

#define SB() __builtin_amdgcn_sched_barrier(0)
#define SGB_MFMA(n) __builtin_amdgcn_sched_group_barrier(0x8, n, 0)
#define SGB_DSR(n) __builtin_amdgcn_sched_group_barrier(0x100, n, 0)
#define SGB_DSW(n) __builtin_amdgcn_sched_group_barrier(0x200, n, 0)
#define STAMP(i) do { __builtin_amdgcn_sched_barrier(0); u64 t_; asm volatile("s_memrealtime %0\n\ts_waitcnt lgkmcnt(0)" : "=s"(t_) :: "memory"); stamp[i] = (u32)t_; __builtin_amdgcn_sched_barrier(0); } while (0)
#define WACC(i, code) do { u64 a_, b_; __builtin_amdgcn_sched_barrier(0); asm volatile("s_memtime %0\n\ts_waitcnt lgkmcnt(0)" : "=s"(a_) :: "memory"); code; asm volatile("s_memtime %0\n\ts_waitcnt lgkmcnt(0)" : "=s"(b_) :: "memory"); stamp[i] += (u32)(b_ - a_); __builtin_amdgcn_sched_barrier(0); } while (0)

DI void lsig(LAS char* byte, u32 v, int lane) { asm volatile("" ::: "memory"); if (lane == 0) *(volatile LAS unsigned char*)byte = (unsigned char)v; asm volatile("" ::: "memory"); }
DI u32 lpeek(LAS char* word) { return *(volatile LAS u32*)word; }
DI bool lge(u32 v, u32 want) { return (((__builtin_amdgcn_readfirstlane(v) | 0x80808080u) - want * 0x01010101u) & 0x80808080u) == 0x80808080u; }
template <bool SLEEP> DI void lwait_from(u32 seen, LAS char* word, u32 want) {
  if (!lge(seen, want)) {
    for (u32 sp = 0; sp < (1u << 18); ++sp) {
      if (SLEEP) __builtin_amdgcn_s_sleep(1);
      if (lge(lpeek(word), want)) break;
    }
  }
  asm volatile("" ::: "memory");
}
template <bool SLEEP> DI void lwait(LAS char* word, u32 want) { lwait_from<SLEEP>(lpeek(word), word, want); }
template <int YOUNGER> DI void pubbar(rsrc_t rws, u32 flag_off, u32 tlo, u32 thi, int tid) {
  waitv<YOUNGER>(); bar();
  if (tid == 0) st64_sc1(u32x2{tlo, thi}, rws, 0u, flag_off);
}
template <int YOUNGER> DI void publish(LAS char* cnt, rsrc_t rws, u32 flag_off, u32 tlo, u32 thi, int lane) {
  waitv<YOUNGER>();
  if (lane == 0) {
    const u32 old = __hip_atomic_fetch_add((LAS u32*)cnt, 1u, __ATOMIC_RELAXED, __HIP_MEMORY_SCOPE_WORKGROUP);
    if (old == 3u) st64_sc1(u32x2{tlo, thi}, rws, 0u, flag_off);
  }
  asm volatile("" ::: "memory");
}
template <int NF> DI u32x2 peek_flags(rsrc_t rws, u32 off, int lane) { return ld64_sc1(rws, (u32)(lane & (NF - 1)) * 8u, off); }
template <int NF> DI void wait_flags(rsrc_t rws, u32 off, int lane, u32 tlo, u32 thi) {
  const u32 vo = (u32)(lane & (NF - 1)) * 8u;
  const u64 t0 = __builtin_amdgcn_s_memrealtime();
  for (;;) {
    u32x2 f = ld64_sc1(rws, vo, off);
    if (__all(f[0] == tlo && f[1] == thi)) break;
    if (spin_out(t0)) break;
  }
  __builtin_amdgcn_fence(__ATOMIC_ACQUIRE, "wavefront");
}
DI bool gran_ok(u32x4 g, u32 glo, u32 ghi) { return (g[0] & 0xffu) == glo && g[1] == ghi && (g[2] & 0xffu) == glo && g[3] == ghi; }

#define STAMP_PARAM , u32 (&stamp)[31]
#define STAMP_ARG , stamp
template <int NF> DI void wait_flags_from(u32x2 seen, rsrc_t rws, u32 off, int lane, u32 tlo, u32 thi) {
  if (!__all(seen[0] == tlo && seen[1] == thi)) wait_flags<NF>(rws, off, lane, tlo, thi);
  __builtin_amdgcn_fence(__ATOMIC_ACQUIRE, "wavefront");
}
struct Gath { u32x4 q[4]; };
DI void gath_load(Gath& G, rsrc_t rws, u32 voff, u32 soff) {
#pragma unroll
  for (int k = 0; k < 4; ++k) G.q[k] = ld128_sc1(rws, voff, WS_MODG + soff + (u32)k * 24576u);
}
DI bool gath_ok(const Gath& G, u32 glo, u32 ghi) { return gran_ok(G.q[0], glo, ghi) && gran_ok(G.q[1], glo, ghi) && gran_ok(G.q[2], glo, ghi) && gran_ok(G.q[3], glo, ghi); }
DI u32 gath_val(const Gath& G) {
  const float a = (f_(G.q[0][0] & 0xffffff00u) + f_(G.q[1][0] & 0xffffff00u)) + (f_(G.q[2][0] & 0xffffff00u) + f_(G.q[3][0] & 0xffffff00u));
  const float b = (f_(G.q[0][2] & 0xffffff00u) + f_(G.q[1][2] & 0xffffff00u)) + (f_(G.q[2][2] & 0xffffff00u) + f_(G.q[3][2] & 0xffffff00u));
  return pk2(a, b);
}
DI void front(const Params& p, LAS char* L, const int tid, const int w, const int b, u32x4 (&wr)[18], u32& tlo, u32& thi STAMP_PARAM) {
  const bool isC = w < 4;
  f32x2 xh[10][4];
  const u32 Lb = (u32)(size_t)L;
  const int lane = tid & 63, cw = w & 3;
  const int xcd = b & 7, idx = b >> 3;
  const int tt = 8 * (xcd >> 1) + (idx >> 2), hgl = idx & 3, hg = 4 * (xcd & 1) + hgl;
  const int jt = 8 * (xcd & 1) + 4 * (tt & 1) + hgl;
  const int T = tt >> 1, cb = hg;
  STAMP(0);
  if (tid < 32) *(LAS u32*)(L + L_CP + tid * 4) = 0u;
  const rsrc_t rx = mk(p.x, S_ * D_ * 2), rw1 = mk(p.wqkv, D_ * 3 * D_ * 2), rwo = mk(p.wo, D_ * D_ * 2);
  const rsrc_t rwg = mk(p.wg, D_ * F_ * 2), rwu = mk(p.wu, D_ * F_ * 2), rwd = mk(p.wd, F_ * D_ * 2);
  const rsrc_t rc = mk(p.c, D_ * 2), rwm = mk(p.wmod, D_ * 6 * D_ * 2), rout = mk(p.out, S_ * D_ * 2), rws = mk(p.ws, WS_BYTES);
  const int g = lane >> 4, i16 = lane & 15, h = lane >> 5, tok = lane & 31, t4 = tid & 255;

  const int nrot = 2 * hgl, rot = 2 * (tt & 7);
  const u32 w1v = (u32)((lane >> 3) * 3072 + (((((lane >> 1) & 3) ^ ((lane >> 4) & 1)) ^ ((cw & 1) << 1)) * 32) + (lane & 1) * 16);
  auto w1slot = [](int n) { return n < 6 ? L_RW + n * L_W1S : (n < 12 ? (n - 6) * L_W1S : L_RW + (n - 12) * L_W1S); };
  {
    u64 pc; asm volatile("s_getpc_b64 %0" : "=s"(pc));
    const rsrc_t rcode = mk((const void*)(pc & ~(u64)255), CODE_BYTES);
    warm(Lb + L_CP + 256, rcode, (u32)((idx * 8 + w) * 2 + (lane & 1)) * 128u, 0u);
  }
  u32x2 ev; ald64_sc1(ev, rws, 0u, WS_E);
  const bool prod = isC && idx < 8, prod2 = isC && idx >= 8 && idx < 24;
  const int mq = idx & 3, mblk = idx < 8 ? xcd * 2 + (idx >> 2) : 16 + xcd * 4 + ((idx - 8) >> 2);
  auto mod_issue = [&](u32x4 (&wm)[4], u32x2& cv) {
    const int r0 = 128 * mq + 32 * cw;
#pragma unroll
    for (int i = 0; i < 4; ++i) ald128_nt(wm[i], rwm, (u32)((lane >> 3) * 4 * 6144 + (lane & 7) * 16), (u32)((r0 + i) * 6144 + mblk * 128));
    ald64(cv, rc, (u32)((lane >> 3) * 8), (u32)(r0 * 2));
  };
  auto dma_w1 = [&](int i) {
    const int n = (i < 9 ? 6 : 0) + 2 * ((i % 9) / 3) + (w >> 2), pp = i % 3;
    dma16(rw1, w1v, (u32)((32 * ((n + rot) & 15) + 8 * cw) * 3072 + pp * 1024 + hg * 128), Lb + (u32)((i < 9 ? (n - 6) * L_W1S : L_RW + n * L_W1S) + pp * 4096 + cw * 1024));
  };
  auto mod_finish = [&](u32 pred, const u32x4 (&wm)[4], const u32x2& cv, u32 tlo, u32 thi) {
    float a8[8];
#pragma unroll
    for (int e = 0; e < 8; ++e) a8[e] = 0.f;
#pragma unroll
    for (int i = 0; i < 4; ++i) {
      const float cf = (i & 1) ? bfhi(cv[i >> 1]) : bflo(cv[i >> 1]);
      const float sv = rbf(cf * __builtin_amdgcn_rcpf(1.f + __expf(-cf)));
#pragma unroll
      for (int e = 0; e < 4; ++e) { a8[2 * e] = fmaf(sv, bflo(wm[i][e]), a8[2 * e]); a8[2 * e + 1] = fmaf(sv, bfhi(wm[i][e]), a8[2 * e + 1]); }
    }
#pragma unroll
    for (int e = 0; e < 8; ++e) a8[e] = dpp_add<0x128>(a8[e]);
    float b4[4], b2[2];
#pragma unroll
    for (int e = 0; e < 4; ++e) b4[e] = swap16_add(a8[e], a8[e + 4]);
#pragma unroll
    for (int e = 0; e < 2; ++e) b2[e] = swap32_add(b4[e], b4[e + 2]);
    if (i16 < 8) *(LAS f32x2*)(L + pred + cw * 256 + (i16 * 8 + (g & 1) * 4 + (g >> 1) * 2) * 4) = f32x2{b2[0], b2[1]};
    asm volatile("s_waitcnt lgkmcnt(0)" ::: "memory");
    if (lane == 0) *(volatile LAS u32x2*)(L + pred + 1024 + cw * 8) = u32x2{tlo, thi};
    if (cw == 0) {
      const u64 t0 = __builtin_amdgcn_s_memrealtime();
      for (;;) {
        const u32x2 tg = *(volatile LAS u32x2*)(L + pred + 1024 + (lane & 3) * 8);
        if (__all(tg[0] == tlo && tg[1] == thi)) break;
        if ((__builtin_amdgcn_s_memrealtime() - t0) > SPIN_TICKS) break;
      }
      asm volatile("" ::: "memory");
      LAS float* s = (LAS float*)(L + pred) + lane;
      const float v = (s[0] + s[64]) + (s[128] + s[192]);
      st64_sc1(u32x2{((u_(v) + 0x80u) & 0xffffff00u) | (tlo & 0xffu), (tlo >> 8) | (thi << 24)}, rws, (u32)(lane * 8), WS_MODG + (u32)(mq * 24576 + mblk * 512));
    }
  };
  u32x2 cv; u32x4 wm[4];
  if (prod) mod_issue(wm, cv);
  u32x4 xr[10];
#pragma unroll
  for (int n = 0; n < 10; ++n) {
    int r8 = n + nrot; r8 = r8 >= 10 ? r8 - 10 : r8;
    int row = 64 * tt - 16 + 8 * r8 + (lane >> 3); row = row < 0 ? 0 : row;
    ald128_nt(xr[n], rx, (u32)(row * 1024 + (lane & 7) * 16), (u32)(w * 128));
  }
  {
    const rsrc_t rt = w == 0 ? rwo : w == 1 ? rwg : w == 2 ? rwu : w == 3 ? rwd : w == 4 ? rout : rws;
    warm(Lb + L_CP + 256, rt, (u32)lane << 20, 0u);
  }
  dma_w1(0); dma_w1(1); dma_w1(2);
  constexpr int Y0 = 3 + ((TOUCH & 4) ? 1 : 0);
  STAMP(1);
  if (prod) {
    asm volatile("s_waitcnt vmcnt(%c6)" : "+v"(wm[0]), "+v"(wm[1]), "+v"(wm[2]), "+v"(wm[3]), "+v"(cv), "+v"(ev) : "i"(10 + Y0) : "memory");
    const u64 e1 = (((u64)__builtin_amdgcn_readfirstlane(ev[1]) << 32) | (u64)__builtin_amdgcn_readfirstlane(ev[0])) + 1ull;
    mod_finish(L_PRED, wm, cv, (u32)e1, (u32)(e1 >> 32));
  }
  STAMP(2);
  waitv<Y0>();
#pragma unroll
  for (int n = 0; n < 10; n += 2) asm volatile("" : "+v"(xr[n]), "+v"(xr[n + 1]));
  asm volatile("" : "+v"(ev));
  {
    const u64 e1 = (((u64)__builtin_amdgcn_readfirstlane(ev[1]) << 32) | (u64)__builtin_amdgcn_readfirstlane(ev[0])) + 1ull;
    tlo = (u32)e1; thi = (u32)(e1 >> 32);
  }
  if (prod2) mod_issue(wm, cv);
  STAMP(3);
  {
    const int c8 = lane & 7, r8l = lane >> 3;
    float pv[10];
#pragma unroll
    for (int n = 0; n < 10; ++n) {
#pragma unroll
      for (int e = 0; e < 4; ++e) xh[n][e] = f32x2{bflo(xr[n][e]), bfhi(xr[n][e])};
      const f32x2 a1 = (xh[n][0] + xh[n][1]) + (xh[n][2] + xh[n][3]);
      const f32x2 a2 = __builtin_elementwise_fma(xh[n][0], xh[n][0], xh[n][1] * xh[n][1]) + __builtin_elementwise_fma(xh[n][2], xh[n][2], xh[n][3] * xh[n][3]);
      const float s1 = a1[0] + a1[1], s2 = a2[0] + a2[1];
      const float keep = (c8 & 1) ? s2 : s1, give = (c8 & 1) ? s1 : s2;
      float v = keep + f_(__builtin_amdgcn_update_dpp(0, u_(give), 0xB1, 0xf, 0xf, true));
      v = dpp_add<0x4E>(v);
      pv[n] = v + f_(__builtin_amdgcn_update_dpp(0, u_(v), 0x104, 0xf, 0xf, true));
      if (n < 6) { asm volatile("" : "+v"(pv[n])); dma_w1(3 + n); SB(); }
    }
    if (c8 < 2) {
#pragma unroll
      for (int n = 0; n < 10; ++n) *(LAS float*)(L + L_LNP + (n * 8 + r8l) * 80 + (w * 2 + c8) * 4) = pv[n];
    }
    bar();
    f32x4 qa[2][4];
#pragma unroll
    for (int k = 0; k < 2; ++k)
#pragma unroll
      for (int e = 0; e < 4; ++e) qa[k][e] = *(const LAS f32x4*)(L + L_LNP + (k ? 64 + i16 : lane) * 80 + e * 16);
    dma_w1(9); dma_w1(10);
    float mus[2];
#pragma unroll
    for (int k = 0; k < 2; ++k) {
      const f32x4 a = (qa[k][0] + qa[k][1]) + (qa[k][2] + qa[k][3]);
      const float mu = (a[0] + a[2]) * (1.f / 512.f);
      const float var = fmaxf((a[1] + a[3]) * (1.f / 512.f) - mu * mu, 0.f);
      const float rs = __builtin_amdgcn_rsqf(var + 1e-5f);
      *(LAS f32x2*)(L + L_RSNB + (k ? 64 + i16 : lane) * 8) = f32x2{rs, -mu * rs};
      mus[k] = mu;
    }
    asm volatile("" ::: "memory");
    f32x2 rn[10];
#pragma unroll
    for (int n = 0; n < 10; ++n) rn[n] = *(const LAS f32x2*)(L + L_RSNB + (n * 8 + r8l) * 8);
    if (w == 0) {
#pragma unroll
      for (int k = 0; k < 2; ++k) { int r = (k ? 64 + i16 : lane) + 8 * nrot; r = r >= 80 ? r - 80 : r; ((LAS float*)(L + L_MU1 - 64))[r] = mus[k]; }
    }
#pragma unroll
    for (int n = 0; n < 10; ++n) {
#pragma unroll
      for (int e = 0; e < 4; ++e) xh[n][e] = __builtin_elementwise_fma(xh[n][e], f32x2{rn[n][0], rn[n][0]}, f32x2{rn[n][1], rn[n][1]});
      if (n < 7) { asm volatile("" : "+v"(xh[n][0]), "+v"(xh[n][1]), "+v"(xh[n][2]), "+v"(xh[n][3])); dma_w1(11 + n); SB(); }
    }
  }
#pragma unroll
  for (int n = 0; n < 10; ++n) asm volatile("" : "+v"(xh[n][0]), "+v"(xh[n][1]), "+v"(xh[n][2]), "+v"(xh[n][3]));
  STAMP(4);
  const u32 glo = tlo & 0xffu, ghi = (tlo >> 8) | (thi << 24);
  const u32 gvo = (u32)(w * 512 + tok * 16 + h * 4096);
  Gath g1_;
#pragma unroll
  for (int k = 0; k < 4; ++k) ald128_sc1<0>(g1_.q[k], rws, gvo, WS_MODG + (u32)k * 24576u);
  if (prod2) {
    asm volatile("s_waitcnt vmcnt(19)" : "+v"(wm[0]), "+v"(wm[1]), "+v"(wm[2]), "+v"(wm[3]), "+v"(cv) :: "memory");
    mod_finish(L_PRED, wm, cv, tlo, thi);
  }
  waitv<13>();
  bar();
  if (isC) {
    LAS char* w1rd = L + ((8 * g + (i16 >> 2)) * 128 + ((cw ^ (((i16 >> 3) & 1) | ((g & 1) << 1))) * 32) + (lane & 3) * 8);

#pragma unroll
    for (int n = 0; n < 6; ++n)
#pragma unroll
      for (int pp = 0; pp < 3; ++pp) wr[3 * n + pp] = trd2(w1rd + n * L_W1S + pp * 4096, 512);
  }
  STAMP(5);
  waitv4<0>(g1_.q[0], g1_.q[1], g1_.q[2], g1_.q[3]);
  if (!__all(gath_ok(g1_, glo, ghi))) {
    const u64 t0 = __builtin_amdgcn_s_memrealtime();
    for (;;) {
      gath_load(g1_, rws, gvo, 0u);
      if (__all(gath_ok(g1_, glo, ghi))) break;
      if (spin_out(t0)) break;
    }
  }
  {
    const u32 v2 = gath_val(g1_);
    const float a = bflo(v2), c = bfhi(v2);
    *(LAS f32x2*)(L + (h ? L_G1 : L_S1) + (w * 32 + tok) * 8) = h ? f32x2{rbf(1.f + a), rbf(1.f + c)} : f32x2{a, c};
  }
  STAMP(6);
  bar();
  if (w < 3) warm(Lb + L_CP + 256, w == 0 ? rwg : w == 1 ? rwu : rwd, (u32)(b + 256 * lane) * 128u, 0u);
  {
    const int c8 = lane & 7, r8l = lane >> 3;
    const LAS f32x4* gp = (const LAS f32x4*)(L + L_G1 + (w * 64 + c8 * 8) * 4);
    const LAS f32x4* sp = (const LAS f32x4*)(L + L_S1 + (w * 64 + c8 * 8) * 4);
    const f32x4 g0 = gp[0], g1 = gp[1], s0 = sp[0], s1 = sp[1];
    LAS char* ab = L + L_AIMG + (2 * w + (c8 >> 2)) * 1024 + (c8 & 3) * 256 + ((r8l ^ c8) * 16);
#pragma unroll
    for (int n = 0; n < 10; ++n) {
      const f32x2 h0 = __builtin_elementwise_fma(xh[n][0], f32x2{g0[0], g0[1]}, f32x2{s0[0], s0[1]}), h1 = __builtin_elementwise_fma(xh[n][1], f32x2{g0[2], g0[3]}, f32x2{s0[2], s0[3]});
      const f32x2 h2 = __builtin_elementwise_fma(xh[n][2], f32x2{g1[0], g1[1]}, f32x2{s1[0], s1[1]}), h3 = __builtin_elementwise_fma(xh[n][3], f32x2{g1[2], g1[3]}, f32x2{s1[2], s1[3]});
      const u32x4 o = u32x4{pk2(h0[0], h0[1]), pk2(h1[0], h1[1]), pk2(h2[0], h2[1]), pk2(h3[0], h3[1])};
      int r8 = n + nrot; r8 = r8 >= 10 ? r8 - 10 : r8;
      *(LAS u32x4*)(ab + (r8 >> 1) * 16384 + (r8 & 1) * 128) = o;
    }
  }
  bar();
  STAMP(7);
}

template <bool isC> DI void role_body(const Params& p, LAS char* L, const int tid, const int w, const int b, u32x4 (&wr)[18], const u32 tlo, const u32 thi STAMP_PARAM) {
  const u32 Lb = (u32)(size_t)L;
  const int lane = tid & 63, cw = w & 3;
  const int xcd = b & 7, idx = b >> 3;
  const int tt = 8 * (xcd >> 1) + (idx >> 2), hgl = idx & 3, hg = 4 * (xcd & 1) + hgl;
  const int jt = 8 * (xcd & 1) + 4 * (tt & 1) + hgl;
  const int T = tt >> 1, cb = hg;
  const rsrc_t rx = mk(p.x, S_ * D_ * 2), rw1 = mk(p.wqkv, D_ * 3 * D_ * 2), rwo = mk(p.wo, D_ * D_ * 2);
  const rsrc_t rwg = mk(p.wg, D_ * F_ * 2), rwu = mk(p.wu, D_ * F_ * 2), rwd = mk(p.wd, F_ * D_ * 2);
  const rsrc_t rc = mk(p.c, D_ * 2), rwm = mk(p.wmod, D_ * 6 * D_ * 2), rout = mk(p.out, S_ * D_ * 2), rws = mk(p.ws, WS_BYTES);
  const int g = lane >> 4, i16 = lane & 15, h = lane >> 5, tok = lane & 31, t4 = tid & 255;

  const int nrot = 2 * hgl, rot = 2 * (tt & 7);
  const u32 w1v = (u32)((lane >> 3) * 3072 + (((((lane >> 1) & 3) ^ ((lane >> 4) & 1)) ^ ((cw & 1) << 1)) * 32) + (lane & 1) * 16);
  auto w1slot = [](int n) { return n < 6 ? L_RW + n * L_W1S : (n < 12 ? (n - 6) * L_W1S : L_RW + (n - 12) * L_W1S); };
  LAS char* w1rd = L + ((8 * g + (i16 >> 2)) * 128 + ((cw ^ (((i16 >> 3) & 1) | ((g & 1) << 1))) * 32) + (lane & 3) * 8);
  const u32 glo = tlo & 0xffu, ghi = (tlo >> 8) | (thi << 24);
  const int p0 = jt & 3, rotg = (4 * (T & 3) + 2 * (jt & 7)) & 15, rotd = (tt + hgl) & 3;
  const u32 wov = (u32)((lane >> 3) * 1024 + ((((lane >> 2) & 1) ^ ((lane >> 4) & 1)) * 64) + (lane & 3) * 16);
  const int mrow = (lane >> 4) & 1, q4 = i16 >> 2;
  const u32 p1base = (u32)(mrow * 4096 + (4 * h + q4) * 128 + (lane & 3) * 8), p1x = (u32)((q4 >> 1) | (mrow << 1));
  const u32 p2base = (u32)(mrow * L_P2M + (4 * h + q4) * 96 + (lane & 3) * 8);

  if constexpr (!isC) {
    LAS char* const CP = L + L_CP; LAS char* const HP = L + L_HP; LAS char* const myHP = HP + cw;
    Gath gs_, gc_, gg_;
#pragma unroll
    for (int k = 0; k < 4; ++k) {
      ald128_sc1<0>(gs_.q[k], rws, (u32)(t4 * 16), WS_MODG + 3 * 4096u + (u32)k * 24576u); ald128_sc1<0>(gc_.q[k], rws, (u32)(t4 * 16), WS_MODG + 4 * 4096u + (u32)k * 24576u);
      ald128_sc1<0>(gg_.q[k], rws, (u32)(tok * 16 + h * 3 * 4096), WS_MODG + 2 * 4096u + (u32)cb * 512u + (u32)k * 24576u);
    }
    bar();
#pragma unroll 1
    for (int n = 0; n < 4; ++n)
#pragma unroll
      for (int pp = 0; pp < 3; ++pp) dma16(rw1, w1v, (u32)((32 * ((12 + n + rot) & 15) + 8 * cw) * 3072 + pp * 1024 + hg * 128), Lb + (u32)(L_RW + n * L_W1S + pp * 4096 + cw * 1024));
    waitv<0>(); bar();
    STAMP(8);
    warm(Lb + L_CP + 256, rwo, (u32)((64 * (tt & 7) + 16 * cw + (lane & 15)) * 1024), (u32)(cb * 128));
    STAMP(9);
    bar();
#pragma unroll 1
    for (int q = 0; q < 16; ++q) { const int qq = (q + rot) & 15; dma16(rwo, wov, (u32)((128 * cw + 8 * qq) * 1024 + cb * 128), Lb + (u32)(L_WO + (16 * cw + qq) * 1024)); }
#pragma unroll
    for (int q = 0; q < 2; ++q) dma16(rx, (u32)((lane >> 3) * 1024 + (lane & 7) * 16), (u32)((64 * tt + 16 * cw + 8 * q) * 1024 + cb * 128), Lb + (u32)(L_XRES + (2 * cw + q) * 1024));
    const u32 p1v = (u32)((lane >> 3) * 3584 + ((((lane >> 1) & 3) ^ ((lane >> 4) & 1)) * 32) + (lane & 1) * 16);
    auto dma_p1 = [&](int k) {
      const u32 so = (u32)((32 * ((k + rotg) & 15) + 8 * cw) * 3584 + jt * 224 + p0 * 32);
      dma16(rwg, p1v, so, Lb + (u32)(k * L_P1B + cw * 1024));
      dma16(rwu, p1v ^ 64u, so, Lb + (u32)(k * L_P1B + 4096 + cw * 1024));
    };
    dma_p1(0); dma_p1(1);
    waitv<4>(); STAMP(10); bar();
#pragma unroll 1
    for (int k = 2; k < 8; ++k) dma_p1(k);
    {
      asm volatile("" : "+v"(gs_.q[0]), "+v"(gs_.q[1]), "+v"(gs_.q[2]), "+v"(gs_.q[3]), "+v"(gc_.q[0]), "+v"(gc_.q[1]), "+v"(gc_.q[2]), "+v"(gc_.q[3]));
      asm volatile("" : "+v"(gg_.q[0]), "+v"(gg_.q[1]), "+v"(gg_.q[2]), "+v"(gg_.q[3]));
      if (!__all(gath_ok(gs_, glo, ghi) && gath_ok(gc_, glo, ghi) && gath_ok(gg_, glo, ghi))) {
        const u64 t0 = __builtin_amdgcn_s_memrealtime();
        for (;;) {
          gath_load(gs_, rws, (u32)(t4 * 16), 3 * 4096u); gath_load(gc_, rws, (u32)(t4 * 16), 4 * 4096u);
          gath_load(gg_, rws, (u32)(tok * 16 + h * 3 * 4096), 2 * 4096u + (u32)cb * 512u);
          if (__all(gath_ok(gs_, glo, ghi) && gath_ok(gc_, glo, ghi) && gath_ok(gg_, glo, ghi))) break;
          if (spin_out(t0)) break;
        }
      }
      const u32 sb2 = gath_val(gs_), cb2 = gath_val(gc_), gg2 = gath_val(gg_);
      const float g2a = rbf(1.f + bflo(cb2)), g2b = rbf(1.f + bfhi(cb2));
      const int kk = (2 * t4) & 31;
      LAS char* gd = L + L_GS + (t4 >> 4) * 128 + ((kk >> 2) & 3) * 32 + ((((kk >> 4) << 2) | (kk & 3)) * 2);
      *(LAS u32*)gd = pk2(g2a, g2b);
      *(LAS u32*)(gd + 16) = sb2;
      if ((t4 >> 5) == cb) *(LAS f32x2*)(L + L_G2C + tok * 8) = f32x2{g2a, g2b};
      if (cw == 0) *(LAS f32x2*)(L + (h ? L_GBV : L_GA) + tok * 8) = f32x2{bflo(gg2), bfhi(gg2)};
    }
    waitv<0>(); STAMP(11); bar();
#pragma unroll 1
    for (int k = 8; k < 16; ++k) dma_p1(k);
    bar();
    u32 p2v[3];
#pragma unroll
    for (int i = 0; i < 3; ++i) { const int n = 64 * i + lane, row = n / 6, c = n - row * 6; p2v[i] = (u32)(row * 3584 + c * 16 + ((c >> 1) >= p0 ? 128 : 0)); }
    const rsrc_t rgu = (cw & 1) ? rwu : rwg;
    auto dma_p2 = [&](int k0, int k1) {
#pragma unroll 1
      for (int k = k0; k < k1; ++k) {
        const int pos = 2 * k + (cw >> 1);
#pragma unroll
        for (int i = 0; i < 3; ++i) dma16(rgu, p2v[i], (u32)(((pos + rotg) & 15) * 32 * 3584 + jt * 224), Lb + (u32)(pos * L_P2B + (cw & 1) * L_P2M + i * 1024));
      }
    };
    auto dma_wd = [&](int u) {
#pragma unroll
      for (int q = 0; q < 8; ++q) {
        const int n = 8 * (u - 4) + q;
        const int jj = ((u < 4 ? u : n / 6) + rotd) & 3, r6 = n % 6;
        const int row = u < 4 ? 112 * jj + 16 * jj + 8 * q : 112 * jj + 8 * r6 + ((r6 >> 1) >= jj ? 64 : 0);
        dma16(rwd, wov, (u32)((448 * cw + row) * 1024 + cb * 128), Lb + (u32)(wd_unit_off(u) + cw * 8192 + q * 1024));
      }
    };
    f32x4 ua, ub;
    auto ut_blocks = [&](LAS char* ba, LAS char* bb, int bstride, int tstride, int k0, int k1) {
      u32x4 fa[8], fb[8], gv[8];
#pragma unroll
      for (int k = k0; k < k1; ++k) {
        fa[k - k0] = trd2(ba + k * bstride, tstride); fb[k - k0] = trd2(bb + k * bstride, tstride);
        gv[k - k0] = *(const LAS u32x4*)(L + L_GS + ((k + rotg) & 15) * 128 + g * 32 + (lane & 1) * 16);
      }
      SB();
#pragma unroll
      for (int k = k0; k < k1; ++k) { ua = mfma16(gv[k - k0], fa[k - k0], ua); ub = mfma16(gv[k - k0], fb[k - k0], ub); }
      asm volatile("" : "+v"(ua), "+v"(ub));
    };
    auto put_fix = [&](const f32x4& v, int slot, int m) {
      const u32 hi = pk2(v[0], v[1]), lo = pk2(v[0] - bflo(hi), v[1] - bfhi(hi));
      const u32 u1 = hi & 0xffffu, t1 = hi >> 16, u2 = lo & 0xffffu, t2 = lo >> 16;
      const u32x4 o = u32x4{u1 | (u1 << 16), u2 | (u2 << 16), t1 | (t1 << 16), t2 | (t2 << 16)};
      if (lane < 16) { LAS char* d = L + L_FIX + slot * 1024 + (16 * m + lane) * 16; *(LAS u32x4*)d = o; *(LAS u32x4*)(d + 512) = o; }
    };
    LAS char* u1a = L + ((4 * g + q4) * 128 + (lane & 3) * 8 + ((cw ^ (q4 >> 1)) * 32));
    LAS char* u1b = L + (4096 + (4 * g + q4) * 128 + (lane & 3) * 8 + ((cw ^ (q4 >> 1) ^ 2) * 32));
    ua = f32x4{0, 0, 0, 0}; ub = f32x4{0, 0, 0, 0};
    ut_blocks(u1a, u1b, L_P1B, 2048, 0, 8);
    STAMP(12); bar();
    dma_p2(0, 2);
    waitv<6>(); STAMP(13); bar();
    dma_p2(2, 5);
    ut_blocks(u1a, u1b, L_P1B, 2048, 8, 12);
    STAMP(14); bar();
    dma_p2(5, 7);
    ut_blocks(u1a, u1b, L_P1B, 2048, 12, 16);
    put_fix(ua, cw, 0); put_fix(ub, cw, 1);
    LAS char* u2a = L + ((cw & 1) * L_P2M + (4 * g + q4) * 96 + (lane & 3) * 8 + (cw >> 1) * 32);
    LAS char* u2b = L + ((cw & 1) * L_P2M + (4 * g + q4) * 96 + (lane & 3) * 8 + 64);
    ua = f32x4{0, 0, 0, 0}; ub = f32x4{0, 0, 0, 0};
    waitv<15>(); STAMP(15); bar();
    dma_p2(7, 8);
    dma_wd(0);
    bar();
    ut_blocks(u2a, u2b, L_P2B, 1536, 0, 4);
    waitv<17>(); STAMP(16); bar();
    ut_blocks(u2a, u2b, L_P2B, 1536, 4, 10);
    waitv<8>(); bar();
    dma_wd(1);
    ut_blocks(u2a, u2b, L_P2B, 1536, 10, 13);
    bar();
    dma_wd(2);
    ut_blocks(u2a, u2b, L_P2B, 1536, 13, 16);
    put_fix(ua, cw >> 1, cw & 1); if (cw < 2) put_fix(ub, 2, cw & 1);
    waitv<16>(); bar();
    dma_wd(3);
    waitv<16>(); STAMP(17); bar();
    dma_wd(4);
    waitv<16>(); bar();
    dma_wd(5);
    waitv<16>(); bar();
    dma_wd(6);
    waitv<16>(); STAMP(18); bar();
    waitv<8>(); bar();
    waitv<0>(); bar();
    bar();
  } else {
    LAS char* const CP = L + L_CP; LAS char* const HP = L + L_HP; LAS char* const myCP = CP + cw;
    u32 hv;
    f32x4 Kt[5], Qt[4], Vn[5];
#pragma unroll
    for (int r = 0; r < 5; ++r) { Kt[r] = f32x4{0, 0, 0, 0}; Vn[r] = f32x4{0, 0, 0, 0}; if (r < 4) Qt[r] = f32x4{0, 0, 0, 0}; }
    {
      u32x4 a[2][5], wl[2][3];
      LAS char* const w1a = opq(w1rd + L_RW); LAS char* const w1b = opq(w1rd + L_RW + 4 * L_W1S);
      auto ld = [&](int n, int bi) {
        LAS char* ab = L + L_AIMG + ((n + rot) & 15) * 1024 + g * 256 + ((i16 ^ (((n & 1) << 2) | g)) * 16);
#pragma unroll
        for (int rb = 0; rb < 5; ++rb) a[bi][rb] = *(const LAS u32x4*)(ab + rb * 16384);
        if (n < 6 || n >= 12) {
#pragma unroll
          for (int pp = 0; pp < 3; ++pp) wl[bi][pp] = trd2((n % 6 < 4 ? w1a : w1b) + (n % 6 % 4) * L_W1S + pp * 4096, 512);
        }
      };
      ld(0, 0); SB();
#pragma unroll
      for (int n = 0; n < 16; ++n) {
        if (n == 3) bar();
        if (n == 11) WACC(24, bar());
        if (n < 15) ld(n + 1, (n + 1) & 1);
        const bool rg = n >= 6 && n < 12;
        const u32x4 wq = rg ? wr[rg ? 3 * (n - 6) : 0] : wl[n & 1][0], wk = rg ? wr[rg ? 3 * (n - 6) + 1 : 0] : wl[n & 1][1], wv = rg ? wr[rg ? 3 * (n - 6) + 2 : 0] : wl[n & 1][2];
#pragma unroll
        for (int rb = 0; rb < 5; ++rb) {
          Kt[rb] = mfma16(wk, a[n & 1][rb], Kt[rb]);
          Vn[rb] = mfma16(a[n & 1][rb], wv, Vn[rb]);
          if (rb > 0) Qt[rb - 1] = mfma16(wq, a[n & 1][rb], Qt[rb - 1]);
        }
#pragma unroll
        for (int k = 0; k < 11; ++k) { SGB_MFMA(1); SGB_DSR(1); }
        SGB_MFMA(3);
        SB();
      }
    }
    bar();
    STAMP(8);
    const int tb = cw & 1, kh = cw >> 1;
    {
      u32x2 kp[5], vp[5];
#pragma unroll
      for (int r = 0; r < 5; ++r) { kp[r] = u32x2{pk2(Kt[r][0], Kt[r][1]), pk2(Kt[r][2], Kt[r][3])}; vp[r] = u32x2{pk2(Vn[r][0], Vn[r][1]), pk2(Vn[r][2], Vn[r][3])}; }
      f32x4 m0, m1, m0f;
#pragma unroll
      for (int r = 0; r < 4; ++r) {
        const int d1 = i16 - 4 * g - r, d0 = d1 + 16;
        m0[r] = (d0 <= 14 && !(d0 & 1)) ? 0.f : -1e30f;
        m1[r] = (d1 >= 0 && d1 <= 14 && !(d1 & 1)) ? 0.f : -1e30f;
        m0f[r] = tt == 0 ? -1e30f : m0[r];
      }
      u32x2 op[4];
      const u32x4 ones = u32x4{0x3f803f80u, 0x3f803f80u, 0x3f803f80u, 0x3f803f80u};
#pragma unroll
      for (int qb = 0; qb < 4; ++qb) {
        const u32x2 qp = u32x2{pk2(Qt[qb][0], Qt[qb][1]), pk2(Qt[qb][2], Qt[qb][3])};
        const f32x4 s0 = mfma16k16(kp[qb], qp, qb == 0 ? m0f : m0), s1 = mfma16k16(kp[qb + 1], qp, m1);
        float mx = fmaxf(fmaxf(fmaxf(s0[0], s0[1]), fmaxf(s0[2], s0[3])), fmaxf(fmaxf(s1[0], s1[1]), fmaxf(s1[2], s1[3])));
        mx = swap16_max(mx); mx = swap32_max(mx);
        const float cs = 0.25f * 1.44269504088896f, nm = -mx * cs;
        float e0[4], e1[4];
#pragma unroll
        for (int r = 0; r < 4; ++r) { e0[r] = __builtin_amdgcn_exp2f(fmaf(s0[r], cs, nm)); e1[r] = __builtin_amdgcn_exp2f(fmaf(s1[r], cs, nm)); }
        const u32x4 pf = u32x4{pk2(e0[0], e0[1]), pk2(e0[2], e0[3]), pk2(e1[0], e1[1]), pk2(e1[2], e1[3])};
        const f32x4 z = f32x4{0, 0, 0, 0};
        const f32x4 ot = mfma16(u32x4{vp[qb][0], vp[qb][1], vp[qb + 1][0], vp[qb + 1][1]}, pf, z);
        const f32x4 sm = mfma16(ones, pf, z);
        const float ri = __builtin_amdgcn_rcpf(sm[0]);
        op[qb] = u32x2{pk2(ot[0] * ri, ot[1] * ri), pk2(ot[2] * ri, ot[3] * ri)};
      }
#pragma unroll
      for (int t2 = 0; t2 < 2; ++t2) {
        auto r0 = __builtin_amdgcn_permlane16_swap(op[2 * t2][0], op[2 * t2 + 1][0], false, false);
        auto r1 = __builtin_amdgcn_permlane16_swap(op[2 * t2][1], op[2 * t2 + 1][1], false, false);
        const u32 a0 = r0[0], b0 = r0[1], a1 = r1[0], b1 = r1[1];
        st128_sc1(u32x4{a0, a1, b0, b1}, rws, (u32)(lane * 16), WS_O + (u32)(((2 * tt + t2) * 32 + 4 * hg + cw) * 1024));
      }
    }
    STAMP(9);
    pubbar<0>(rws, WS_FO + (u32)(tt * 256 + hg * 8), tlo, thi, tid);
    STAMP(10);
    wait_flags<8>(rws, WS_FO + (u32)(tt * 256), lane, tlo, thi);
    STAMP(11);
    u32 x1p[8];
    {
      u32x4 of[16];
      const u32 ob = WS_O + (u32)(((2 * tt + tb) * 32 + 16 * kh) * 1024);
#pragma unroll
      for (int s = 0; s < 16; ++s) { of[s] = ld128_sc1(rws, (u32)(lane * 16), ob + (u32)(((s + 4 * hgl) & 15) * 1024)); SB(); }
      SB();
      LAS char* q0 = L + (L_WO + (256 * kh + 8 * h + q4) * 128 + mrow * 32 + (lane & 3) * 8);
      LAS char* qa = q0 + ((q4 >> 1) ^ kh) * 64; LAS char* qb = q0 + ((q4 >> 1) ^ kh ^ 1) * 64;
      f32x16 a0, a1;
#pragma unroll
      for (int r = 0; r < 16; ++r) { a0[r] = 0.f; a1[r] = 0.f; }
      u32x4 wl[2][2];
      wl[0][0] = trd2(qa + hgl * 8192, 512); wl[0][1] = trd2(qb + hgl * 8192, 512); SB();
#pragma unroll
      for (int s = 0; s < 16; ++s) {
        if (s < 15) { const int o2 = ((((s + 1) >> 2) + hgl) & 3) * 8192 + ((s + 1) & 3) * 2048; wl[(s + 1) & 1][0] = trd2(qa + o2, 512); wl[(s + 1) & 1][1] = trd2(qb + o2, 512); }
        a0 = mfma32(wl[s & 1][0], of[s], a0); a1 = mfma32(wl[s & 1][1], of[s], a1);
        SGB_MFMA(1); SGB_DSR(2); SGB_MFMA(1); SGB_DSR(2);
        SB();
      }
      u32x2 xres[4];
#pragma unroll
      for (int q = 0; q < 4; ++q) xres[q] = *(const LAS u32x2*)(L + L_XRES + (32 * tb + tok) * 128 + (32 * kh + 8 * q + 4 * h) * 2);
      const f32x16 snd = a1, acc = a0;
#pragma unroll
      for (int q = 0; q < 4; ++q) *(LAS f32x4*)(L + L_RED2 + cw * 4096 + q * 1024 + lane * 16) = f32x4{snd[4 * q], snd[4 * q + 1], snd[4 * q + 2], snd[4 * q + 3]};
      STAMP(19);
      bar();
      STAMP(20);
      const float mu1 = ((const LAS float*)(L + L_MU1))[32 * tb + tok];
      float s1 = 0.f, s2 = 0.f; u32 yp[8];
#pragma unroll
      for (int q = 0; q < 4; ++q) {
        const f32x4 o4 = *(const LAS f32x4*)(L + L_RED2 + (cw ^ 2) * 4096 + q * 1024 + lane * 16);
        const u32x2 xq = xres[q];
        const f32x4 ga4 = *(const LAS f32x4*)(L + L_GA + (32 * kh + 8 * q + 4 * h) * 4), g24 = *(const LAS f32x4*)(L + L_G2C + (32 * kh + 8 * q + 4 * h) * 4);
        const float v0 = fmaf(ga4[0], acc[4 * q] + o4[0], bflo(xq[0])), v1 = fmaf(ga4[1], acc[4 * q + 1] + o4[1], bfhi(xq[0]));
        const float v2 = fmaf(ga4[2], acc[4 * q + 2] + o4[2], bflo(xq[1])), v3 = fmaf(ga4[3], acc[4 * q + 3] + o4[3], bfhi(xq[1]));
        x1p[2 * q] = pk2(v0, v1); x1p[2 * q + 1] = pk2(v2, v3);
        const float d0 = bflo(x1p[2 * q]) - mu1, d1 = bfhi(x1p[2 * q]) - mu1, d2 = bflo(x1p[2 * q + 1]) - mu1, d3 = bfhi(x1p[2 * q + 1]) - mu1;
        s1 += (d0 + d1) + (d2 + d3); s2 = fmaf(d0, d0, fmaf(d1, d1, fmaf(d2, d2, fmaf(d3, d3, s2))));
        yp[2 * q] = pk2(d0 * g24[0], d1 * g24[1]); yp[2 * q + 1] = pk2(d2 * g24[2], d3 * g24[3]);
      }
      const u32 yb = WS_Y + (u32)(((2 * tt + tb) * 32 + 4 * cb + 2 * kh) * 1024);
      st128_sc1(u32x4{yp[0], yp[1], yp[2], yp[3]}, rws, (u32)(lane * 16), yb);
      st128_sc1(u32x4{yp[4], yp[5], yp[6], yp[7]}, rws, (u32)(lane * 16), yb + 1024);
      s1 = swap32_add(s1, s1); s2 = swap32_add(s2, s2);
      if (lane < 32) st64_sc1(u32x2{u_(s1), u_(s2)}, rws, (u32)(tok * 8), WS_ST + (u32)(((2 * tt + tb) * 16 + 2 * cb + kh) * 256));
    }
#pragma unroll
    for (int q = 0; q < 8; q += 4) asm volatile("" : "+v"(x1p[q]), "+v"(x1p[q + 1]), "+v"(x1p[q + 2]), "+v"(x1p[q + 3]));
    STAMP(12);
    pubbar<0>(rws, WS_FY + (u32)(tt * 256 + cb * 8), tlo, thi, tid);
    STAMP(13);
    wait_flags<8>(rws, WS_FY + (u32)((2 * T + (cw >> 1)) * 256), lane, tlo, thi);
    STAMP(14);
    const int tb32 = 4 * T + cw;
    u32x2 stv[8];
#pragma unroll
    for (int q = 0; q < 8; ++q) stv[q] = ld64_sc1(rws, (u32)(tok * 8 + h * 2048), WS_ST + (u32)((tb32 * 16 + q) * 256));
    u32x4 yf[32];
    auto ldy = [&](int s) { yf[s] = ld128_sc1(rws, (u32)(lane * 16), WS_Y + (u32)((tb32 * 32 + ((s + 2 * rotg) & 31)) * 1024)); };
#pragma unroll
    for (int s = 0; s < (EXPW ? 32 : 10); ++s) { ldy(s); SB(); }
    SB();
    u32 mz, sz; float r2;
    {
      float s1 = 0.f, s2 = 0.f;
#pragma unroll
      for (int q = 0; q < 8; ++q) { s1 += f_(stv[q][0]); s2 += f_(stv[q][1]); }
      s1 = swap32_add(s1, s1); s2 = swap32_add(s2, s2);
      const float md = s1 * (1.f / 512.f);
      const float ve = fmaxf(s2 * (1.f / 512.f) - md * md, 0.f) + 1e-5f;
      const float sg = __builtin_amdgcn_sqrtf(ve); r2 = __builtin_amdgcn_rcpf(ve);
      const u32 hi = pk2(-md, sg), lo = pk2(-md - bflo(hi), sg - bfhi(hi));
      const u32 mm = (hi & 0xffffu) | (lo << 16), ss = (hi >> 16) | (lo & 0xffff0000u);
      mz = h ? 0u : mm; sz = h ? 0u : ss;
      asm volatile("" : "+v"(mz), "+v"(sz), "+v"(r2));
    }
    SB();
    auto act_store = [&](const f32x16& ac, int pp) {
      u32x4 o;
#pragma unroll
      for (int e = 0; e < 4; ++e) o[e] = pk2(fmaxf(ac[2 * e], 0.f) * r2 * ac[8 + 2 * e], fmaxf(ac[2 * e + 1], 0.f) * r2 * ac[9 + 2 * e]);
      st128_sc1(o, rws, (u32)(lane * 16), WS_ACT + (u32)((tb32 * 112 + 7 * jt + pp) * 1024));
    };
    {
      f32x16 acc[4];
#pragma unroll
      for (int q = 0; q < 4; ++q)
#pragma unroll
        for (int r = 0; r < 16; ++r) acc[q][r] = 0.f;
      LAS char* pa[4];
#pragma unroll
      for (int pp = 0; pp < 4; ++pp) pa[pp] = L + (p1base + (((u32)pp ^ p1x) * 32));
      u32x4 wf[2][4];
      auto ldw = [&](int st, int bi) {
#pragma unroll
        for (int pp = 0; pp < 4; ++pp) { if (st == 16) pa[pp] = opq(pa[pp] + 8 * L_P1B); wf[bi][pp] = trd2(pa[pp] + ((st >> 1) & 7) * L_P1B + (st & 1) * 2048, 1024); }
      };
      ldw(0, 0); SB();
#pragma unroll
      for (int st = 0; st < 32; ++st) {
        if (st == 7 || st == 15 || st == 23) WACC(25, bar());
        if (st + 1 < 32) ldw(st + 1, (st + 1) & 1);
        if (!EXPW && st + 10 < 32) ldy(st + 10);
#pragma unroll
        for (int pp = 0; pp < 4; ++pp) acc[pp] = mfma32(wf[st & 1][pp], yf[st], acc[pp]);
#pragma unroll
        for (int pp = 0; pp < 4; ++pp) { SGB_MFMA(1); SGB_DSR(2); }
        SB();
      }
      WACC(26, bar());
#pragma unroll
      for (int pp = 0; pp < 4; ++pp) acc[pp] = mfma32(*(const LAS u32x4*)(L + L_FIX + pp * 1024 + lane * 16), u32x4{mz, mz, sz, sz}, acc[pp]);
      STAMP(15);
#pragma unroll
      for (int pp = 0; pp < 4; ++pp) act_store(acc[pp], p0 + pp);
    }
    u32x4 af[16][2]; u32x2 fl;
    auto lda = [&](int n) { const int s = n >> 1, t2 = n & 1; af[s][t2] = ld128_sc1(rws, (u32)(lane * 16), WS_ACT + (u32)(((2 * tt + (t2 ^ tb)) * 112 + 28 * cw + 8 * (((s >> 2) + rotd) & 3) + (s & 3)) * 1024)); };
    {
      f32x16 acc[3];
#pragma unroll
      for (int q = 0; q < 3; ++q)
#pragma unroll
        for (int r = 0; r < 16; ++r) acc[q][r] = 0.f;
      LAS char* pb = L + p2base;
      u32x4 wf[2][3];
      auto ldw = [&](int st, int bi) {
        if (st == 16) pb = opq(pb + 8 * L_P2B);
#pragma unroll
        for (int pp = 0; pp < 3; ++pp) wf[bi][pp] = trd2(pb + ((st >> 1) & 7) * L_P2B + (st & 1) * 1536 + pp * 32, 768);
      };
      ldw(0, 0); SB();
#pragma unroll
      for (int st = 0; st < 32; ++st) {
        if (st == 3) WACC(30, pubbar<0>(rws, WS_FA + (u32)(T * 256 + jt * 8), tlo, thi, tid));
        if (st == 7 || st == 15 || st == 23) WACC(27, bar());
        if (st == FA1P) fl = peek_flags<4>(rws, WS_FA + (u32)(T * 256 + cw * 32), lane);
        if (st == FA1P + 4) { STAMP(23); WACC(28, wait_flags_from<4>(fl, rws, WS_FA + (u32)(T * 256 + cw * 32), lane, tlo, thi)); }
        if (st + 1 < 32) ldw(st + 1, (st + 1) & 1);
        if (st >= FA1P + 4) lda(st - FA1P - 4);
#pragma unroll
        for (int pp = 0; pp < 3; ++pp) acc[pp] = mfma32(wf[st & 1][pp], yf[st], acc[pp]);
#pragma unroll
        for (int pp = 0; pp < 3; ++pp) { SGB_MFMA(1); SGB_DSR(2); }
        SB();
      }
      WACC(27, bar());
#pragma unroll
      for (int pp = 0; pp < 3; ++pp) acc[pp] = mfma32(*(const LAS u32x4*)(L + L_FIX + pp * 1024 + lane * 16), u32x4{mz, mz, sz, sz}, acc[pp]);
      STAMP(16);
#pragma unroll
      for (int pp = 0; pp < 3; ++pp) act_store(acc[pp], pp >= p0 ? pp + 4 : pp);
#pragma unroll
      for (int n = 28 - FA1P; n < 24; ++n) { lda(n); SB(); }
    }
    SB();
    {
      f32x16 acc[4];
#pragma unroll
      for (int q = 0; q < 4; ++q)
#pragma unroll
        for (int r = 0; r < 16; ++r) acc[q][r] = 0.f;
      LAS char* d0 = L + (cw * 8192 + (4 * h + q4) * 128 + mrow * 32 + (lane & 3) * 8);
      LAS char* da = d0 + ((q4 >> 1) ^ kh) * 64; LAS char* db = d0 + ((q4 >> 1) ^ kh ^ 1) * 64;
      u32x4 wl[2][2], a2[12][2];
      auto lda2 = [&](int n) {
        const int s2 = n >> 1, t2 = n & 1, jj = (s2 / 3 + rotd) & 3, pp = s2 % 3;
        a2[s2][t2] = ld128_sc1(rws, (u32)(lane * 16), WS_ACT + (u32)(((2 * tt + (t2 ^ tb)) * 112 + 28 * cw + 7 * jj + (pp >= jj ? pp + 4 : pp)) * 1024));
      };
      LAS char* const dah = opq(da + 65536); LAS char* const dbh = opq(db + 65536);
      auto ldw = [&](int s, int bi) { const u32 o = wd_unit_off(s >> 2) + (s & 3) * 2048; wl[bi][0] = trd2(o < 65536 ? da + o : dah + (o - 65536), 1024); wl[bi][1] = trd2(o < 65536 ? db + o : dbh + (o - 65536), 1024); };
      ldw(0, 0); SB();
#pragma unroll
      for (int s = 0; s < 28; ++s) {
        if (s == 3) WACC(30, pubbar<FA1P - 4 + 3>(rws, WS_FA2 + (u32)(T * 256 + jt * 8), tlo, thi, tid));
        if ((s & 3) == 3 && s > 3 && s < 24) WACC(29, bar());
        if (s == 12) {
          STAMP(17);
          wait_flags_from<4>(fl, rws, WS_FA2 + (u32)(T * 256 + cw * 32), lane, tlo, thi);
          STAMP(21);
#pragma unroll
          for (int n = 0; n < 12; ++n) { lda2(n); SB(); }
          SB();
        }
        if (s < 8) lda(24 + s);
        if (s == 8) fl = peek_flags<4>(rws, WS_FA2 + (u32)(T * 256 + cw * 32), lane);
        if (s >= 13 && s < 19) { lda2(12 + 2 * (s - 13)); SB(); lda2(13 + 2 * (s - 13)); }
        if (s + 1 < 28) ldw(s + 1, (s + 1) & 1);
        const u32x4 b0 = s < 16 ? af[s < 16 ? s : 0][0] : a2[s < 16 ? 0 : s - 16][0], b1 = s < 16 ? af[s < 16 ? s : 0][1] : a2[s < 16 ? 0 : s - 16][1];
        acc[0] = mfma32(wl[s & 1][0], b0, acc[0]); acc[2] = mfma32(wl[s & 1][1], b0, acc[2]);
        acc[1] = mfma32(wl[s & 1][0], b1, acc[1]); acc[3] = mfma32(wl[s & 1][1], b1, acc[3]);
        SGB_MFMA(1); SGB_DSR(2); SGB_MFMA(1); SGB_DSR(2); SGB_MFMA(2);
        SB();
      }
      STAMP(22);
      int ln2 = lane; asm volatile("" : "+v"(ln2));
#pragma unroll
      for (int i = 1; i < 4; ++i)
#pragma unroll
        for (int q = 0; q < 4; ++q) *(LAS f32x4*)(L + L_RED4 + ((i ^ cw) * 3 + i - 1) * 4096 + q * 1024 + ln2 * 16) = f32x4{acc[i][4 * q], acc[i][4 * q + 1], acc[i][4 * q + 2], acc[i][4 * q + 3]};
      const f32x16 mine = acc[0];
      bar();
      u32 ob[8];
#pragma unroll
      for (int q = 0; q < 4; ++q) {
        const LAS char* rp = L + L_RED4 + cw * 3 * 4096 + q * 1024 + ln2 * 16;
        const f32x4 r0 = *(const LAS f32x4*)rp, r1 = *(const LAS f32x4*)(rp + 4096), r2_ = *(const LAS f32x4*)(rp + 8192);
        const f32x4 gb4 = *(const LAS f32x4*)(L + L_GBV + (32 * kh + 8 * q + 4 * (ln2 >> 5)) * 4);
        float v[4];
#pragma unroll
        for (int e = 0; e < 4; ++e) v[e] = (mine[4 * q + e] + r0[e]) + (r1[e] + r2_[e]);
        ob[2 * q] = pk2(fmaf(gb4[0], v[0], bflo(x1p[2 * q])), fmaf(gb4[1], v[1], bfhi(x1p[2 * q])));
        ob[2 * q + 1] = pk2(fmaf(gb4[2], v[2], bflo(x1p[2 * q + 1])), fmaf(gb4[3], v[3], bfhi(x1p[2 * q + 1])));
      }
#pragma unroll
      for (int m = 0; m < 2; ++m) {
        auto r0 = __builtin_amdgcn_permlane32_swap(ob[4 * m], ob[4 * m + 2], false, false);
        auto r1 = __builtin_amdgcn_permlane32_swap(ob[4 * m + 1], ob[4 * m + 3], false, false);
        const u32 a0 = r0[0], b0 = r0[1], a1 = r1[0], b1 = r1[1];
        __builtin_amdgcn_raw_buffer_store_b128(u32x4{a0, a1, b0, b1}, rout, (u32)((64 * tt + 32 * tb + (ln2 & 31)) * 1024 + (64 * cb + 32 * kh + 16 * m + 8 * (ln2 >> 5)) * 2), 0, 16);
      }
    }
    STAMP(18);
    if (b == 0 && cw == 0) {
      const u64 t0 = __builtin_amdgcn_s_memrealtime();
      for (;;) {
        bool ok = true;
#pragma unroll
        for (int k = 0; k < 4; ++k) { const int f = 64 * k + lane; const u32x2 v = ld64_sc1(rws, (u32)((f >> 3) * 256 + (f & 7) * 8), WS_FO); ok = ok && v[0] == tlo && v[1] == thi; }
        if (__all(ok)) break;
        if (spin_out(t0)) break;
      }
      if (lane == 0) st64_sc1(u32x2{tlo, thi}, rws, 0u, WS_E);
    }
  }
  if (isC && cw == 0 && b == ENCB) {
    const u32 target = stamp[0] + 6000u + (stamp[ENC] - stamp[0]);
    for (int sp = 0; sp < 2000000; ++sp) { if ((int)((u32)__builtin_amdgcn_s_memrealtime() - target) >= 0) break; __builtin_amdgcn_s_sleep(1); }
  }
}

__global__ __launch_bounds__(512, 2) void fused_block(Params p) {
  extern __shared__ __attribute__((aligned(16))) char smem_[];
  asm volatile("" :: "s"(p.x), "s"(p.wqkv), "s"(p.wo), "s"(p.wg), "s"(p.wu), "s"(p.wd), "s"(p.c), "s"(p.wmod), "s"(p.out), "s"(p.ws));
  int w = __builtin_amdgcn_readfirstlane(threadIdx.x >> 6);
  u32 stamp[31];
#pragma unroll
  for (int i = 0; i < 31; ++i) stamp[i] = 0;
  u32x4 wr[18]; u32 tlo, thi;
  front(p, (LAS char*)smem_, threadIdx.x, w, blockIdx.x, wr, tlo, thi STAMP_ARG);
  if (w >= 4) {
    int b = blockIdx.x, tid = threadIdx.x; asm volatile("; H role" : "+s"(b), "+s"(w)); asm volatile("; H role" : "+v"(tid));
    role_body<false>(p, (LAS char*)smem_, tid, w, b, wr, tlo, thi STAMP_ARG);
  } else {
    int b = blockIdx.x, tid = threadIdx.x; asm volatile("; C role" : "+s"(b), "+s"(w)); asm volatile("; C role" : "+v"(tid));
    role_body<true>(p, (LAS char*)smem_, tid, w, b, wr, tlo, thi STAMP_ARG);
  }
}

extern "C" void kernel_launch(void* const* d_in, const int* in_sizes, int n_in, void* d_out, int out_size, void* d_ws, size_t ws_size, hipStream_t stream) {
  Params p;
  p.x = d_in[0]; p.wqkv = d_in[1]; p.wo = d_in[2]; p.wg = d_in[4]; p.wu = d_in[5]; p.wd = d_in[6]; p.c = d_in[7]; p.wmod = d_in[8];
  p.out = d_out; p.ws = d_ws;
  (void)hipFuncSetAttribute((const void*)fused_block, hipFuncAttributeMaxDynamicSharedMemorySize, LDS_BYTES);
  fused_block<<<256, 512, LDS_BYTES, stream>>>(p);
}
```

```cpp
#include <hip/hip_runtime.h>
#include <stdint.h>

typedef unsigned short u16;
typedef unsigned u32;
typedef unsigned long long u64;
typedef u32 u32x2 __attribute__((ext_vector_type(2)));
typedef u32 u32x4 __attribute__((ext_vector_type(4)));
typedef short s16x4 __attribute__((ext_vector_type(4)));
typedef short s16x8 __attribute__((ext_vector_type(8)));
typedef __bf16 bf16x2 __attribute__((ext_vector_type(2)));
typedef __bf16 bf16x8 __attribute__((ext_vector_type(8)));
typedef float f32x2 __attribute__((ext_vector_type(2)));
typedef float f32x4 __attribute__((ext_vector_type(4)));
typedef float f32x16 __attribute__((ext_vector_type(16)));
typedef __amdgpu_buffer_rsrc_t rsrc_t;
#define LAS __attribute__((address_space(3)))
#define DI __device__ __forceinline__

#define EXPW 0
#define TOUCH 30
#define THR 4
constexpr unsigned CODE_BYTES = 50000;
#define NTX 1
#define FA1P 16
#define OPT 0
#define ELIM 0
#define DIAG 0
#define XMAP 2

constexpr int S_ = 2048, D_ = 512, F_ = 1792;
constexpr u32 WS_E = 0, WS_MODG = 393216, WS_FU = 57344, WS_FO = 8192, WS_FY = 983040, WS_FA = 327680, WS_FA2 = 360448, WS_DBG = 65536;
constexpr u32 WS_UT = 524288;
constexpr u32 WS_O = 1u << 20, WS_Y = 3u << 20, WS_ST = 5u << 20, WS_ACT = 6u << 20, WS_BYTES = 13u << 20;
constexpr u32 L_AIMG = 0, L_RW = 81920, L_W1S = 12288;
constexpr u32 L_LNP = 73728, L_PRED = L_LNP + 6400, L_RSNB = L_PRED + 1088;
constexpr u32 L_XRES = 65536, L_WO = 73728, L_RED2 = 139264;
constexpr u32 L_P1B = 8192, L_P2B = 6400, L_P2M = 3200;
constexpr u32 L_RED4 = 65536;
constexpr u32 L_MISC = 155648, L_G1 = L_MISC, L_S1 = L_MISC + 2048, L_FIX = L_MISC;
constexpr u32 L_SL = L_MISC + 4096, L_SCRAP = L_MISC + 6656;
constexpr u32 L_MU1 = L_MISC + 7168, L_GA = L_MU1 + 256, L_G2C = L_GA + 256, L_GBV = L_G2C + 256;
constexpr u32 LDS_BYTES = 163840;
__host__ __device__ constexpr u32 wd_unit_off(int u) { return u == 0 || u == 4 ? 102400u : (u == 1 || u == 5 ? 0u : (u == 2 || u == 6 ? 32768u : 65536u)); }

template <int N> struct ic { static constexpr int value = N; };
struct Params { const void *x, *wqkv, *wo, *wg, *wu, *wd, *c, *wmod; void *out; void *ws; };

DI rsrc_t mk(const void* p, u32 bytes) { return __builtin_amdgcn_make_buffer_rsrc((void*)p, 0, bytes, 0x00020000); }
DI float bflo(u32 v) { return __builtin_bit_cast(float, v << 16); }
DI float bfhi(u32 v) { return __builtin_bit_cast(float, v & 0xffff0000u); }
DI u32 pk2(float a, float b) { bf16x2 r; r[0] = (__bf16)a; r[1] = (__bf16)b; return __builtin_bit_cast(u32, r); }
DI float rbf(float a) { return bflo(pk2(a, a)); }
DI f32x4 mfma16(u32x4 a, u32x4 b, f32x4 c) { return __builtin_amdgcn_mfma_f32_16x16x32_bf16(__builtin_bit_cast(bf16x8, a), __builtin_bit_cast(bf16x8, b), c, 0, 0, 0); }
DI f32x4 mfma16k16(u32x2 a, u32x2 b, f32x4 c) { return __builtin_amdgcn_mfma_f32_16x16x16bf16_1k(__builtin_bit_cast(s16x4, a), __builtin_bit_cast(s16x4, b), c, 0, 0, 0); }
DI f32x16 mfma32(u32x4 a, u32x4 b, f32x16 c) { return __builtin_amdgcn_mfma_f32_32x32x16_bf16(__builtin_bit_cast(bf16x8, a), __builtin_bit_cast(bf16x8, b), c, 0, 0, 0); }
DI u32x2 trd(LAS char* p) { return __builtin_bit_cast(u32x2, __builtin_amdgcn_ds_read_tr16_b64_v4i16((LAS s16x4*)p)); }
DI u32x4 trd2(LAS char* p, int d) { u32x2 a = trd(p), b = trd(p + d); return u32x4{a[0], a[1], b[0], b[1]}; }
DI LAS char* opq(LAS char* p) { asm volatile("" : "+v"(p)); return p; }
DI void dma16(rsrc_t rs, u32 voff, u32 soff, u32 lds) {
  asm volatile("s_mov_b32 m0, %0\n\ts_nop 0\n\tbuffer_load_dwordx4 %1, %2, %3 offen lds" :: "s"(lds), "v"(voff), "s"(rs), "s"(soff) : "m0", "memory");
}
template <int OFF> DI void ald128(u32x4& d, rsrc_t rs, u32 voff, u32 soff) {
  asm volatile("buffer_load_dwordx4 %0, %1, %2, %3 offen offset:%c4" : "=v"(d) : "v"(voff), "s"(rs), "s"(soff), "i"(OFF) : "memory");
}
DI void ald128_nt(u32x4& d, rsrc_t rs, u32 voff, u32 soff) {
  asm volatile("buffer_load_dwordx4 %0, %1, %2, %3 offen nt" : "=v"(d) : "v"(voff), "s"(rs), "s"(soff) : "memory");
}
template <int OFF> DI void ald128_sc1(u32x4& d, rsrc_t rs, u32 voff, u32 soff) {
  asm volatile("buffer_load_dwordx4 %0, %1, %2, %3 offen offset:%c4 sc1" : "=v"(d) : "v"(voff), "s"(rs), "s"(soff), "i"(OFF) : "memory");
}
DI void ald64_sc1(u32x2& d, rsrc_t rs, u32 voff, u32 soff) {
  asm volatile("buffer_load_dwordx2 %0, %1, %2, %3 offen sc1" : "=v"(d) : "v"(voff), "s"(rs), "s"(soff) : "memory");
}
DI void ald64(u32x2& d, rsrc_t rs, u32 voff, u32 soff) {
  asm volatile("buffer_load_dwordx2 %0, %1, %2, %3 offen" : "=v"(d) : "v"(voff), "s"(rs), "s"(soff) : "memory");
}
DI void warm(u32 lds, rsrc_t rs, u32 voff, u32 soff) {
  asm volatile("s_mov_b32 m0, %0\n\ts_nop 0\n\tbuffer_load_dword %1, %2, %3 offen lds" :: "s"(lds), "v"(voff), "s"(rs), "s"(soff) : "m0", "memory");
}
template <int N> DI void waitv() { asm volatile("s_waitcnt vmcnt(%c0)" :: "i"(N) : "memory"); }
template <int N> DI void waitv4(u32x4& a, u32x4& b, u32x4& c, u32x4& d) { asm volatile("s_waitcnt vmcnt(%c4)" : "+v"(a), "+v"(b), "+v"(c), "+v"(d) : "i"(N) : "memory"); }
template <int N> DI void waitv1(u32x4& a) { asm volatile("s_waitcnt vmcnt(%c1)" : "+v"(a) : "i"(N) : "memory"); }
DI void bar() { __builtin_amdgcn_sched_barrier(0); asm volatile("s_waitcnt lgkmcnt(0)\n\ts_barrier" ::: "memory"); __builtin_amdgcn_sched_barrier(0); }
DI u32x4 ld128_sc1(rsrc_t rs, u32 voff, u32 soff) { return __builtin_amdgcn_raw_buffer_load_b128(rs, voff, soff, 16); }
DI u32x2 ld64_sc1(rsrc_t rs, u32 voff, u32 soff) { return __builtin_amdgcn_raw_buffer_load_b64(rs, voff, soff, 16); }
DI void st128_sc1(u32x4 v, rsrc_t rs, u32 voff, u32 soff) { __builtin_amdgcn_raw_buffer_store_b128(v, rs, voff, soff, 16); }
DI void st64_sc1(u32x2 v, rsrc_t rs, u32 voff, u32 soff) { __builtin_amdgcn_raw_buffer_store_b64(v, rs, voff, soff, 16); }
DI float f_(u32 v) { return __builtin_bit_cast(float, v); }
DI u32 u_(float v) { return __builtin_bit_cast(u32, v); }
DI float swap16_add(float a, float b) { auto r = __builtin_amdgcn_permlane16_swap(u_(a), u_(b), false, false); u32 r0 = r[0], r1 = r[1]; return f_(r0) + f_(r1); }
DI float swap32_add(float a, float b) { auto r = __builtin_amdgcn_permlane32_swap(u_(a), u_(b), false, false); u32 r0 = r[0], r1 = r[1]; return f_(r0) + f_(r1); }
DI float swap16_max(float a) { auto r = __builtin_amdgcn_permlane16_swap(u_(a), u_(a), false, false); u32 r0 = r[0], r1 = r[1]; return fmaxf(f_(r0), f_(r1)); }
DI float swap32_max(float a) { auto r = __builtin_amdgcn_permlane32_swap(u_(a), u_(a), false, false); u32 r0 = r[0], r1 = r[1]; return fmaxf(f_(r0), f_(r1)); }
template <int CTRL> DI float dpp_add(float a) { return a + f_(__builtin_amdgcn_update_dpp(0, u_(a), CTRL, 0xf, 0xf, true)); }
DI float dot2(u32 a, u32 b, float c) { return __builtin_amdgcn_fdot2_f32_bf16(__builtin_bit_cast(bf16x2, a), __builtin_bit_cast(bf16x2, b), c, false); }

constexpr u64 SPIN_TICKS = 20000;
DI bool spin_out(u64 t0) { __builtin_amdgcn_s_sleep(1); return (__builtin_amdgcn_s_memrealtime() - t0) > SPIN_TICKS; }

#define SB() __builtin_amdgcn_sched_barrier(0)
#define SGB_MFMA(n) __builtin_amdgcn_sched_group_barrier(0x8, n, 0)
#define SGB_DSR(n) __builtin_amdgcn_sched_group_barrier(0x100, n, 0)
#define SGB_DSW(n) __builtin_amdgcn_sched_group_barrier(0x200, n, 0)
#define STAMP(i) do {} while (0)
#define WACC(i, code) do { code; } while (0)

template <int YOUNGER> DI void pubbar(int NC, rsrc_t rws, u32 flag_off, u32 tlo, u32 thi, int tid) {
  waitv<YOUNGER>(); bar();
  if (tid < NC) st64_sc1(u32x2{tlo, thi}, rws, (u32)tid * 128u, flag_off);
}
template <int NF> DI u32x2 peek_flags(rsrc_t rws, u32 off, int lane) { return ld64_sc1(rws, (u32)(lane & (NF - 1)) * 8u, off); }
template <int NF> DI void wait_flags(rsrc_t rws, u32 off, int lane, u32 tlo, u32 thi) {
  const u32 vo = (u32)(lane & (NF - 1)) * 8u;
  const u64 t0 = __builtin_amdgcn_s_memrealtime();
  for (;;) {
    u32x2 f = ld64_sc1(rws, vo, off);
    if (__all(f[0] == tlo && f[1] == thi)) break;
    if (spin_out(t0)) break;
  }
  __builtin_amdgcn_fence(__ATOMIC_ACQUIRE, "wavefront");
}
DI bool gran_ok(u32x4 g, u32 glo, u32 ghi) { return (g[0] & 0xffu) == glo && g[1] == ghi && (g[2] & 0xffu) == glo && g[3] == ghi; }

#define STAMP_PARAM
#define STAMP_ARG
template <int NF> DI void wait_flags_from(u32x2 seen, rsrc_t rws, u32 off, int lane, u32 tlo, u32 thi) {
  if (!__all(seen[0] == tlo && seen[1] == thi)) wait_flags<NF>(rws, off, lane, tlo, thi);
  __builtin_amdgcn_fence(__ATOMIC_ACQUIRE, "wavefront");
}
struct Gath { u32x4 q[4]; };
DI void gath_load(Gath& G, rsrc_t rws, u32 voff, u32 soff) {
#pragma unroll
  for (int k = 0; k < 4; ++k) G.q[k] = ld128_sc1(rws, voff, WS_MODG + soff + (u32)k * 24576u);
}
DI bool gath_ok(const Gath& G, u32 glo, u32 ghi) { return gran_ok(G.q[0], glo, ghi) && gran_ok(G.q[1], glo, ghi) && gran_ok(G.q[2], glo, ghi) && gran_ok(G.q[3], glo, ghi); }
DI u32 gath_val(const Gath& G) {
  const float a = (f_(G.q[0][0] & 0xffffff00u) + f_(G.q[1][0] & 0xffffff00u)) + (f_(G.q[2][0] & 0xffffff00u) + f_(G.q[3][0] & 0xffffff00u));
  const float b = (f_(G.q[0][2] & 0xffffff00u) + f_(G.q[1][2] & 0xffffff00u)) + (f_(G.q[2][2] & 0xffffff00u) + f_(G.q[3][2] & 0xffffff00u));
  return pk2(a, b);
}
DI void mod_issue(u32x4 (&wm)[4], u32x2& cv, rsrc_t rwm, rsrc_t rc, int mq, int mblk, int cw, int lane) {
    const int r0 = 128 * mq + 32 * cw;
#pragma unroll
    for (int i = 0; i < 4; ++i) ald128_nt(wm[i], rwm, (u32)((lane >> 3) * 4 * 6144 + (lane & 7) * 16), (u32)((r0 + i) * 6144 + mblk * 128));
    ald64(cv, rc, (u32)((lane >> 3) * 8), (u32)(r0 * 2));
}
DI void mod_finish(LAS char* L, u32 pred, const u32x4 (&wm)[4], const u32x2& cv, u32 tlo, u32 thi, rsrc_t rws, int mq, int mblk, int cw, int lane) {
    const int g = lane >> 4, i16 = lane & 15;
    float a8[8];
#pragma unroll
    for (int e = 0; e < 8; ++e) a8[e] = 0.f;
#pragma unroll
    for (int i = 0; i < 4; ++i) {
      const float cf = (i & 1) ? bfhi(cv[i >> 1]) : bflo(cv[i >> 1]);
      const float sv = rbf(cf * __builtin_amdgcn_rcpf(1.f + __expf(-cf)));
#pragma unroll
      for (int e = 0; e < 4; ++e) { a8[2 * e] = fmaf(sv, bflo(wm[i][e]), a8[2 * e]); a8[2 * e + 1] = fmaf(sv, bfhi(wm[i][e]), a8[2 * e + 1]); }
    }
#pragma unroll
    for (int e = 0; e < 8; ++e) a8[e] = dpp_add<0x128>(a8[e]);
    float b4[4], b2[2];
#pragma unroll
    for (int e = 0; e < 4; ++e) b4[e] = swap16_add(a8[e], a8[e + 4]);
#pragma unroll
    for (int e = 0; e < 2; ++e) b2[e] = swap32_add(b4[e], b4[e + 2]);
    if (i16 < 8) *(LAS f32x2*)(L + pred + cw * 256 + (i16 * 8 + (g & 1) * 4 + (g >> 1) * 2) * 4) = f32x2{b2[0], b2[1]};
    asm volatile("s_waitcnt lgkmcnt(0)" ::: "memory");
    if (lane == 0) *(volatile LAS u32x2*)(L + pred + 1024 + cw * 8) = u32x2{tlo, thi};
    if (cw == 0) {
      const u64 t0 = __builtin_amdgcn_s_memrealtime();
      for (;;) {
        const u32x2 tg = *(volatile LAS u32x2*)(L + pred + 1024 + (lane & 3) * 8);
        if (__all(tg[0] == tlo && tg[1] == thi)) break;
        if ((__builtin_amdgcn_s_memrealtime() - t0) > SPIN_TICKS) break;
      }
      asm volatile("" ::: "memory");
      LAS float* s = (LAS float*)(L + pred) + lane;
      const float v = (s[0] + s[64]) + (s[128] + s[192]);
      st64_sc1(u32x2{((u_(v) + 0x80u) & 0xffffff00u) | (tlo & 0xffu), (tlo >> 8) | (thi << 24)}, rws, (u32)(lane * 8), WS_MODG + (u32)(mq * 24576 + mblk * 512));
    }
}
DI void front(const Params& p, LAS char* L, const int tid, const int w, const int b, u32x4 (&wr)[18], u32& tlo, u32& thi STAMP_PARAM) {
  const bool isC = w < 4;
  f32x2 xh[10][4];
  const u32 Lb = (u32)(size_t)L;
  const int lane = tid & 63, cw = w & 3;
  const int xcd = b & 7, idx = b >> 3;
  const int tt = 8 * (xcd >> 1) + (idx >> 2), hgl = idx & 3, hg = 4 * (xcd & 1) + hgl;
  const int jt = 8 * (xcd & 1) + 4 * (tt & 1) + hgl;
  const int T = tt >> 1, cb = hg;
  STAMP(0);
  const rsrc_t rx = mk(p.x, S_ * D_ * 2), rw1 = mk(p.wqkv, D_ * 3 * D_ * 2), rwo = mk(p.wo, D_ * D_ * 2);
  const rsrc_t rwg = mk(p.wg, D_ * F_ * 2), rwu = mk(p.wu, D_ * F_ * 2), rwd = mk(p.wd, F_ * D_ * 2);
  const rsrc_t rc = mk(p.c, D_ * 2), rwm = mk(p.wmod, D_ * 6 * D_ * 2), rout = mk(p.out, S_ * D_ * 2), rws = mk(p.ws, WS_BYTES);
  const int g = lane >> 4, i16 = lane & 15, h = lane >> 5, tok = lane & 31, t4 = tid & 255;

  const int nrot = 2 * hgl, rot = 2 * (tt & 7);
  const u32 w1v = (u32)((lane >> 3) * 3072 + (((((lane >> 1) & 3) ^ ((lane >> 4) & 1)) ^ ((cw & 1) << 1)) * 32) + (lane & 1) * 16);
  auto w1slot = [](int n) { return n < 6 ? L_RW + n * L_W1S : (n < 12 ? (n - 6) * L_W1S : L_RW + (n - 12) * L_W1S); };
  {
    u64 pc; asm volatile("s_getpc_b64 %0" : "=s"(pc));
    const rsrc_t rcode = mk((const void*)(pc & ~(u64)255), CODE_BYTES);
    warm(Lb + L_SCRAP, rcode, (u32)((idx * 8 + w) * 2 + (lane & 1)) * 128u, 0u);
  }
  u32x2 ev; ald64_sc1(ev, rws, 0u, WS_E);
  const bool prod = isC && idx < 8;
  const int mq = idx & 3, mblk = xcd * 2 + (idx >> 2);
  auto dma_w1 = [&](int i) {
    const int n = (i < 9 ? 6 : 0) + 2 * ((i % 9) / 3) + (w >> 2), pp = i % 3;
    dma16(rw1, w1v, (u32)((32 * ((n + rot) & 15) + 8 * cw) * 3072 + pp * 1024 + hg * 128), Lb + (u32)((i < 9 ? (n - 6) * L_W1S : L_RW + n * L_W1S) + pp * 4096 + cw * 1024));
  };
  u32x2 cv; u32x4 wm[4];
  if (prod) mod_issue(wm, cv, rwm, rc, mq, mblk, cw, lane);
  u32x4 xr[10];
#pragma unroll
  for (int n = 0; n < 10; ++n) {
    int r8 = n + nrot; r8 = r8 >= 10 ? r8 - 10 : r8;
    int row = 64 * tt - 16 + 8 * r8 + (lane >> 3); row = row < 0 ? 0 : row;
    ald128_nt(xr[n], rx, (u32)(row * 1024 + (lane & 7) * 16), (u32)(w * 128));
  }
  constexpr int Y0 = 0;
  STAMP(1);
  if (prod) {
    asm volatile("s_waitcnt vmcnt(%c6)" : "+v"(wm[0]), "+v"(wm[1]), "+v"(wm[2]), "+v"(wm[3]), "+v"(cv), "+v"(ev) : "i"(10 + Y0) : "memory");
    const u64 e1 = (((u64)__builtin_amdgcn_readfirstlane(ev[1]) << 32) | (u64)__builtin_amdgcn_readfirstlane(ev[0])) + 1ull;
    mod_finish(L, L_PRED, wm, cv, (u32)e1, (u32)(e1 >> 32), rws, mq, mblk, cw, lane);
  }
  STAMP(2);
  STAMP(3);
  {
    const int c8 = lane & 7, r8l = lane >> 3;
    float pv[10];
#pragma unroll
    for (int n = 0; n < 10; ++n) {
      waitv1<9>(xr[n]);
#pragma unroll
      for (int e = 0; e < 4; ++e) xh[n][e] = f32x2{bflo(xr[n][e]), bfhi(xr[n][e])};
      const f32x2 a1 = (xh[n][0] + xh[n][1]) + (xh[n][2] + xh[n][3]);
      const f32x2 a2 = __builtin_elementwise_fma(xh[n][0], xh[n][0], xh[n][1] * xh[n][1]) + __builtin_elementwise_fma(xh[n][2], xh[n][2], xh[n][3] * xh[n][3]);
      const float s1 = a1[0] + a1[1], s2 = a2[0] + a2[1];
      const float keep = (c8 & 1) ? s2 : s1, give = (c8 & 1) ? s1 : s2;
      float v = keep + f_(__builtin_amdgcn_update_dpp(0, u_(give), 0xB1, 0xf, 0xf, true));
      v = dpp_add<0x4E>(v);
      pv[n] = v + f_(__builtin_amdgcn_update_dpp(0, u_(v), 0x104, 0xf, 0xf, true));
      asm volatile("" : "+v"(pv[n])); dma_w1(n); SB();
    }
    asm volatile("" : "+v"(ev));
    {
      const u64 e1 = (((u64)__builtin_amdgcn_readfirstlane(ev[1]) << 32) | (u64)__builtin_amdgcn_readfirstlane(ev[0])) + 1ull;
      tlo = (u32)e1; thi = (u32)(e1 >> 32);
    }
    {
    const rsrc_t rt = w == 0 ? rwo : w == 1 ? rwg : w == 2 ? rwu : w == 3 ? rwd : w == 4 ? rout : rws;
    warm(Lb + L_SCRAP, rt, (u32)lane << 20, 0u);
  }

    if (c8 < 2) {
#pragma unroll
      for (int n = 0; n < 10; ++n) *(LAS float*)(L + L_LNP + (n * 8 + r8l) * 80 + (w * 2 + c8) * 4) = pv[n];
    }
    bar();
    f32x4 qa[2][4];
#pragma unroll
    for (int k = 0; k < 2; ++k)
#pragma unroll
      for (int e = 0; e < 4; ++e) qa[k][e] = *(const LAS f32x4*)(L + L_LNP + (k ? 64 + i16 : lane) * 80 + e * 16);
    dma_w1(10);
    float mus[2];
#pragma unroll
    for (int k = 0; k < 2; ++k) {
      const f32x4 a = (qa[k][0] + qa[k][1]) + (qa[k][2] + qa[k][3]);
      const float mu = (a[0] + a[2]) * (1.f / 512.f);
      const float var = fmaxf((a[1] + a[3]) * (1.f / 512.f) - mu * mu, 0.f);
      const float rs = __builtin_amdgcn_rsqf(var + 1e-5f);
      *(LAS f32x2*)(L + L_RSNB + (k ? 64 + i16 : lane) * 8) = f32x2{rs, -mu * rs};
      mus[k] = mu;
    }
    asm volatile("" ::: "memory");
    f32x2 rn[10];
#pragma unroll
    for (int n = 0; n < 10; ++n) rn[n] = *(const LAS f32x2*)(L + L_RSNB + (n * 8 + r8l) * 8);
    if (w == 0) {
#pragma unroll
      for (int k = 0; k < 2; ++k) { int r = (k ? 64 + i16 : lane) + 8 * nrot; r = r >= 80 ? r - 80 : r; ((LAS float*)(L + L_MU1 - 64))[r] = mus[k]; }
    }
#pragma unroll
    for (int n = 0; n < 10; ++n) {
#pragma unroll
      for (int e = 0; e < 4; ++e) xh[n][e] = __builtin_elementwise_fma(xh[n][e], f32x2{rn[n][0], rn[n][0]}, f32x2{rn[n][1], rn[n][1]});
      if (n < 7) { asm volatile("" : "+v"(xh[n][0]), "+v"(xh[n][1]), "+v"(xh[n][2]), "+v"(xh[n][3])); dma_w1(11 + n); SB(); }
    }
  }
#pragma unroll
  for (int n = 0; n < 10; ++n) asm volatile("" : "+v"(xh[n][0]), "+v"(xh[n][1]), "+v"(xh[n][2]), "+v"(xh[n][3]));
  STAMP(4);
  const u32 glo = tlo & 0xffu, ghi = (tlo >> 8) | (thi << 24);
  const u32 gvo = (u32)(w * 512 + tok * 16 + h * 4096);
  Gath g1_;
#pragma unroll
  for (int k = 0; k < 4; ++k) ald128_sc1<0>(g1_.q[k], rws, gvo, WS_MODG + (u32)k * 24576u);
  waitv<13>();
  bar();
  if (isC) {
    LAS char* w1rd = L + ((8 * g + (i16 >> 2)) * 128 + ((cw ^ (((i16 >> 3) & 1) | ((g & 1) << 1))) * 32) + (lane & 3) * 8);

#pragma unroll
    for (int n = 0; n < 6; ++n)
#pragma unroll
      for (int pp = 0; pp < 3; ++pp) wr[3 * n + pp] = trd2(w1rd + n * L_W1S + pp * 4096, 512);
  }
  STAMP(5);
  waitv4<0>(g1_.q[0], g1_.q[1], g1_.q[2], g1_.q[3]);
  if (!__all(gath_ok(g1_, glo, ghi))) {
    const u64 t0 = __builtin_amdgcn_s_memrealtime();
    for (;;) {
      gath_load(g1_, rws, gvo, 0u);
      if (__all(gath_ok(g1_, glo, ghi))) break;
      if (spin_out(t0)) break;
    }
  }
  {
    const u32 v2 = gath_val(g1_);
    const float a = bflo(v2), c = bfhi(v2);
    *(LAS f32x2*)(L + (h ? L_G1 : L_S1) + (w * 32 + tok) * 8) = h ? f32x2{rbf(1.f + a), rbf(1.f + c)} : f32x2{a, c};
  }
  STAMP(6);
  bar();
  {
    const int c8 = lane & 7, r8l = lane >> 3;
    const LAS f32x4* gp = (const LAS f32x4*)(L + L_G1 + (w * 64 + c8 * 8) * 4);
    const LAS f32x4* sp = (const LAS f32x4*)(L + L_S1 + (w * 64 + c8 * 8) * 4);
    const f32x4 g0 = gp[0], g1 = gp[1], s0 = sp[0], s1 = sp[1];
    LAS char* ab = L + L_AIMG + (2 * w + (c8 >> 2)) * 1024 + (c8 & 3) * 256 + ((r8l ^ c8) * 16);
#pragma unroll
    for (int n = 0; n < 10; ++n) {
      const f32x2 h0 = __builtin_elementwise_fma(xh[n][0], f32x2{g0[0], g0[1]}, f32x2{s0[0], s0[1]}), h1 = __builtin_elementwise_fma(xh[n][1], f32x2{g0[2], g0[3]}, f32x2{s0[2], s0[3]});
      const f32x2 h2 = __builtin_elementwise_fma(xh[n][2], f32x2{g1[0], g1[1]}, f32x2{s1[0], s1[1]}), h3 = __builtin_elementwise_fma(xh[n][3], f32x2{g1[2], g1[3]}, f32x2{s1[2], s1[3]});
      const u32x4 o = u32x4{pk2(h0[0], h0[1]), pk2(h1[0], h1[1]), pk2(h2[0], h2[1]), pk2(h3[0], h3[1])};
      int r8 = n + nrot; r8 = r8 >= 10 ? r8 - 10 : r8;
      *(LAS u32x4*)(ab + (r8 >> 1) * 16384 + (r8 & 1) * 128) = o;
    }
  }
  bar();
  STAMP(7);
}

template <bool isC> DI void role_body(const Params& p, LAS char* L, const int tid, const int w, const int b, u32x4 (&wr)[18], const u32 tlo, const u32 thi STAMP_PARAM) {
  const u32 Lb = (u32)(size_t)L;
  const int lane = tid & 63, cw = w & 3;
  const int xcd = b & 7, idx = b >> 3;
  const int tt = 8 * (xcd >> 1) + (idx >> 2), hgl = idx & 3, hg = 4 * (xcd & 1) + hgl;
  const int jt = 8 * (xcd & 1) + 4 * (tt & 1) + hgl;
  const int T = tt >> 1, cb = hg;
  const rsrc_t rx = mk(p.x, S_ * D_ * 2), rw1 = mk(p.wqkv, D_ * 3 * D_ * 2), rwo = mk(p.wo, D_ * D_ * 2);
  const rsrc_t rwg = mk(p.wg, D_ * F_ * 2), rwu = mk(p.wu, D_ * F_ * 2), rwd = mk(p.wd, F_ * D_ * 2);
  const rsrc_t rc = mk(p.c, D_ * 2), rwm = mk(p.wmod, D_ * 6 * D_ * 2), rout = mk(p.out, S_ * D_ * 2), rws = mk(p.ws, WS_BYTES);
  const int g = lane >> 4, i16 = lane & 15, h = lane >> 5, tok = lane & 31, t4 = tid & 255;

  const int nrot = 2 * hgl, rot = 2 * (tt & 7);
  const u32 w1v = (u32)((lane >> 3) * 3072 + (((((lane >> 1) & 3) ^ ((lane >> 4) & 1)) ^ ((cw & 1) << 1)) * 32) + (lane & 1) * 16);
  auto w1slot = [](int n) { return n < 6 ? L_RW + n * L_W1S : (n < 12 ? (n - 6) * L_W1S : L_RW + (n - 12) * L_W1S); };
  LAS char* w1rd = L + ((8 * g + (i16 >> 2)) * 128 + ((cw ^ (((i16 >> 3) & 1) | ((g & 1) << 1))) * 32) + (lane & 3) * 8);
  const u32 glo = tlo & 0xffu, ghi = (tlo >> 8) | (thi << 24);
  const int p0 = jt & 3, rotg = (4 * (T & 3) + 2 * (jt & 7)) & 15, rotd = (tt + hgl) & 3;
  const u32 wov = (u32)((lane >> 3) * 1024 + ((((lane >> 2) & 1) ^ ((lane >> 4) & 1)) * 64) + (lane & 3) * 16);
  const int mrow = (lane >> 4) & 1, q4 = i16 >> 2;
  const u32 p1base = (u32)(mrow * 4096 + (4 * h + q4) * 128 + (lane & 3) * 8), p1x = (u32)((q4 >> 1) | (mrow << 1));
  const u32 p2base = (u32)(mrow * L_P2M + (4 * h + q4) * 96 + (lane & 3) * 8);

  if constexpr (!isC) {
    const bool prod2 = idx >= 8 && idx < 24;
    const int mq = idx & 3, mblk = 16 + xcd * 4 + ((idx - 8) >> 2);
    u32x2 cv; u32x4 wm[4];
    if (prod2) mod_issue(wm, cv, rwm, rc, mq, mblk, cw, lane);
    bar();
#pragma unroll 1
    for (int n = 0; n < 4; ++n)
#pragma unroll
      for (int pp = 0; pp < 3; ++pp) dma16(rw1, w1v, (u32)((32 * ((12 + n + rot) & 15) + 8 * cw) * 3072 + pp * 1024 + hg * 128), Lb + (u32)(L_RW + n * L_W1S + pp * 4096 + cw * 1024));
    warm(Lb + L_SCRAP, cw == 0 ? rwg : cw == 1 ? rwu : cw == 2 ? rwd : rwo, (u32)(b + 256 * lane) * 128u, 0u);
    if (prod2) {
      asm volatile("s_waitcnt vmcnt(13)" : "+v"(wm[0]), "+v"(wm[1]), "+v"(wm[2]), "+v"(wm[3]), "+v"(cv) :: "memory");
      mod_finish(L, L_G1, wm, cv, tlo, thi, rws, mq, mblk, cw, lane);
    }
    if (prod2 && cw == 0) waitv<2>(); else waitv<1>();
    bar();
    STAMP(8);
    STAMP(9);
    bar();
    const u32 gch = h ? (u32)((cw == 0 ? 1024 : cw == 1 ? 2048 : 2560) + 64 * cb + 2 * tok) : (u32)((lane & 16 ? 1536 : 2048) + 32 * T + 2 * i16);
    Gath g2_;
#pragma unroll
    for (int k = 0; k < 4; ++k) ald128_sc1<0>(g2_.q[k], rws, gch * 8u, WS_MODG + (u32)k * 24576u);
    const rsrc_t rgu = (cw & 1) ? rwu : rwg;
    const int uc = 7 * (cw >> 1) + ((lane & 7) < 7 ? (lane & 7) : 6);
    u32x4 uw[4];
    u32 uso = (u32)(T * (32 * 3584) + jt * 224); asm volatile("" : "+s"(uso));
#pragma unroll
    for (int i = 0; i < 4; ++i) ald128<0>(uw[i], rgu, (u32)((lane >> 3) * 3584 + uc * 16), uso + (u32)(8 * i * 3584));
#pragma unroll 1
    for (int q = 0; q < 16; ++q) { const int qq = (q + rot) & 15; dma16(rwo, wov, (u32)((128 * cw + 8 * qq) * 1024 + cb * 128), Lb + (u32)(L_WO + (16 * cw + qq) * 1024)); }
#pragma unroll
    for (int q = 0; q < 2; ++q) dma16(rx, (u32)((lane >> 3) * 1024 + (lane & 7) * 16), (u32)((64 * tt + 16 * cw + 8 * q) * 1024 + cb * 128), Lb + (u32)(L_XRES + (2 * cw + q) * 1024));
    const u32 p1v = (u32)((lane >> 3) * 3584 + ((((lane >> 1) & 3) ^ ((lane >> 4) & 1)) * 32) + (lane & 1) * 16);
    auto dma_p1 = [&](int k) {
      const u32 so = (u32)((32 * ((k + rotg) & 15) + 8 * cw) * 3584 + jt * 224 + p0 * 32);
      dma16(rwg, p1v, so, Lb + (u32)(k * L_P1B + cw * 1024));
      dma16(rwu, p1v ^ 64u, so, Lb + (u32)(k * L_P1B + 4096 + cw * 1024));
    };
    dma_p1(0); dma_p1(1);
    bar();
    {
      waitv4<26>(g2_.q[0], g2_.q[1], g2_.q[2], g2_.q[3]);
      if (!__all(gath_ok(g2_, glo, ghi))) {
        const u64 t0 = __builtin_amdgcn_s_memrealtime();
        for (;;) {
          gath_load(g2_, rws, gch * 8u, 0u);
          if (__all(gath_ok(g2_, glo, ghi))) break;
          if (spin_out(t0)) break;
        }
      }
      const u32 v2 = gath_val(g2_);
      const bool one = h ? cw == 1 : !(lane & 16);
      const float a = bflo(v2), c = bfhi(v2);
      const u32 dst = h ? (cw == 0 ? L_GA : cw == 1 ? L_G2C : cw == 2 ? L_GBV : L_SCRAP) + tok * 8 : L_SL + cw * 256 + tok * 8;
      *(LAS f32x2*)(L + dst) = one ? f32x2{rbf(1.f + a), rbf(1.f + c)} : f32x2{a, c};
    }
    {
      waitv4<22>(uw[0], uw[1], uw[2], uw[3]);
      float a[16];
#pragma unroll
      for (int e = 0; e < 16; ++e) a[e] = 0.f;
#pragma unroll
      for (int i = 0; i < 4; ++i) {
        const float gk = ((const LAS float*)(L + L_SL + cw * 256))[8 * i + (lane >> 3)], sk = ((const LAS float*)(L + L_SL + cw * 256))[32 + 8 * i + (lane >> 3)];
#pragma unroll
        for (int e = 0; e < 4; ++e) {
          const float lo = bflo(uw[i][e]), hi = bfhi(uw[i][e]);
          a[2 * e] = fmaf(gk, lo, a[2 * e]); a[2 * e + 1] = fmaf(gk, hi, a[2 * e + 1]);
          a[8 + 2 * e] = fmaf(sk, lo, a[8 + 2 * e]); a[9 + 2 * e] = fmaf(sk, hi, a[9 + 2 * e]);
        }
      }
#pragma unroll
      for (int e = 0; e < 16; ++e) a[e] = dpp_add<0x128>(a[e]);
      float b8[8], ut4[4];
#pragma unroll
      for (int e = 0; e < 8; ++e) b8[e] = swap16_add(a[e], a[e + 8]);
#pragma unroll
      for (int e = 0; e < 4; ++e) ut4[e] = swap32_add(b8[e], b8[e + 4]);
      auto r0 = __builtin_amdgcn_permlane16_swap(u_(ut4[0]), u_(ut4[2]), false, false);
      auto r1 = __builtin_amdgcn_permlane16_swap(u_(ut4[1]), u_(ut4[3]), false, false);
      const u32 a0 = r0[0], b0 = r0[1], a1 = r1[0], b1 = r1[1];
      const int pr = uc >> 1, slot = pr >= p0 && pr < p0 + 4 ? pr - p0 : (pr < p0 ? 4 + pr : pr);
      if (i16 < 7) st128_sc1(u32x4{a0, b0, a1, b1}, rws, (u32)((slot * 32 + (cw & 1) * 16 + (uc & 1) * 8 + h * 4 + (g & 1) * 2) * 8), WS_UT + (u32)((jt * 16 + T) * 1792));
    }
    waitv<4>(); STAMP(10); bar();
#pragma unroll 1
    for (int k = 2; k < 8; ++k) dma_p1(k);
    waitv<12>(); STAMP(11); bar();
    if (tid == 256) st64_sc1(u32x2{tlo, thi}, rws, 0u, WS_FU + (u32)(jt * 256 + T * 8));
#pragma unroll 1
    for (int k = 8; k < 16; ++k) dma_p1(k);
    waitv<16>(); bar();
    u32 p2v[3];
#pragma unroll
    for (int i = 0; i < 3; ++i) { const int n = 64 * i + lane, row = n / 6, c = n - row * 6; p2v[i] = (u32)(row * 3584 + c * 16 + ((c >> 1) >= p0 ? 128 : 0)); }
    auto dma_p2 = [&](int k0, int k1) {
      if (ELIM & 1) return;
#pragma unroll 1
      for (int k = k0; k < k1; ++k) {
        const int pos = 2 * k + (cw >> 1);
#pragma unroll
        for (int i = 0; i < 3; ++i) dma16(rgu, p2v[i], (u32)(((pos + rotg) & 15) * 32 * 3584 + jt * 224), Lb + (u32)(pos * L_P2B + (cw & 1) * L_P2M + i * 1024));
      }
    };
    auto dma_wd = [&](int u) {
      if (ELIM & 4) return;
#pragma unroll
      for (int q = 0; q < 8; ++q) {
        const int n = 8 * (u - 4) + q;
        const int jj = ((u < 4 ? u : n / 6) + rotd) & 3, r6 = n % 6;
        const int row = u < 4 ? 112 * jj + 16 * jj + 8 * q : 112 * jj + 8 * r6 + ((r6 >> 1) >= jj ? 64 : 0);
        dma16(rwd, wov, (u32)((448 * cw + row) * 1024 + cb * 128), Lb + (u32)(wd_unit_off(u) + cw * 8192 + q * 1024));
      }
    };
    {
      wait_flags<16>(rws, WS_FU + (u32)(jt * 256), lane, tlo, thi);
      u32x2 pt[16];
#pragma unroll
      for (int k = 0; k < 16; ++k) ald64_sc1(pt[k], rws, (u32)(t4 < 224 ? t4 : 0) * 8u, WS_UT + (u32)((jt * 16 + k) * 1792));
      SB();
      float uu = 0.f, tt_ = 0.f;
      waitv<0>();
#pragma unroll
      for (int k = 0; k < 16; ++k) { asm volatile("" : "+v"(pt[k])); uu += f_(pt[k][0]); tt_ += f_(pt[k][1]); }
      const u32 hi = pk2(uu, tt_), lo = pk2(uu - bflo(hi), tt_ - bfhi(hi));
      const u32 u1 = hi & 0xffffu, t1 = hi >> 16, u2 = lo & 0xffffu, t2 = lo >> 16;
      const u32x4 o = u32x4{u1 | (u1 << 16), u2 | (u2 << 16), t1 | (t1 << 16), t2 | (t2 << 16)};
      if (t4 < 224) { LAS char* d = L + L_FIX + (t4 >> 5) * 1024 + (t4 & 31) * 16; *(LAS u32x4*)d = o; *(LAS u32x4*)(d + 512) = o; }
    }
    STAMP(12); bar();
    dma_p2(0, 5);
    waitv<0>(); STAMP(15); bar();
    dma_p2(5, 8);
    bar();
    dma_wd(0);
    waitv<8>(); STAMP(16); bar();
    dma_wd(1);
    bar();
    dma_wd(2);
    waitv<16>(); bar();
    dma_wd(3);
    waitv<16>(); STAMP(17); bar();
    dma_wd(4);
    waitv<16>(); bar();
    dma_wd(5);
    waitv<16>(); bar();
    dma_wd(6);
    waitv<16>(); STAMP(18); bar();
    waitv<8>(); bar();
    waitv<0>(); bar();
    bar();
  } else {
    f32x4 Kt[5], Qt[4], Vn[5];
#pragma unroll
    for (int r = 0; r < 5; ++r) { Kt[r] = f32x4{0, 0, 0, 0}; Vn[r] = f32x4{0, 0, 0, 0}; if (r < 4) Qt[r] = f32x4{0, 0, 0, 0}; }
    {
      u32x4 a[2][5], wl[2][3];
      LAS char* const w1a = opq(w1rd + L_RW); LAS char* const w1b = opq(w1rd + L_RW + 4 * L_W1S);
      auto ld = [&](int n, int bi) {
        LAS char* ab = L + L_AIMG + ((n + rot) & 15) * 1024 + g * 256 + ((i16 ^ (((n & 1) << 2) | g)) * 16);
#pragma unroll
        for (int rb = 0; rb < 5; ++rb) a[bi][rb] = *(const LAS u32x4*)(ab + rb * 16384);
        if (n < 6 || n >= 12) {
#pragma unroll
          for (int pp = 0; pp < 3; ++pp) wl[bi][pp] = trd2((n % 6 < 4 ? w1a : w1b) + (n % 6 % 4) * L_W1S + pp * 4096, 512);
        }
      };
      ld(0, 0); SB();
#pragma unroll
      for (int n = 0; n < 16; ++n) {
        if (n == 3) bar();
        if (n == 11) WACC(24, bar());
        if (n < 15) ld(n + 1, (n + 1) & 1);
        const bool rg = n >= 6 && n < 12;
        const u32x4 wq = rg ? wr[rg ? 3 * (n - 6) : 0] : wl[n & 1][0], wk = rg ? wr[rg ? 3 * (n - 6) + 1 : 0] : wl[n & 1][1], wv = rg ? wr[rg ? 3 * (n - 6) + 2 : 0] : wl[n & 1][2];
#pragma unroll
        for (int rb = 0; rb < 5; ++rb) {
          Kt[rb] = mfma16(wk, a[n & 1][rb], Kt[rb]);
          Vn[rb] = mfma16(a[n & 1][rb], wv, Vn[rb]);
          if (rb > 0) Qt[rb - 1] = mfma16(wq, a[n & 1][rb], Qt[rb - 1]);
        }
#pragma unroll
        for (int k = 0; k < 11; ++k) { SGB_MFMA(1); SGB_DSR(1); }
        SGB_MFMA(3);
        SB();
      }
    }
    bar();
    STAMP(8);
    const int tb = cw & 1, kh = cw >> 1;
    {
      u32x2 kp[5], vp[5];
#pragma unroll
      for (int r = 0; r < 5; ++r) { kp[r] = u32x2{pk2(Kt[r][0], Kt[r][1]), pk2(Kt[r][2], Kt[r][3])}; vp[r] = u32x2{pk2(Vn[r][0], Vn[r][1]), pk2(Vn[r][2], Vn[r][3])}; }
      f32x4 m0, m1, m0f;
#pragma unroll
      for (int r = 0; r < 4; ++r) {
        const int d1 = i16 - 4 * g - r, d0 = d1 + 16;
        m0[r] = (d0 <= 14 && !(d0 & 1)) ? 0.f : -1e30f;
        m1[r] = (d1 >= 0 && d1 <= 14 && !(d1 & 1)) ? 0.f : -1e30f;
        m0f[r] = tt == 0 ? -1e30f : m0[r];
      }
      u32x2 op[4];
      const u32x4 ones = u32x4{0x3f803f80u, 0x3f803f80u, 0x3f803f80u, 0x3f803f80u};
#pragma unroll
      for (int qb = 0; qb < 4; ++qb) {
        const u32x2 qp = u32x2{pk2(Qt[qb][0], Qt[qb][1]), pk2(Qt[qb][2], Qt[qb][3])};
        const f32x4 s0 = mfma16k16(kp[qb], qp, qb == 0 ? m0f : m0), s1 = mfma16k16(kp[qb + 1], qp, m1);
        float mx = fmaxf(fmaxf(fmaxf(s0[0], s0[1]), fmaxf(s0[2], s0[3])), fmaxf(fmaxf(s1[0], s1[1]), fmaxf(s1[2], s1[3])));
        mx = swap16_max(mx); mx = swap32_max(mx);
        const float cs = 0.25f * 1.44269504088896f, nm = -mx * cs;
        float e0[4], e1[4];
#pragma unroll
        for (int r = 0; r < 4; ++r) { e0[r] = __builtin_amdgcn_exp2f(fmaf(s0[r], cs, nm)); e1[r] = __builtin_amdgcn_exp2f(fmaf(s1[r], cs, nm)); }
        const u32x4 pf = u32x4{pk2(e0[0], e0[1]), pk2(e0[2], e0[3]), pk2(e1[0], e1[1]), pk2(e1[2], e1[3])};
        const f32x4 z = f32x4{0, 0, 0, 0};
        const f32x4 ot = mfma16(u32x4{vp[qb][0], vp[qb][1], vp[qb + 1][0], vp[qb + 1][1]}, pf, z);
        const f32x4 sm = mfma16(ones, pf, z);
        const float ri = __builtin_amdgcn_rcpf(sm[0]);
        op[qb] = u32x2{pk2(ot[0] * ri, ot[1] * ri), pk2(ot[2] * ri, ot[3] * ri)};
      }
#pragma unroll
      for (int t2 = 0; t2 < 2; ++t2) {
        auto r0 = __builtin_amdgcn_permlane16_swap(op[2 * t2][0], op[2 * t2 + 1][0], false, false);
        auto r1 = __builtin_amdgcn_permlane16_swap(op[2 * t2][1], op[2 * t2 + 1][1], false, false);
        const u32 a0 = r0[0], b0 = r0[1], a1 = r1[0], b1 = r1[1];
        st128_sc1(u32x4{a0, a1, b0, b1}, rws, (u32)(lane * 16), WS_O + (u32)(((2 * tt + t2) * 32 + 4 * hg + cw) * 1024));
      }
    }
    STAMP(9);
    pubbar<0>(8, rws, WS_FO + (u32)(tt * 1024 + hg * 8), tlo, thi, tid);
    STAMP(10);
    wait_flags<8>(rws, WS_FO + (u32)(tt * 1024 + cb * 128), lane, tlo, thi);
    STAMP(11);
    u32 x1p[8];
    {
      u32x4 of[16];
      const u32 ob = WS_O + (u32)(((2 * tt + tb) * 32 + 16 * kh) * 1024);
#pragma unroll
      for (int s = 0; s < 16; ++s) { of[s] = ld128_sc1(rws, (u32)(lane * 16), ob + (u32)(((s + 4 * hgl) & 15) * 1024)); SB(); }
      SB();
      WACC(24, bar());
      LAS char* q0 = L + (L_WO + (256 * kh + 8 * h + q4) * 128 + mrow * 32 + (lane & 3) * 8);
      LAS char* qa = q0 + ((q4 >> 1) ^ kh) * 64; LAS char* qb = q0 + ((q4 >> 1) ^ kh ^ 1) * 64;
      f32x16 a0, a1;
#pragma unroll
      for (int r = 0; r < 16; ++r) { a0[r] = 0.f; a1[r] = 0.f; }
      u32x4 wl[2][2];
      wl[0][0] = trd2(qa + hgl * 8192, 512); wl[0][1] = trd2(qb + hgl * 8192, 512); SB();
#pragma unroll
      for (int s = 0; s < 16; ++s) {
        if (s < 15) { const int o2 = ((((s + 1) >> 2) + hgl) & 3) * 8192 + ((s + 1) & 3) * 2048; wl[(s + 1) & 1][0] = trd2(qa + o2, 512); wl[(s + 1) & 1][1] = trd2(qb + o2, 512); }
        a0 = mfma32(wl[s & 1][0], of[s], a0); a1 = mfma32(wl[s & 1][1], of[s], a1);
        SGB_MFMA(1); SGB_DSR(2); SGB_MFMA(1); SGB_DSR(2);
        SB();
      }
      u32x2 xres[4];
#pragma unroll
      for (int q = 0; q < 4; ++q) xres[q] = *(const LAS u32x2*)(L + L_XRES + (32 * tb + tok) * 128 + (32 * kh + 8 * q + 4 * h) * 2);
      const f32x16 snd = a1, acc = a0;
#pragma unroll
      for (int q = 0; q < 4; ++q) *(LAS f32x4*)(L + L_RED2 + cw * 4096 + q * 1024 + lane * 16) = f32x4{snd[4 * q], snd[4 * q + 1], snd[4 * q + 2], snd[4 * q + 3]};
      STAMP(19);
      bar();
      STAMP(20);
      const float mu1 = ((const LAS float*)(L + L_MU1))[32 * tb + tok];
      float s1 = 0.f, s2 = 0.f; u32 yp[8];
#pragma unroll
      for (int q = 0; q < 4; ++q) {
        const f32x4 o4 = *(const LAS f32x4*)(L + L_RED2 + (cw ^ 2) * 4096 + q * 1024 + lane * 16);
        const u32x2 xq = xres[q];
        const f32x4 ga4 = *(const LAS f32x4*)(L + L_GA + (32 * kh + 8 * q + 4 * h) * 4), g24 = *(const LAS f32x4*)(L + L_G2C + (32 * kh + 8 * q + 4 * h) * 4);
        const float v0 = fmaf(ga4[0], acc[4 * q] + o4[0], bflo(xq[0])), v1 = fmaf(ga4[1], acc[4 * q + 1] + o4[1], bfhi(xq[0]));
        const float v2 = fmaf(ga4[2], acc[4 * q + 2] + o4[2], bflo(xq[1])), v3 = fmaf(ga4[3], acc[4 * q + 3] + o4[3], bfhi(xq[1]));
        x1p[2 * q] = pk2(v0, v1); x1p[2 * q + 1] = pk2(v2, v3);
        const float d0 = bflo(x1p[2 * q]) - mu1, d1 = bfhi(x1p[2 * q]) - mu1, d2 = bflo(x1p[2 * q + 1]) - mu1, d3 = bfhi(x1p[2 * q + 1]) - mu1;
        s1 += (d0 + d1) + (d2 + d3); s2 = fmaf(d0, d0, fmaf(d1, d1, fmaf(d2, d2, fmaf(d3, d3, s2))));
        yp[2 * q] = pk2(d0 * g24[0], d1 * g24[1]); yp[2 * q + 1] = pk2(d2 * g24[2], d3 * g24[3]);
      }
      const u32 yb = WS_Y + (u32)(((2 * tt + tb) * 32 + 4 * cb + 2 * kh) * 1024);
      st128_sc1(u32x4{yp[0], yp[1], yp[2], yp[3]}, rws, (u32)(lane * 16), yb);
      st128_sc1(u32x4{yp[4], yp[5], yp[6], yp[7]}, rws, (u32)(lane * 16), yb + 1024);
      s1 = swap32_add(s1, s1); s2 = swap32_add(s2, s2);
      if (lane < 32) st64_sc1(u32x2{u_(s1), u_(s2)}, rws, (u32)(tok * 8), WS_ST + (u32)(((2 * tt + tb) * 16 + 2 * cb + kh) * 256));
    }
#pragma unroll
    for (int q = 0; q < 8; q += 4) asm volatile("" : "+v"(x1p[q]), "+v"(x1p[q + 1]), "+v"(x1p[q + 2]), "+v"(x1p[q + 3]));
    STAMP(12);
    pubbar<0>(16, rws, WS_FY + (u32)(tt * 2048 + cb * 8), tlo, thi, tid);
    STAMP(13);
    wait_flags<8>(rws, WS_FY + (u32)((2 * T + (cw >> 1)) * 2048 + jt * 128), lane, tlo, thi);
    STAMP(14);
    const int tb32 = 4 * T + cw;
    u32x2 stv[8];
#pragma unroll
    for (int q = 0; q < 8; ++q) stv[q] = ld64_sc1(rws, (u32)(tok * 8 + h * 2048), WS_ST + (u32)((tb32 * 16 + q) * 256));
    u32x4 yf[32];
    auto ldy = [&](int s) { yf[s] = ld128_sc1(rws, (u32)(lane * 16), WS_Y + (u32)((tb32 * 32 + ((s + 2 * rotg) & 31)) * 1024)); };
#pragma unroll
    for (int s = 0; s < (EXPW ? 32 : 10); ++s) { ldy(s); SB(); }
    SB();
    u32 mz, sz; float r2;
    {
      float s1 = 0.f, s2 = 0.f;
#pragma unroll
      for (int q = 0; q < 8; ++q) { s1 += f_(stv[q][0]); s2 += f_(stv[q][1]); }
      s1 = swap32_add(s1, s1); s2 = swap32_add(s2, s2);
      const float md = s1 * (1.f / 512.f);
      const float ve = fmaxf(s2 * (1.f / 512.f) - md * md, 0.f) + 1e-5f;
      const float sg = __builtin_amdgcn_sqrtf(ve); r2 = __builtin_amdgcn_rcpf(ve);
      const u32 hi = pk2(-md, sg), lo = pk2(-md - bflo(hi), sg - bfhi(hi));
      const u32 mm = (hi & 0xffffu) | (lo << 16), ss = (hi >> 16) | (lo & 0xffff0000u);
      mz = h ? 0u : mm; sz = h ? 0u : ss;
      asm volatile("" : "+v"(mz), "+v"(sz), "+v"(r2));
    }
    SB();
    auto act_store = [&](const f32x16& ac, int pp) {
      u32x4 o;
#pragma unroll
      for (int e = 0; e < 4; ++e) o[e] = pk2(fmaxf(ac[2 * e], 0.f) * r2 * ac[8 + 2 * e], fmaxf(ac[2 * e + 1], 0.f) * r2 * ac[9 + 2 * e]);
      st128_sc1(o, rws, (u32)(lane * 16), WS_ACT + (u32)((tb32 * 112 + 7 * jt + pp) * 1024));
    };
    {
      f32x16 acc[4];
#pragma unroll
      for (int q = 0; q < 4; ++q)
#pragma unroll
        for (int r = 0; r < 16; ++r) acc[q][r] = 0.f;
      LAS char* pa[4];
#pragma unroll
      for (int pp = 0; pp < 4; ++pp) pa[pp] = L + (p1base + (((u32)pp ^ p1x) * 32));
      u32x4 wf[2][4];
      auto ldw = [&](int st, int bi) {
#pragma unroll
        for (int pp = 0; pp < 4; ++pp) { if (st == 16) pa[pp] = opq(pa[pp] + 8 * L_P1B); wf[bi][pp] = trd2(pa[pp] + ((st >> 1) & 7) * L_P1B + (st & 1) * 2048, 1024); }
      };
      ldw(0, 0); SB();
#pragma unroll
      for (int st = 0; st < 32; ++st) {
        if (st == 15) WACC(25, bar());
        if (st + 1 < 32) ldw(st + 1, (st + 1) & 1);
        if (OPT & 2) { if (2 * st + 10 < 32) { ldy(2 * st + 10); SB(); ldy(2 * st + 11); } }
        else if (!EXPW && st + 10 < 32) ldy(st + 10);
#pragma unroll
        for (int pp = 0; pp < 4; ++pp) acc[pp] = mfma32(wf[st & 1][pp], yf[st], acc[pp]);
#pragma unroll
        for (int pp = 0; pp < 4; ++pp) { SGB_MFMA(1); SGB_DSR(2); }
        SB();
      }
      WACC(26, bar());
#pragma unroll
      for (int pp = 0; pp < 4; ++pp) acc[pp] = mfma32(*(const LAS u32x4*)(L + L_FIX + pp * 1024 + lane * 16), u32x4{mz, mz, sz, sz}, acc[pp]);
      STAMP(15);
#pragma unroll
      for (int pp = 0; pp < 4; ++pp) act_store(acc[pp], p0 + pp);
    }
    u32x4 af[16][2]; u32x2 fl;
    auto lda = [&](int n) { const int s = n >> 1, t2 = n & 1; af[s][t2] = ld128_sc1(rws, (u32)(lane * 16), WS_ACT + (u32)(((2 * tt + (t2 ^ tb)) * 112 + 28 * cw + 8 * (((s >> 2) + rotd) & 3) + (s & 3)) * 1024)); };
    {
      f32x16 acc[3];
#pragma unroll
      for (int q = 0; q < 3; ++q)
#pragma unroll
        for (int r = 0; r < 16; ++r) acc[q][r] = 0.f;
      LAS char* pb = L + p2base;
      u32x4 wf[2][3];
      auto ldw = [&](int st, int bi) {
        if (st == 16) pb = opq(pb + 8 * L_P2B);
#pragma unroll
        for (int pp = 0; pp < 3; ++pp) wf[bi][pp] = trd2(pb + ((st >> 1) & 7) * L_P2B + (st & 1) * 1536 + pp * 32, 768);
      };
      ldw(0, 0); SB();
#pragma unroll
      for (int st = 0; st < 32; ++st) {
        if (st == 3) WACC(30, pubbar<0>(16, rws, WS_FA + (u32)(T * 2048 + jt * 8), tlo, thi, tid));
        if (st == 15 || st == 23) WACC(27, bar());
        if (st == FA1P) fl = peek_flags<4>(rws, WS_FA + (u32)(T * 2048 + ((tt & 1) * 8 + cb) * 128 + cw * 32), lane);
        if (st == FA1P + 4) { STAMP(23); WACC(28, wait_flags_from<4>(fl, rws, WS_FA + (u32)(T * 2048 + ((tt & 1) * 8 + cb) * 128 + cw * 32), lane, tlo, thi)); }
        if (st + 1 < 32) ldw(st + 1, (st + 1) & 1);
        if (st >= FA1P + 4) lda(st - FA1P - 4);
#pragma unroll
        for (int pp = 0; pp < 3; ++pp) acc[pp] = mfma32(wf[st & 1][pp], yf[st], acc[pp]);
#pragma unroll
        for (int pp = 0; pp < 3; ++pp) { SGB_MFMA(1); SGB_DSR(2); }
        SB();
      }
      WACC(27, bar());
#pragma unroll
      for (int pp = 0; pp < 3; ++pp) acc[pp] = mfma32(*(const LAS u32x4*)(L + L_FIX + (4 + pp) * 1024 + lane * 16), u32x4{mz, mz, sz, sz}, acc[pp]);
      STAMP(16);
#pragma unroll
      for (int pp = 0; pp < 3; ++pp) act_store(acc[pp], pp >= p0 ? pp + 4 : pp);
#pragma unroll
      for (int n = 28 - FA1P; n < 24; ++n) { lda(n); SB(); }
    }
    SB();
    {
      f32x16 acc[4];
#pragma unroll
      for (int q = 0; q < 4; ++q)
#pragma unroll
        for (int r = 0; r < 16; ++r) acc[q][r] = 0.f;
      LAS char* d0 = L + (cw * 8192 + (4 * h + q4) * 128 + mrow * 32 + (lane & 3) * 8);
      LAS char* da = d0 + ((q4 >> 1) ^ kh) * 64; LAS char* db = d0 + ((q4 >> 1) ^ kh ^ 1) * 64;
      u32x4 wl[2][2], a2[12][2];
      auto lda2 = [&](int n) {
        const int s2 = n >> 1, t2 = n & 1, jj = (s2 / 3 + rotd) & 3, pp = s2 % 3;
        a2[s2][t2] = ld128_sc1(rws, (u32)(lane * 16), WS_ACT + (u32)(((2 * tt + (t2 ^ tb)) * 112 + 28 * cw + 7 * jj + (pp >= jj ? pp + 4 : pp)) * 1024));
      };
      LAS char* const dah = opq(da + 65536); LAS char* const dbh = opq(db + 65536);
      auto ldw = [&](int s, int bi) { const u32 o = wd_unit_off(s >> 2) + (s & 3) * 2048; wl[bi][0] = trd2(o < 65536 ? da + o : dah + (o - 65536), 1024); wl[bi][1] = trd2(o < 65536 ? db + o : dbh + (o - 65536), 1024); };
      ldw(0, 0); SB();
#pragma unroll
      for (int s = 0; s < 28; ++s) {
        if (s == 3) WACC(30, pubbar<FA1P - 4 + 3>(16, rws, WS_FA2 + (u32)(T * 2048 + jt * 8), tlo, thi, tid));
        if ((s & 3) == 3 && s > 3 && s < 24) WACC(29, bar());
        if (s == 12) {
          STAMP(17);
          wait_flags_from<4>(fl, rws, WS_FA2 + (u32)(T * 2048 + ((tt & 1) * 8 + cb) * 128 + cw * 32), lane, tlo, thi);
          STAMP(21);
#pragma unroll
          for (int n = 0; n < 12; ++n) { lda2(n); SB(); }
          SB();
        }
        if (s < 8) lda(24 + s);
        if (s == 8) fl = peek_flags<4>(rws, WS_FA2 + (u32)(T * 2048 + ((tt & 1) * 8 + cb) * 128 + cw * 32), lane);
        if (s >= 13 && s < 19) { lda2(12 + 2 * (s - 13)); SB(); lda2(13 + 2 * (s - 13)); }
        if (s + 1 < 28) ldw(s + 1, (s + 1) & 1);
        const u32x4 b0 = s < 16 ? af[s < 16 ? s : 0][0] : a2[s < 16 ? 0 : s - 16][0], b1 = s < 16 ? af[s < 16 ? s : 0][1] : a2[s < 16 ? 0 : s - 16][1];
        acc[0] = mfma32(wl[s & 1][0], b0, acc[0]); acc[2] = mfma32(wl[s & 1][1], b0, acc[2]);
        acc[1] = mfma32(wl[s & 1][0], b1, acc[1]); acc[3] = mfma32(wl[s & 1][1], b1, acc[3]);
        SGB_MFMA(1); SGB_DSR(2); SGB_MFMA(1); SGB_DSR(2); SGB_MFMA(2);
        SB();
      }
      STAMP(22);
      int ln2 = lane; asm volatile("" : "+v"(ln2));
#pragma unroll
      for (int i = 1; i < 4; ++i)
#pragma unroll
        for (int q = 0; q < 4; ++q) *(LAS f32x4*)(L + L_RED4 + ((i ^ cw) * 3 + i - 1) * 4096 + q * 1024 + ln2 * 16) = f32x4{acc[i][4 * q], acc[i][4 * q + 1], acc[i][4 * q + 2], acc[i][4 * q + 3]};
      const f32x16 mine = acc[0];
      bar();
      u32 ob[8];
#pragma unroll
      for (int q = 0; q < 4; ++q) {
        const LAS char* rp = L + L_RED4 + cw * 3 * 4096 + q * 1024 + ln2 * 16;
        const f32x4 r0 = *(const LAS f32x4*)rp, r1 = *(const LAS f32x4*)(rp + 4096), r2_ = *(const LAS f32x4*)(rp + 8192);
        const f32x4 gb4 = *(const LAS f32x4*)(L + L_GBV + (32 * kh + 8 * q + 4 * (ln2 >> 5)) * 4);
        float v[4];
#pragma unroll
        for (int e = 0; e < 4; ++e) v[e] = (mine[4 * q + e] + r0[e]) + (r1[e] + r2_[e]);
        ob[2 * q] = pk2(fmaf(gb4[0], v[0], bflo(x1p[2 * q])), fmaf(gb4[1], v[1], bfhi(x1p[2 * q])));
        ob[2 * q + 1] = pk2(fmaf(gb4[2], v[2], bflo(x1p[2 * q + 1])), fmaf(gb4[3], v[3], bfhi(x1p[2 * q + 1])));
      }
#pragma unroll
      for (int m = 0; m < 2; ++m) {
        auto r0 = __builtin_amdgcn_permlane32_swap(ob[4 * m], ob[4 * m + 2], false, false);
        auto r1 = __builtin_amdgcn_permlane32_swap(ob[4 * m + 1], ob[4 * m + 3], false, false);
        const u32 a0 = r0[0], b0 = r0[1], a1 = r1[0], b1 = r1[1];
        __builtin_amdgcn_raw_buffer_store_b128(u32x4{a0, a1, b0, b1}, rout, (u32)((64 * tt + 32 * tb + (ln2 & 31)) * 1024 + (64 * cb + 32 * kh + 16 * m + 8 * (ln2 >> 5)) * 2), 0, 0);
      }
    }
    STAMP(18);
    if (b == 0 && cw == 0) {
      const u64 t0 = __builtin_amdgcn_s_memrealtime();
      for (;;) {
        bool ok = true;
#pragma unroll
        for (int k = 0; k < 4; ++k) { const int f = 64 * k + lane; const u32x2 v = ld64_sc1(rws, (u32)((f >> 3) * 1024 + (f & 7) * 8), WS_FO); ok = ok && v[0] == tlo && v[1] == thi; }
        if (__all(ok)) break;
        if (spin_out(t0)) break;
      }
      if (lane == 0) st64_sc1(u32x2{tlo, thi}, rws, 0u, WS_E);
    }
  }
}

__global__ __launch_bounds__(512, 2) void fused_block(Params p) {
  extern __shared__ __attribute__((aligned(16))) char smem_[];
  asm volatile("" :: "s"(p.x), "s"(p.wqkv), "s"(p.wo), "s"(p.wg), "s"(p.wu), "s"(p.wd), "s"(p.c), "s"(p.wmod), "s"(p.out), "s"(p.ws));
  int w = __builtin_amdgcn_readfirstlane(threadIdx.x >> 6);
  u32x4 wr[18]; u32 tlo, thi;
  front(p, (LAS char*)smem_, threadIdx.x, w, blockIdx.x, wr, tlo, thi STAMP_ARG);
  if (w >= 4) {
    int b = blockIdx.x, tid = threadIdx.x; asm volatile("; H role" : "+s"(b), "+s"(w)); asm volatile("; H role" : "+v"(tid));
    role_body<false>(p, (LAS char*)smem_, tid, w, b, wr, tlo, thi STAMP_ARG);
  } else {
    int b = blockIdx.x, tid = threadIdx.x; asm volatile("; C role" : "+s"(b), "+s"(w)); asm volatile("; C role" : "+v"(tid));
    role_body<true>(p, (LAS char*)smem_, tid, w, b, wr, tlo, thi STAMP_ARG);
  }
}

extern "C" void kernel_launch(void* const* d_in, const int* in_sizes, int n_in, void* d_out, int out_size, void* d_ws, size_t ws_size, hipStream_t stream) {
  Params p;
  p.x = d_in[0]; p.wqkv = d_in[1]; p.wo = d_in[2]; p.wg = d_in[4]; p.wu = d_in[5]; p.wd = d_in[6]; p.c = d_in[7]; p.wmod = d_in[8];
  p.out = d_out; p.ws = d_ws;
  (void)hipFuncSetAttribute((const void*)fused_block, hipFuncAttributeMaxDynamicSharedMemorySize, LDS_BYTES);
  fused_block<<<256, 512, LDS_BYTES, stream>>>(p);
}
```

```cpp
#include <hip/hip_runtime.h>
#include <stdint.h>

typedef unsigned short u16;
typedef unsigned u32;
typedef unsigned long long u64;
typedef u32 u32x2 __attribute__((ext_vector_type(2)));
typedef u32 u32x4 __attribute__((ext_vector_type(4)));
typedef short s16x4 __attribute__((ext_vector_type(4)));
typedef short s16x8 __attribute__((ext_vector_type(8)));
typedef __bf16 bf16x2 __attribute__((ext_vector_type(2)));
typedef __bf16 bf16x8 __attribute__((ext_vector_type(8)));
typedef float f32x2 __attribute__((ext_vector_type(2)));
typedef float f32x4 __attribute__((ext_vector_type(4)));
typedef float f32x16 __attribute__((ext_vector_type(16)));
typedef __amdgpu_buffer_rsrc_t rsrc_t;
#define LAS __attribute__((address_space(3)))
#define DI __device__ __forceinline__

#define EXPW 0
#define TOUCH 30
#define THR 4
constexpr unsigned CODE_BYTES = 50000;
#define NTX 1
#define FA1P 16
#define OPT 0
#define ELIM 0
#define S0P 3
#define A1R 1
#define A1N (A1R == 1 ? 28 - FA1P : A1R == 2 ? 2 * (28 - FA1P) : 3 * (28 - FA1P) / 2)
#define W1T 0
#define D0 0
#define A2C 9
#define LITE 0
#define DIAG 0
#define XMAP 2

constexpr int S_ = 2048, D_ = 512, F_ = 1792;
constexpr u32 WS_E = 0, WS_MODG = 393216, WS_FU = 57344, WS_FO = 8192, WS_FY = 983040, WS_FA = 327680, WS_FA2 = 360448, WS_DBG = 65536;
constexpr u32 WS_UT = 524288;
constexpr u32 WS_O = 1u << 20, WS_Y = 3u << 20, WS_ST = 5u << 20, WS_ACT = 6u << 20, WS_BYTES = 13u << 20;
constexpr u32 L_AIMG = 0, L_RW = 81920, L_W1S = 12288;
constexpr u32 L_LNP = 73728, L_PRED = L_LNP + 6400, L_RSNB = L_PRED + 1088;
constexpr u32 L_XRES = 65536, L_WO = 73728, L_RED2 = 139264;
constexpr u32 L_P1B = 8192, L_P2B = 6400, L_P2M = 3200;
constexpr u32 L_RED4 = 65536;
constexpr u32 L_MISC = 155648, L_G1 = L_MISC, L_S1 = L_MISC + 2048, L_FIX = L_MISC;
constexpr u32 L_SL = L_MISC + 4096, L_SCRAP = L_MISC + 6656;
constexpr u32 L_MU1 = L_MISC + 7168, L_GA = L_MU1 + 256, L_G2C = L_GA + 256, L_GBV = L_G2C + 256;
constexpr u32 LDS_BYTES = 163840;
__host__ __device__ constexpr u32 wd_unit_off(int u) { return u == 0 || u == 4 ? 102400u : (u == 1 || u == 5 ? 0u : (u == 2 || u == 6 ? 32768u : 65536u)); }

template <int N> struct ic { static constexpr int value = N; };
struct Params { const void *x, *wqkv, *wo, *wg, *wu, *wd, *c, *wmod; void *out; void *ws; };

DI rsrc_t mk(const void* p, u32 bytes) { return __builtin_amdgcn_make_buffer_rsrc((void*)p, 0, bytes, 0x00020000); }
DI float bflo(u32 v) { return __builtin_bit_cast(float, v << 16); }
DI float bfhi(u32 v) { return __builtin_bit_cast(float, v & 0xffff0000u); }
DI u32 pk2(float a, float b) { bf16x2 r; r[0] = (__bf16)a; r[1] = (__bf16)b; return __builtin_bit_cast(u32, r); }
DI float rbf(float a) { return bflo(pk2(a, a)); }
DI f32x4 mfma16(u32x4 a, u32x4 b, f32x4 c) { return __builtin_amdgcn_mfma_f32_16x16x32_bf16(__builtin_bit_cast(bf16x8, a), __builtin_bit_cast(bf16x8, b), c, 0, 0, 0); }
DI f32x4 mfma16k16(u32x2 a, u32x2 b, f32x4 c) { return __builtin_amdgcn_mfma_f32_16x16x16bf16_1k(__builtin_bit_cast(s16x4, a), __builtin_bit_cast(s16x4, b), c, 0, 0, 0); }
DI f32x16 mfma32(u32x4 a, u32x4 b, f32x16 c) { return __builtin_amdgcn_mfma_f32_32x32x16_bf16(__builtin_bit_cast(bf16x8, a), __builtin_bit_cast(bf16x8, b), c, 0, 0, 0); }
DI u32x2 trd(LAS char* p) { return __builtin_bit_cast(u32x2, __builtin_amdgcn_ds_read_tr16_b64_v4i16((LAS s16x4*)p)); }
DI u32x4 trd2(LAS char* p, int d) { u32x2 a = trd(p), b = trd(p + d); return u32x4{a[0], a[1], b[0], b[1]}; }
DI LAS char* opq(LAS char* p) { asm volatile("" : "+v"(p)); return p; }
DI void dma16(rsrc_t rs, u32 voff, u32 soff, u32 lds) {
  asm volatile("s_mov_b32 m0, %0\n\ts_nop 0\n\tbuffer_load_dwordx4 %1, %2, %3 offen lds" :: "s"(lds), "v"(voff), "s"(rs), "s"(soff) : "m0", "memory");
}
template <int OFF> DI void ald128(u32x4& d, rsrc_t rs, u32 voff, u32 soff) {
  asm volatile("buffer_load_dwordx4 %0, %1, %2, %3 offen offset:%c4" : "=v"(d) : "v"(voff), "s"(rs), "s"(soff), "i"(OFF) : "memory");
}
DI void ald128_nt(u32x4& d, rsrc_t rs, u32 voff, u32 soff) {
  asm volatile("buffer_load_dwordx4 %0, %1, %2, %3 offen nt" : "=v"(d) : "v"(voff), "s"(rs), "s"(soff) : "memory");
}
template <int OFF> DI void ald128_sc1(u32x4& d, rsrc_t rs, u32 voff, u32 soff) {
  asm volatile("buffer_load_dwordx4 %0, %1, %2, %3 offen offset:%c4 sc1" : "=v"(d) : "v"(voff), "s"(rs), "s"(soff), "i"(OFF) : "memory");
}
DI void ald64_sc1(u32x2& d, rsrc_t rs, u32 voff, u32 soff) {
  asm volatile("buffer_load_dwordx2 %0, %1, %2, %3 offen sc1" : "=v"(d) : "v"(voff), "s"(rs), "s"(soff) : "memory");
}
DI void ald64(u32x2& d, rsrc_t rs, u32 voff, u32 soff) {
  asm volatile("buffer_load_dwordx2 %0, %1, %2, %3 offen" : "=v"(d) : "v"(voff), "s"(rs), "s"(soff) : "memory");
}
DI void warm(u32 lds, rsrc_t rs, u32 voff, u32 soff) {
  asm volatile("s_mov_b32 m0, %0\n\ts_nop 0\n\tbuffer_load_dword %1, %2, %3 offen lds" :: "s"(lds), "v"(voff), "s"(rs), "s"(soff) : "m0", "memory");
}
template <int N> DI void waitv() { asm volatile("s_waitcnt vmcnt(%c0)" :: "i"(N) : "memory"); }
template <int N> DI void waitv4(u32x4& a, u32x4& b, u32x4& c, u32x4& d) { asm volatile("s_waitcnt vmcnt(%c4)" : "+v"(a), "+v"(b), "+v"(c), "+v"(d) : "i"(N) : "memory"); }
template <int N> DI void waitv1(u32x4& a) { asm volatile("s_waitcnt vmcnt(%c1)" : "+v"(a) : "i"(N) : "memory"); }
DI void bar() { __builtin_amdgcn_sched_barrier(0); asm volatile("s_waitcnt lgkmcnt(0)\n\ts_barrier" ::: "memory"); __builtin_amdgcn_sched_barrier(0); }
DI u32x4 ld128_sc1(rsrc_t rs, u32 voff, u32 soff) { return __builtin_amdgcn_raw_buffer_load_b128(rs, voff, soff, 16); }
DI u32x2 ld64_sc1(rsrc_t rs, u32 voff, u32 soff) { return __builtin_amdgcn_raw_buffer_load_b64(rs, voff, soff, 16); }
DI void st128_sc1(u32x4 v, rsrc_t rs, u32 voff, u32 soff) { __builtin_amdgcn_raw_buffer_store_b128(v, rs, voff, soff, 16); }
DI void st64_sc1(u32x2 v, rsrc_t rs, u32 voff, u32 soff) { __builtin_amdgcn_raw_buffer_store_b64(v, rs, voff, soff, 16); }
DI float f_(u32 v) { return __builtin_bit_cast(float, v); }
DI u32 u_(float v) { return __builtin_bit_cast(u32, v); }
DI float swap16_add(float a, float b) { auto r = __builtin_amdgcn_permlane16_swap(u_(a), u_(b), false, false); u32 r0 = r[0], r1 = r[1]; return f_(r0) + f_(r1); }
DI float swap32_add(float a, float b) { auto r = __builtin_amdgcn_permlane32_swap(u_(a), u_(b), false, false); u32 r0 = r[0], r1 = r[1]; return f_(r0) + f_(r1); }
DI float swap16_max(float a) { auto r = __builtin_amdgcn_permlane16_swap(u_(a), u_(a), false, false); u32 r0 = r[0], r1 = r[1]; return fmaxf(f_(r0), f_(r1)); }
DI float swap32_max(float a) { auto r = __builtin_amdgcn_permlane32_swap(u_(a), u_(a), false, false); u32 r0 = r[0], r1 = r[1]; return fmaxf(f_(r0), f_(r1)); }
DI float mx2(float a, float b) { return __builtin_amdgcn_fmed3f(a, b, __builtin_inff()); }
template <int CTRL> DI float dpp_add(float a) { return a + f_(__builtin_amdgcn_update_dpp(0, u_(a), CTRL, 0xf, 0xf, true)); }
DI float dot2(u32 a, u32 b, float c) { return __builtin_amdgcn_fdot2_f32_bf16(__builtin_bit_cast(bf16x2, a), __builtin_bit_cast(bf16x2, b), c, false); }

constexpr u64 SPIN_TICKS = 20000;
DI bool spin_out(u64 t0) { __builtin_amdgcn_s_sleep(1); return (__builtin_amdgcn_s_memrealtime() - t0) > SPIN_TICKS; }

#define SB() __builtin_amdgcn_sched_barrier(0)
#define SGB_MFMA(n) __builtin_amdgcn_sched_group_barrier(0x8, n, 0)
#define SGB_DSR(n) __builtin_amdgcn_sched_group_barrier(0x100, n, 0)
#define SGB_DSW(n) __builtin_amdgcn_sched_group_barrier(0x200, n, 0)
#define STAMP(i) do {} while (0)
#define WACC(i, code) do { code; } while (0)

template <int YOUNGER> DI void pubbar(int NC, rsrc_t rws, u32 flag_off, u32 tlo, u32 thi, int tid) {
  waitv<YOUNGER>(); bar();
  if (tid < NC) st64_sc1(u32x2{tlo, thi}, rws, (u32)tid * 128u, flag_off);
}
template <int NF> DI u32x2 peek_flags(rsrc_t rws, u32 off, int lane) { return ld64_sc1(rws, (u32)(lane & (NF - 1)) * 8u, off); }
template <int NF> DI void wait_flags(rsrc_t rws, u32 off, int lane, u32 tlo, u32 thi) {
  const u32 vo = (u32)(lane & (NF - 1)) * 8u;
  const u64 t0 = __builtin_amdgcn_s_memrealtime();
  for (;;) {
    u32x2 f = ld64_sc1(rws, vo, off);
    if (__all(f[0] == tlo && f[1] == thi)) break;
    if (spin_out(t0)) break;
  }
  __builtin_amdgcn_fence(__ATOMIC_ACQUIRE, "wavefront");
}
DI bool gran_ok(u32x4 g, u32 glo, u32 ghi) { return (g[0] & 0xffu) == glo && g[1] == ghi && (g[2] & 0xffu) == glo && g[3] == ghi; }

#define STAMP_PARAM
#define STAMP_ARG
template <int NF> DI void wait_flags_from(u32x2 seen, rsrc_t rws, u32 off, int lane, u32 tlo, u32 thi) {
  if (!__all(seen[0] == tlo && seen[1] == thi)) wait_flags<NF>(rws, off, lane, tlo, thi);
  __builtin_amdgcn_fence(__ATOMIC_ACQUIRE, "wavefront");
}
struct Gath { u32x4 q[4]; };
DI void gath_load(Gath& G, rsrc_t rws, u32 voff, u32 soff) {
#pragma unroll
  for (int k = 0; k < 4; ++k) G.q[k] = ld128_sc1(rws, voff, WS_MODG + soff + (u32)k * 24576u);
}
DI bool gath_ok(const Gath& G, u32 glo, u32 ghi) { return gran_ok(G.q[0], glo, ghi) && gran_ok(G.q[1], glo, ghi) && gran_ok(G.q[2], glo, ghi) && gran_ok(G.q[3], glo, ghi); }
DI u32 gath_val(const Gath& G) {
  const float a = (f_(G.q[0][0] & 0xffffff00u) + f_(G.q[1][0] & 0xffffff00u)) + (f_(G.q[2][0] & 0xffffff00u) + f_(G.q[3][0] & 0xffffff00u));
  const float b = (f_(G.q[0][2] & 0xffffff00u) + f_(G.q[1][2] & 0xffffff00u)) + (f_(G.q[2][2] & 0xffffff00u) + f_(G.q[3][2] & 0xffffff00u));
  return pk2(a, b);
}
DI void mod_issue(u32x4 (&wm)[4], u32x2& cv, rsrc_t rwm, rsrc_t rc, int mq, int mblk, int cw, int lane) {
    const int r0 = 128 * mq + 32 * cw;
#pragma unroll
    for (int i = 0; i < 4; ++i) ald128_nt(wm[i], rwm, (u32)((lane >> 3) * 4 * 6144 + (lane & 7) * 16), (u32)((r0 + i) * 6144 + mblk * 128));
    ald64(cv, rc, (u32)((lane >> 3) * 8), (u32)(r0 * 2));
}
DI void mod_finish(LAS char* L, u32 pred, const u32x4 (&wm)[4], const u32x2& cv, u32 tlo, u32 thi, rsrc_t rws, int mq, int mblk, int cw, int lane) {
    const int g = lane >> 4, i16 = lane & 15;
    float a8[8];
#pragma unroll
    for (int e = 0; e < 8; ++e) a8[e] = 0.f;
#pragma unroll
    for (int i = 0; i < 4; ++i) {
      const float cf = (i & 1) ? bfhi(cv[i >> 1]) : bflo(cv[i >> 1]);
      const float sv = rbf(cf * __builtin_amdgcn_rcpf(1.f + __expf(-cf)));
#pragma unroll
      for (int e = 0; e < 4; ++e) { a8[2 * e] = fmaf(sv, bflo(wm[i][e]), a8[2 * e]); a8[2 * e + 1] = fmaf(sv, bfhi(wm[i][e]), a8[2 * e + 1]); }
    }
#pragma unroll
    for (int e = 0; e < 8; ++e) a8[e] = dpp_add<0x128>(a8[e]);
    float b4[4], b2[2];
#pragma unroll
    for (int e = 0; e < 4; ++e) b4[e] = swap16_add(a8[e], a8[e + 4]);
#pragma unroll
    for (int e = 0; e < 2; ++e) b2[e] = swap32_add(b4[e], b4[e + 2]);
    if (i16 < 8) *(LAS f32x2*)(L + pred + cw * 256 + (i16 * 8 + (g & 1) * 4 + (g >> 1) * 2) * 4) = f32x2{b2[0], b2[1]};
    asm volatile("s_waitcnt lgkmcnt(0)" ::: "memory");
    if (lane == 0) *(volatile LAS u32x2*)(L + pred + 1024 + cw * 8) = u32x2{tlo, thi};
    if (cw == 0) {
      const u64 t0 = __builtin_amdgcn_s_memrealtime();
      for (;;) {
        const u32x2 tg = *(volatile LAS u32x2*)(L + pred + 1024 + (lane & 3) * 8);
        if (__all(tg[0] == tlo && tg[1] == thi)) break;
        if ((__builtin_amdgcn_s_memrealtime() - t0) > SPIN_TICKS) break;
      }
      asm volatile("" ::: "memory");
      LAS float* s = (LAS float*)(L + pred) + lane;
      const float v = (s[0] + s[64]) + (s[128] + s[192]);
      st64_sc1(u32x2{((u_(v) + 0x80u) & 0xffffff00u) | (tlo & 0xffu), (tlo >> 8) | (thi << 24)}, rws, (u32)(lane * 8), WS_MODG + (u32)(mq * 24576 + mblk * 512));
    }
}
DI void front(const Params& p, LAS char* L, const int tid, const int w, const int b, u32x4 (&wr)[18], u32& tlo, u32& thi STAMP_PARAM) {
  const bool isC = w < 4;
  f32x2 xh[10][4];
  const u32 Lb = (u32)(size_t)L;
  const int lane = tid & 63, cw = w & 3;
  const int xcd = b & 7, idx = b >> 3;
  const int tt = 8 * (xcd >> 1) + (idx >> 2), hgl = idx & 3, hg = 4 * (xcd & 1) + hgl;
  const int jt = 8 * (xcd & 1) + 4 * (tt & 1) + hgl;
  const int T = tt >> 1, cb = hg;
  STAMP(0);
  const rsrc_t rx = mk(p.x, S_ * D_ * 2), rw1 = mk(p.wqkv, D_ * 3 * D_ * 2), rwo = mk(p.wo, D_ * D_ * 2);
  const rsrc_t rwg = mk(p.wg, D_ * F_ * 2), rwu = mk(p.wu, D_ * F_ * 2), rwd = mk(p.wd, F_ * D_ * 2);
  const rsrc_t rc = mk(p.c, D_ * 2), rwm = mk(p.wmod, D_ * 6 * D_ * 2), rout = mk(p.out, S_ * D_ * 2), rws = mk(p.ws, WS_BYTES);
  const int g = lane >> 4, i16 = lane & 15, h = lane >> 5, tok = lane & 31, t4 = tid & 255;

  const int nrot = 2 * hgl, rot = 2 * (tt & 7);
  const u32 w1v = (u32)((lane >> 3) * 3072 + (((((lane >> 1) & 3) ^ ((lane >> 4) & 1)) ^ ((cw & 1) << 1)) * 32) + (lane & 1) * 16);
  auto w1slot = [](int n) { return n < 6 ? L_RW + n * L_W1S : (n < 12 ? (n - 6) * L_W1S : L_RW + (n - 12) * L_W1S); };
  {
    u64 pc; asm volatile("s_getpc_b64 %0" : "=s"(pc));
    const rsrc_t rcode = mk((const void*)(pc & ~(u64)255), CODE_BYTES);
    warm(Lb + L_SCRAP, rcode, (u32)((idx * 8 + w) * 2 + (lane & 1)) * 128u, 0u);
  }
  u32x2 ev; ald64_sc1(ev, rws, 0u, WS_E);
  const bool prod = isC && idx < 8;
  const int mq = idx & 3, mblk = xcd * 2 + (idx >> 2);
  auto dma_w1 = [&](int i) {
    const int n = (i < 9 ? 6 : 0) + 2 * ((i % 9) / 3) + (w >> 2), pp = i % 3;
    dma16(rw1, w1v, (u32)((32 * ((n + rot) & 15) + 8 * cw) * 3072 + pp * 1024 + hg * 128), Lb + (u32)((i < 9 ? (n - 6) * L_W1S : L_RW + n * L_W1S) + pp * 4096 + cw * 1024));
  };
  u32x2 cv; u32x4 wm[4];
  if (prod) mod_issue(wm, cv, rwm, rc, mq, mblk, cw, lane);
  u32x4 xr[10];
#pragma unroll
  for (int n = 0; n < 10; ++n) {
    int r8 = n + nrot; r8 = r8 >= 10 ? r8 - 10 : r8;
    int row = 64 * tt - 16 + 8 * r8 + (lane >> 3); row = row < 0 ? 0 : row;
    ald128_nt(xr[n], rx, (u32)(row * 1024 + (lane & 7) * 16), (u32)(w * 128));
  }
  constexpr int Y0 = 0;
  STAMP(1);
  if (prod) {
    asm volatile("s_waitcnt vmcnt(%c6)" : "+v"(wm[0]), "+v"(wm[1]), "+v"(wm[2]), "+v"(wm[3]), "+v"(cv), "+v"(ev) : "i"(10 + Y0) : "memory");
    const u64 e1 = (((u64)__builtin_amdgcn_readfirstlane(ev[1]) << 32) | (u64)__builtin_amdgcn_readfirstlane(ev[0])) + 1ull;
    mod_finish(L, L_PRED, wm, cv, (u32)e1, (u32)(e1 >> 32), rws, mq, mblk, cw, lane);
  }
  STAMP(2);
  STAMP(3);
  {
    const int c8 = lane & 7, r8l = lane >> 3;
    float pv[10];
#pragma unroll
    for (int n = 0; n < 10; ++n) {
      waitv1<9>(xr[n]);
#pragma unroll
      for (int e = 0; e < 4; ++e) xh[n][e] = f32x2{bflo(xr[n][e]), bfhi(xr[n][e])};
      const f32x2 a1 = (xh[n][0] + xh[n][1]) + (xh[n][2] + xh[n][3]);
      const f32x2 a2 = __builtin_elementwise_fma(xh[n][0], xh[n][0], xh[n][1] * xh[n][1]) + __builtin_elementwise_fma(xh[n][2], xh[n][2], xh[n][3] * xh[n][3]);
      const float s1 = a1[0] + a1[1], s2 = a2[0] + a2[1];
      const float keep = (c8 & 1) ? s2 : s1, give = (c8 & 1) ? s1 : s2;
      float v = keep + f_(__builtin_amdgcn_update_dpp(0, u_(give), 0xB1, 0xf, 0xf, true));
      v = dpp_add<0x4E>(v);
      pv[n] = v + f_(__builtin_amdgcn_update_dpp(0, u_(v), 0x104, 0xf, 0xf, true));
      asm volatile("" : "+v"(pv[n])); dma_w1(n); SB();
    }
    asm volatile("" : "+v"(ev));
    {
      const u64 e1 = (((u64)__builtin_amdgcn_readfirstlane(ev[1]) << 32) | (u64)__builtin_amdgcn_readfirstlane(ev[0])) + 1ull;
      tlo = (u32)e1; thi = (u32)(e1 >> 32);
    }
    {
    const rsrc_t rt = w == 0 ? rwo : w == 1 ? rwg : w == 2 ? rwu : w == 3 ? rwd : w == 4 ? rout : rws;
    warm(Lb + L_SCRAP, rt, (u32)lane << 20, 0u);
  }

    if (c8 < 2) {
#pragma unroll
      for (int n = 0; n < 10; ++n) *(LAS float*)(L + L_LNP + (n * 8 + r8l) * 80 + (w * 2 + c8) * 4) = pv[n];
    }
    bar();
    f32x4 qa[2][4];
#pragma unroll
    for (int k = 0; k < 2; ++k)
#pragma unroll
      for (int e = 0; e < 4; ++e) qa[k][e] = *(const LAS f32x4*)(L + L_LNP + (k ? 64 + i16 : lane) * 80 + e * 16);
    dma_w1(10);
    float mus[2];
#pragma unroll
    for (int k = 0; k < 2; ++k) {
      const f32x4 a = (qa[k][0] + qa[k][1]) + (qa[k][2] + qa[k][3]);
      const float mu = (a[0] + a[2]) * (1.f / 512.f);
      const float var = fmaxf((a[1] + a[3]) * (1.f / 512.f) - mu * mu, 0.f);
      const float rs = __builtin_amdgcn_rsqf(var + 1e-5f);
      *(LAS f32x2*)(L + L_RSNB + (k ? 64 + i16 : lane) * 8) = f32x2{rs, -mu * rs};
      mus[k] = mu;
    }
    asm volatile("" ::: "memory");
    f32x2 rn[10];
#pragma unroll
    for (int n = 0; n < 10; ++n) rn[n] = *(const LAS f32x2*)(L + L_RSNB + (n * 8 + r8l) * 8);
    if (w == 0) {
#pragma unroll
      for (int k = 0; k < 2; ++k) { int r = (k ? 64 + i16 : lane) + 8 * nrot; r = r >= 80 ? r - 80 : r; ((LAS float*)(L + L_MU1 - 64))[r] = mus[k]; }
    }
#pragma unroll
    for (int n = 0; n < 10; ++n) {
#pragma unroll
      for (int e = 0; e < 4; ++e) xh[n][e] = __builtin_elementwise_fma(xh[n][e], f32x2{rn[n][0], rn[n][0]}, f32x2{rn[n][1], rn[n][1]});
      if (n < 7) { asm volatile("" : "+v"(xh[n][0]), "+v"(xh[n][1]), "+v"(xh[n][2]), "+v"(xh[n][3])); dma_w1(11 + n); SB(); }
    }
  }
#pragma unroll
  for (int n = 0; n < 10; ++n) asm volatile("" : "+v"(xh[n][0]), "+v"(xh[n][1]), "+v"(xh[n][2]), "+v"(xh[n][3]));
  STAMP(4);
  const u32 glo = tlo & 0xffu, ghi = (tlo >> 8) | (thi << 24);
  const u32 gvo = (u32)(w * 512 + tok * 16 + h * 4096);
  Gath g1_;
#pragma unroll
  for (int k = 0; k < 4; ++k) ald128_sc1<0>(g1_.q[k], rws, gvo, WS_MODG + (u32)k * 24576u);
  waitv<13>();
  bar();
  if (isC) {
    LAS char* w1rd = L + ((8 * g + (i16 >> 2)) * 128 + ((cw ^ (((i16 >> 3) & 1) | ((g & 1) << 1))) * 32) + (lane & 3) * 8);

#pragma unroll
    for (int n = 0; n < 6; ++n)
#pragma unroll
      for (int pp = 0; pp < 3; ++pp) wr[3 * n + pp] = trd2(w1rd + n * L_W1S + pp * 4096, 512);
  }
  STAMP(5);
  waitv4<0>(g1_.q[0], g1_.q[1], g1_.q[2], g1_.q[3]);
  if (!__all(gath_ok(g1_, glo, ghi))) {
    const u64 t0 = __builtin_amdgcn_s_memrealtime();
    for (;;) {
      gath_load(g1_, rws, gvo, 0u);
      if (__all(gath_ok(g1_, glo, ghi))) break;
      if (spin_out(t0)) break;
    }
  }
  {
    const u32 v2 = gath_val(g1_);
    const float a = bflo(v2), c = bfhi(v2);
    *(LAS f32x2*)(L + (h ? L_G1 : L_S1) + (w * 32 + tok) * 8) = h ? f32x2{rbf(1.f + a), rbf(1.f + c)} : f32x2{a, c};
  }
  STAMP(6);
  bar();
  {
    const int c8 = lane & 7, r8l = lane >> 3;
    const LAS f32x4* gp = (const LAS f32x4*)(L + L_G1 + (w * 64 + c8 * 8) * 4);
    const LAS f32x4* sp = (const LAS f32x4*)(L + L_S1 + (w * 64 + c8 * 8) * 4);
    const f32x4 g0 = gp[0], g1 = gp[1], s0 = sp[0], s1 = sp[1];
    LAS char* ab = L + L_AIMG + (2 * w + (c8 >> 2)) * 1024 + (c8 & 3) * 256 + ((r8l ^ c8) * 16);
#pragma unroll
    for (int n = 0; n < 10; ++n) {
      const f32x2 h0 = __builtin_elementwise_fma(xh[n][0], f32x2{g0[0], g0[1]}, f32x2{s0[0], s0[1]}), h1 = __builtin_elementwise_fma(xh[n][1], f32x2{g0[2], g0[3]}, f32x2{s0[2], s0[3]});
      const f32x2 h2 = __builtin_elementwise_fma(xh[n][2], f32x2{g1[0], g1[1]}, f32x2{s1[0], s1[1]}), h3 = __builtin_elementwise_fma(xh[n][3], f32x2{g1[2], g1[3]}, f32x2{s1[2], s1[3]});
      const u32x4 o = u32x4{pk2(h0[0], h0[1]), pk2(h1[0], h1[1]), pk2(h2[0], h2[1]), pk2(h3[0], h3[1])};
      int r8 = n + nrot; r8 = r8 >= 10 ? r8 - 10 : r8;
      *(LAS u32x4*)(ab + (r8 >> 1) * 16384 + (r8 & 1) * 128) = o;
    }
  }
  bar();
  STAMP(7);
}

template <bool isC> DI void role_body(const Params& p, LAS char* L, const int tid, const int w, const int b, u32x4 (&wr)[18], const u32 tlo, const u32 thi STAMP_PARAM) {
  const u32 Lb = (u32)(size_t)L;
  const int lane = tid & 63, cw = w & 3;
  const int xcd = b & 7, idx = b >> 3;
  const int tt = 8 * (xcd >> 1) + (idx >> 2), hgl = idx & 3, hg = 4 * (xcd & 1) + hgl;
  const int jt = 8 * (xcd & 1) + 4 * (tt & 1) + hgl;
  const int T = tt >> 1, cb = hg;
  const rsrc_t rx = mk(p.x, S_ * D_ * 2), rw1 = mk(p.wqkv, D_ * 3 * D_ * 2), rwo = mk(p.wo, D_ * D_ * 2);
  const rsrc_t rwg = mk(p.wg, D_ * F_ * 2), rwu = mk(p.wu, D_ * F_ * 2), rwd = mk(p.wd, F_ * D_ * 2);
  const rsrc_t rc = mk(p.c, D_ * 2), rwm = mk(p.wmod, D_ * 6 * D_ * 2), rout = mk(p.out, S_ * D_ * 2), rws = mk(p.ws, WS_BYTES);
  const int g = lane >> 4, i16 = lane & 15, h = lane >> 5, tok = lane & 31, t4 = tid & 255;

  const int nrot = 2 * hgl, rot = 2 * (tt & 7);
  const u32 w1v = (u32)((lane >> 3) * 3072 + (((((lane >> 1) & 3) ^ ((lane >> 4) & 1)) ^ ((cw & 1) << 1)) * 32) + (lane & 1) * 16);
  auto w1slot = [](int n) { return n < 6 ? L_RW + n * L_W1S : (n < 12 ? (n - 6) * L_W1S : L_RW + (n - 12) * L_W1S); };
  LAS char* w1rd = L + ((8 * g + (i16 >> 2)) * 128 + ((cw ^ (((i16 >> 3) & 1) | ((g & 1) << 1))) * 32) + (lane & 3) * 8);
  const u32 glo = tlo & 0xffu, ghi = (tlo >> 8) | (thi << 24);
  const int p0 = jt & 3, rotg = (4 * (T & 3) + 2 * (jt & 7)) & 15, rotd = (tt + hgl) & 3;
  const u32 wov = (u32)((lane >> 3) * 1024 + ((((lane >> 2) & 1) ^ ((lane >> 4) & 1)) * 64) + (lane & 3) * 16);
  const int mrow = (lane >> 4) & 1, q4 = i16 >> 2;
  const u32 p1base = (u32)(mrow * 4096 + (4 * h + q4) * 128 + (lane & 3) * 8), p1x = (u32)((q4 >> 1) | (mrow << 1));
  const u32 p2base = (u32)(mrow * L_P2M + (4 * h + q4) * 96 + (lane & 3) * 8);

  if constexpr (!isC) {
    const bool prod2 = idx >= 8 && idx < 24;
    const int mq = idx & 3, mblk = 16 + xcd * 4 + ((idx - 8) >> 2);
    u32x2 cv; u32x4 wm[4];
    if (prod2) mod_issue(wm, cv, rwm, rc, mq, mblk, cw, lane);
    bar();
#pragma unroll 1
    for (int n = 0; n < 4; ++n)
#pragma unroll
      for (int pp = 0; pp < 3; ++pp) dma16(rw1, w1v, (u32)((32 * ((12 + n + rot) & 15) + 8 * cw) * 3072 + pp * 1024 + hg * 128), Lb + (u32)(L_RW + n * L_W1S + pp * 4096 + cw * 1024));
    warm(Lb + L_SCRAP, cw == 0 ? rwg : cw == 1 ? rwu : cw == 2 ? rwd : rwo, (u32)(b + 256 * lane) * 128u, 0u);
    if (prod2) {
      asm volatile("s_waitcnt vmcnt(13)" : "+v"(wm[0]), "+v"(wm[1]), "+v"(wm[2]), "+v"(wm[3]), "+v"(cv) :: "memory");
      mod_finish(L, L_G1, wm, cv, tlo, thi, rws, mq, mblk, cw, lane);
    }
    if (prod2 && cw == 0) waitv<2>(); else waitv<1>();
    bar();
    STAMP(8);
    STAMP(9);
    bar();
    const u32 gch = h ? (u32)((cw == 0 ? 1024 : cw == 1 ? 2048 : 2560) + 64 * cb + 2 * tok) : (u32)((lane & 16 ? 1536 : 2048) + 32 * T + 2 * i16);
    Gath g2_;
#pragma unroll
    for (int k = 0; k < 4; ++k) ald128_sc1<0>(g2_.q[k], rws, gch * 8u, WS_MODG + (u32)k * 24576u);
    const rsrc_t rgu = (cw & 1) ? rwu : rwg;
    const int uc = 7 * (cw >> 1) + ((lane & 7) < 7 ? (lane & 7) : 6);
    u32x4 uw[4];
    u32 uso = (u32)(T * (32 * 3584) + jt * 224); asm volatile("" : "+s"(uso));
#pragma unroll
    for (int i = 0; i < 4; ++i) ald128<0>(uw[i], rgu, (u32)((lane >> 3) * 3584 + uc * 16), uso + (u32)(8 * i * 3584));
#pragma unroll 1
    for (int q = 0; q < 16; ++q) { const int qq = (q + rot) & 15; dma16(rwo, wov, (u32)((128 * cw + 8 * qq) * 1024 + cb * 128), Lb + (u32)(L_WO + (16 * cw + qq) * 1024)); }
#pragma unroll
    for (int q = 0; q < 2; ++q) dma16(rx, (u32)((lane >> 3) * 1024 + (lane & 7) * 16), (u32)((64 * tt + 16 * cw + 8 * q) * 1024 + cb * 128), Lb + (u32)(L_XRES + (2 * cw + q) * 1024));
    const u32 p1v = (u32)((lane >> 3) * 3584 + ((((lane >> 1) & 3) ^ ((lane >> 4) & 1)) * 32) + (lane & 1) * 16);
    auto dma_p1 = [&](int k) {
      const u32 so = (u32)((32 * ((k + rotg) & 15) + 8 * cw) * 3584 + jt * 224 + p0 * 32);
      dma16(rwg, p1v, so, Lb + (u32)(k * L_P1B + cw * 1024));
      dma16(rwu, p1v ^ 64u, so, Lb + (u32)(k * L_P1B + 4096 + cw * 1024));
    };
    dma_p1(0); dma_p1(1);
    bar();
    {
      waitv4<26>(g2_.q[0], g2_.q[1], g2_.q[2], g2_.q[3]);
      if (!__all(gath_ok(g2_, glo, ghi))) {
        const u64 t0 = __builtin_amdgcn_s_memrealtime();
        for (;;) {
          gath_load(g2_, rws, gch * 8u, 0u);
          if (__all(gath_ok(g2_, glo, ghi))) break;
          if (spin_out(t0)) break;
        }
      }
      const u32 v2 = gath_val(g2_);
      const bool one = h ? cw == 1 : !(lane & 16);
      const float a = bflo(v2), c = bfhi(v2);
      const u32 dst = h ? (cw == 0 ? L_GA : cw == 1 ? L_G2C : cw == 2 ? L_GBV : L_SCRAP) + tok * 8 : L_SL + cw * 256 + tok * 8;
      *(LAS f32x2*)(L + dst) = one ? f32x2{rbf(1.f + a), rbf(1.f + c)} : f32x2{a, c};
    }
    {
      waitv4<22>(uw[0], uw[1], uw[2], uw[3]);
      float a[16];
#pragma unroll
      for (int e = 0; e < 16; ++e) a[e] = 0.f;
#pragma unroll
      for (int i = 0; i < 4; ++i) {
        const float gk = ((const LAS float*)(L + L_SL + cw * 256))[8 * i + (lane >> 3)], sk = ((const LAS float*)(L + L_SL + cw * 256))[32 + 8 * i + (lane >> 3)];
#pragma unroll
        for (int e = 0; e < 4; ++e) {
          const float lo = bflo(uw[i][e]), hi = bfhi(uw[i][e]);
          a[2 * e] = fmaf(gk, lo, a[2 * e]); a[2 * e + 1] = fmaf(gk, hi, a[2 * e + 1]);
          a[8 + 2 * e] = fmaf(sk, lo, a[8 + 2 * e]); a[9 + 2 * e] = fmaf(sk, hi, a[9 + 2 * e]);
        }
      }
#pragma unroll
      for (int e = 0; e < 16; ++e) a[e] = dpp_add<0x128>(a[e]);
      float b8[8], ut4[4];
#pragma unroll
      for (int e = 0; e < 8; ++e) b8[e] = swap16_add(a[e], a[e + 8]);
#pragma unroll
      for (int e = 0; e < 4; ++e) ut4[e] = swap32_add(b8[e], b8[e + 4]);
      auto r0 = __builtin_amdgcn_permlane16_swap(u_(ut4[0]), u_(ut4[2]), false, false);
      auto r1 = __builtin_amdgcn_permlane16_swap(u_(ut4[1]), u_(ut4[3]), false, false);
      const u32 a0 = r0[0], b0 = r0[1], a1 = r1[0], b1 = r1[1];
      const int pr = uc >> 1, slot = pr >= p0 && pr < p0 + 4 ? pr - p0 : (pr < p0 ? 4 + pr : pr);
      if (i16 < 7) st128_sc1(u32x4{a0, b0, a1, b1}, rws, (u32)((slot * 32 + (cw & 1) * 16 + (uc & 1) * 8 + h * 4 + (g & 1) * 2) * 8), WS_UT + (u32)((jt * 16 + T) * 1792));
    }
    waitv<4>(); STAMP(10); bar();
#pragma unroll 1
    for (int k = 2; k < 8; ++k) dma_p1(k);
    waitv<12>(); STAMP(11); bar();
    if (tid == 256) st64_sc1(u32x2{tlo, thi}, rws, 0u, WS_FU + (u32)(jt * 256 + T * 8));
#pragma unroll 1
    for (int k = 8; k < 16; ++k) dma_p1(k);
    waitv<16>(); bar();
    u32 p2v[3];
#pragma unroll
    for (int i = 0; i < 3; ++i) { const int n = 64 * i + lane, row = n / 6, c = n - row * 6; p2v[i] = (u32)(row * 3584 + c * 16 + ((c >> 1) >= p0 ? 128 : 0)); }
    auto dma_p2 = [&](int k0, int k1) {
      if (ELIM & 1) return;
#pragma unroll 1
      for (int k = k0; k < k1; ++k) {
        const int pos = 2 * k + (cw >> 1);
#pragma unroll
        for (int i = 0; i < 3; ++i) dma16(rgu, p2v[i], (u32)(((pos + rotg) & 15) * 32 * 3584 + jt * 224), Lb + (u32)(pos * L_P2B + (cw & 1) * L_P2M + i * 1024));
      }
    };
    auto dma_wd = [&](int u) {
      if (ELIM & 4) return;
#pragma unroll
      for (int q = 0; q < 8; ++q) {
        const int n = 8 * (u - 4) + q;
        const int jj = ((u < 4 ? u : n / 6) + rotd) & 3, r6 = n % 6;
        const int row = u < 4 ? 112 * jj + 16 * jj + 8 * q : 112 * jj + 8 * r6 + ((r6 >> 1) >= jj ? 64 : 0);
        dma16(rwd, wov, (u32)((448 * cw + row) * 1024 + cb * 128), Lb + (u32)(wd_unit_off(u) + cw * 8192 + q * 1024));
      }
    };
    {
      wait_flags<16>(rws, WS_FU + (u32)(jt * 256), lane, tlo, thi);
      u32x2 pt[16];
#pragma unroll
      for (int k = 0; k < 16; ++k) ald64_sc1(pt[k], rws, (u32)(t4 < 224 ? t4 : 0) * 8u, WS_UT + (u32)((jt * 16 + k) * 1792));
      SB();
      float uu = 0.f, tt_ = 0.f;
      waitv<0>();
#pragma unroll
      for (int k = 0; k < 16; ++k) { asm volatile("" : "+v"(pt[k])); uu += f_(pt[k][0]); tt_ += f_(pt[k][1]); }
      const u32 hi = pk2(uu, tt_), lo = pk2(uu - bflo(hi), tt_ - bfhi(hi));
      const u32 u1 = hi & 0xffffu, t1 = hi >> 16, u2 = lo & 0xffffu, t2 = lo >> 16;
      const u32x4 o = u32x4{u1 | (u1 << 16), u2 | (u2 << 16), t1 | (t1 << 16), t2 | (t2 << 16)};
      if (t4 < 224) { LAS char* d = L + L_FIX + (t4 >> 5) * 1024 + (t4 & 31) * 16; *(LAS u32x4*)d = o; *(LAS u32x4*)(d + 512) = o; }
    }
    STAMP(12); bar();
    dma_p2(0, 5);
    if (b == 0 && cw == 3) {
      const u64 t0 = __builtin_amdgcn_s_memrealtime();
      for (;;) {
        bool ok = true;
#pragma unroll
        for (int k = 0; k < 4; ++k) { const int f = 64 * k + lane; const u32x2 v = ld64_sc1(rws, (u32)((f >> 3) * 1024 + (f & 7) * 8), WS_FO); ok = ok && v[0] == tlo && v[1] == thi; }
        if (__all(ok)) break;
        if (spin_out(t0)) break;
      }
      if (lane == 0) st64_sc1(u32x2{tlo, thi}, rws, 0u, WS_E);
    }
    waitv<0>(); STAMP(15); bar();
    dma_p2(5, 8);
    bar();
    dma_wd(0);
    waitv<8>(); STAMP(16); bar();
    dma_wd(1);
    bar();
    dma_wd(2);
    waitv<16>(); bar();
    dma_wd(3);
    waitv<16>(); STAMP(17); bar();
    dma_wd(4);
    waitv<16>(); bar();
    dma_wd(5);
    waitv<16>(); bar();
    dma_wd(6);
    waitv<16>(); STAMP(18); bar();
    waitv<8>(); bar();
    waitv<0>(); bar();
    bar();
  } else {
    f32x4 m0, m1, m0f;
#pragma unroll
    for (int r = 0; r < 4; ++r) {
      const int d1 = i16 - 4 * g - r, d0 = d1 + 16;
      m0[r] = (d0 <= 14 && !(d0 & 1)) ? 0.f : -1e30f;
      m1[r] = (d1 >= 0 && d1 <= 14 && !(d1 & 1)) ? 0.f : -1e30f;
      m0f[r] = tt == 0 ? -1e30f : m0[r];
    }
    f32x4 Kt[5], Qt[4], Vn[5];
#pragma unroll
    for (int r = 0; r < 5; ++r) { Kt[r] = f32x4{0, 0, 0, 0}; Vn[r] = f32x4{0, 0, 0, 0}; if (r < 4) Qt[r] = f32x4{0, 0, 0, 0}; }
    {
      u32x4 a[2][5], wl[2][3];
      LAS char* const w1a = opq(w1rd + L_RW); LAS char* const w1b = opq(w1rd + L_RW + 4 * L_W1S);
      auto ld = [&](int n, int bi) {
        LAS char* ab = L + L_AIMG + ((n + rot) & 15) * 1024 + g * 256 + ((i16 ^ (((n & 1) << 2) | g)) * 16);
#pragma unroll
        for (int rb = 0; rb < 5; ++rb) a[bi][rb] = *(const LAS u32x4*)(ab + rb * 16384);
        if (n < 6 || n >= 12) {
#pragma unroll
          for (int pp = 0; pp < 3; ++pp) wl[bi][pp] = trd2((n % 6 < 4 ? w1a : w1b) + (n % 6 % 4) * L_W1S + pp * 4096, 512);
        }
      };
      ld(0, 0); SB();
#pragma unroll
      for (int n = 0; n < 16; ++n) {
        if (n == 3) bar();
        if (n == 8) asm volatile("" : "+v"(m0), "+v"(m1), "+v"(m0f));
        if (n == 11) WACC(24, bar());
        if (n < 15) ld(n + 1, (n + 1) & 1);
        const bool rg = n >= 6 && n < 12;
        const u32x4 wq = rg ? wr[rg ? 3 * (n - 6) : 0] : wl[n & 1][0], wk = rg ? wr[rg ? 3 * (n - 6) + 1 : 0] : wl[n & 1][1], wv = rg ? wr[rg ? 3 * (n - 6) + 2 : 0] : wl[n & 1][2];
#pragma unroll
        for (int rb = 0; rb < 5; ++rb) {
          Kt[rb] = mfma16(wk, a[n & 1][rb], Kt[rb]);
          Vn[rb] = mfma16(a[n & 1][rb], wv, Vn[rb]);
          if (rb > 0) Qt[rb - 1] = mfma16(wq, a[n & 1][rb], Qt[rb - 1]);
        }
#pragma unroll
        for (int k = 0; k < 11; ++k) { SGB_MFMA(1); SGB_DSR(1); }
        SGB_MFMA(3);
        SB();
      }
    }
    bar();
    STAMP(8);
    const int tb = cw & 1, kh = cw >> 1;
    {
      u32x2 kp[5], vp[5], qp[4];
#pragma unroll
      for (int r = 0; r < 5; ++r) { kp[r] = u32x2{pk2(Kt[r][0], Kt[r][1]), pk2(Kt[r][2], Kt[r][3])}; if (r < 4) qp[r] = u32x2{pk2(Qt[r][0], Qt[r][1]), pk2(Qt[r][2], Qt[r][3])}; }
      f32x4 s0[4], s1[4];
#pragma unroll
      for (int qb = 0; qb < 4; ++qb) { s0[qb] = mfma16k16(kp[qb], qp[qb], qb == 0 ? m0f : m0); s1[qb] = mfma16k16(kp[qb + 1], qp[qb], m1); }
#pragma unroll
      for (int r = 0; r < 5; ++r) vp[r] = u32x2{pk2(Vn[r][0], Vn[r][1]), pk2(Vn[r][2], Vn[r][3])};
      float mx[4];
#pragma unroll
      for (int qb = 0; qb < 4; ++qb) mx[qb] = mx2(mx2(mx2(s0[qb][0], s0[qb][1]), mx2(s0[qb][2], s0[qb][3])), mx2(mx2(s1[qb][0], s1[qb][1]), mx2(s1[qb][2], s1[qb][3])));
#pragma unroll
      for (int qb = 0; qb < 4; ++qb) { auto r = __builtin_amdgcn_permlane16_swap(u_(mx[qb]), u_(mx[qb]), false, false); const u32 r0 = r[0], r1 = r[1]; mx[qb] = mx2(f_(r0), f_(r1)); }
#pragma unroll
      for (int qb = 0; qb < 4; ++qb) { auto r = __builtin_amdgcn_permlane32_swap(u_(mx[qb]), u_(mx[qb]), false, false); const u32 r0 = r[0], r1 = r[1]; mx[qb] = mx2(f_(r0), f_(r1)); }
      const float cs = 0.25f * 1.44269504088896f;
      const u32x4 ones = u32x4{0x3f803f80u, 0x3f803f80u, 0x3f803f80u, 0x3f803f80u};
      const f32x4 z = f32x4{0, 0, 0, 0};
      f32x4 ot[4], sm[4];
#pragma unroll
      for (int qb = 0; qb < 4; ++qb) {
        const float nm = -mx[qb] * cs;
        float e0[4], e1[4];
#pragma unroll
        for (int r = 0; r < 4; ++r) { e0[r] = __builtin_amdgcn_exp2f(fmaf(s0[qb][r], cs, nm)); e1[r] = __builtin_amdgcn_exp2f(fmaf(s1[qb][r], cs, nm)); }
        const u32x4 pf = u32x4{pk2(e0[0], e0[1]), pk2(e0[2], e0[3]), pk2(e1[0], e1[1]), pk2(e1[2], e1[3])};
        sm[qb] = mfma16(ones, pf, z);
        ot[qb] = mfma16(u32x4{vp[qb][0], vp[qb][1], vp[qb + 1][0], vp[qb + 1][1]}, pf, z);
      }
      u32x2 op[4];
#pragma unroll
      for (int qb = 0; qb < 4; ++qb) {
        const float ri = __builtin_amdgcn_rcpf(sm[qb][0]);
        op[qb] = u32x2{pk2(ot[qb][0] * ri, ot[qb][1] * ri), pk2(ot[qb][2] * ri, ot[qb][3] * ri)};
      }
#pragma unroll
      for (int t2 = 0; t2 < 2; ++t2) {
        auto r0 = __builtin_amdgcn_permlane16_swap(op[2 * t2][0], op[2 * t2 + 1][0], false, false);
        auto r1 = __builtin_amdgcn_permlane16_swap(op[2 * t2][1], op[2 * t2 + 1][1], false, false);
        const u32 a0 = r0[0], b0 = r0[1], a1 = r1[0], b1 = r1[1];
        st128_sc1(u32x4{a0, a1, b0, b1}, rws, (u32)(lane * 16), WS_O + (u32)(((2 * tt + t2) * 32 + 4 * hg + cw) * 1024));
      }
    }
    STAMP(9);
    pubbar<0>(8, rws, WS_FO + (u32)(tt * 1024 + hg * 8), tlo, thi, tid);
    STAMP(10);
    wait_flags<8>(rws, WS_FO + (u32)(tt * 1024 + cb * 128), lane, tlo, thi);
    STAMP(11);
    u32 x1p[8];
    {
      u32x4 of[16];
      const u32 ob = WS_O + (u32)(((2 * tt + tb) * 32 + 16 * kh) * 1024);
#pragma unroll
      for (int s = 0; s < 16; ++s) { of[s] = ld128_sc1(rws, (u32)(lane * 16), ob + (u32)(((s + 4 * hgl) & 15) * 1024)); SB(); }
      SB();
      WACC(24, bar());
      LAS char* q0 = L + (L_WO + (256 * kh + 8 * h + q4) * 128 + mrow * 32 + (lane & 3) * 8);
      LAS char* qa = q0 + ((q4 >> 1) ^ kh) * 64; LAS char* qb = q0 + ((q4 >> 1) ^ kh ^ 1) * 64;
      f32x16 a0, a1;
#pragma unroll
      for (int r = 0; r < 16; ++r) { a0[r] = 0.f; a1[r] = 0.f; }
      u32x4 wl[2][2];
      wl[0][0] = trd2(qa + hgl * 8192, 512); wl[0][1] = trd2(qb + hgl * 8192, 512); SB();
#pragma unroll
      for (int s = 0; s < 16; ++s) {
        if (s < 15) { const int o2 = ((((s + 1) >> 2) + hgl) & 3) * 8192 + ((s + 1) & 3) * 2048; wl[(s + 1) & 1][0] = trd2(qa + o2, 512); wl[(s + 1) & 1][1] = trd2(qb + o2, 512); }
        a0 = mfma32(wl[s & 1][0], of[s], a0); a1 = mfma32(wl[s & 1][1], of[s], a1);
        SGB_MFMA(1); SGB_DSR(2); SGB_MFMA(1); SGB_DSR(2);
        SB();
      }
      u32x2 xres[4];
#pragma unroll
      for (int q = 0; q < 4; ++q) xres[q] = *(const LAS u32x2*)(L + L_XRES + (32 * tb + tok) * 128 + (32 * kh + 8 * q + 4 * h) * 2);
      const f32x16 snd = a1, acc = a0;
#pragma unroll
      for (int q = 0; q < 4; ++q) *(LAS f32x4*)(L + L_RED2 + cw * 4096 + q * 1024 + lane * 16) = f32x4{snd[4 * q], snd[4 * q + 1], snd[4 * q + 2], snd[4 * q + 3]};
      STAMP(19);
      bar();
      STAMP(20);
      const float mu1 = ((const LAS float*)(L + L_MU1))[32 * tb + tok];
      float s1 = 0.f, s2 = 0.f; u32 yp[8];
#pragma unroll
      for (int q = 0; q < 4; ++q) {
        const f32x4 o4 = *(const LAS f32x4*)(L + L_RED2 + (cw ^ 2) * 4096 + q * 1024 + lane * 16);
        const u32x2 xq = xres[q];
        const f32x4 ga4 = *(const LAS f32x4*)(L + L_GA + (32 * kh + 8 * q + 4 * h) * 4), g24 = *(const LAS f32x4*)(L + L_G2C + (32 * kh + 8 * q + 4 * h) * 4);
        const float v0 = fmaf(ga4[0], acc[4 * q] + o4[0], bflo(xq[0])), v1 = fmaf(ga4[1], acc[4 * q + 1] + o4[1], bfhi(xq[0]));
        const float v2 = fmaf(ga4[2], acc[4 * q + 2] + o4[2], bflo(xq[1])), v3 = fmaf(ga4[3], acc[4 * q + 3] + o4[3], bfhi(xq[1]));
        x1p[2 * q] = pk2(v0, v1); x1p[2 * q + 1] = pk2(v2, v3);
        const float d0 = bflo(x1p[2 * q]) - mu1, d1 = bfhi(x1p[2 * q]) - mu1, d2 = bflo(x1p[2 * q + 1]) - mu1, d3 = bfhi(x1p[2 * q + 1]) - mu1;
        s1 += (d0 + d1) + (d2 + d3); s2 = fmaf(d0, d0, fmaf(d1, d1, fmaf(d2, d2, fmaf(d3, d3, s2))));
        yp[2 * q] = pk2(d0 * g24[0], d1 * g24[1]); yp[2 * q + 1] = pk2(d2 * g24[2], d3 * g24[3]);
      }
      const u32 yb = WS_Y + (u32)(((2 * tt + tb) * 32 + 4 * cb + 2 * kh) * 1024);
      st128_sc1(u32x4{yp[0], yp[1], yp[2], yp[3]}, rws, (u32)(lane * 16), yb);
      st128_sc1(u32x4{yp[4], yp[5], yp[6], yp[7]}, rws, (u32)(lane * 16), yb + 1024);
      s1 = swap32_add(s1, s1); s2 = swap32_add(s2, s2);
      if (lane < 32) st64_sc1(u32x2{u_(s1), u_(s2)}, rws, (u32)(tok * 8), WS_ST + (u32)(((2 * tt + tb) * 16 + 2 * cb + kh) * 256));
    }
#pragma unroll
    for (int q = 0; q < 8; q += 4) asm volatile("" : "+v"(x1p[q]), "+v"(x1p[q + 1]), "+v"(x1p[q + 2]), "+v"(x1p[q + 3]));
    STAMP(12);
    pubbar<0>(16, rws, WS_FY + (u32)(tt * 2048 + cb * 8), tlo, thi, tid);
    STAMP(13);
    wait_flags<8>(rws, WS_FY + (u32)((2 * T + (cw >> 1)) * 2048 + jt * 128), lane, tlo, thi);
    STAMP(14);
    const int tb32 = 4 * T + cw;
    u32x4 yf[32];
    auto ldy = [&](int s) { yf[s] = ld128_sc1(rws, (u32)(lane * 16), WS_Y + (u32)((tb32 * 32 + ((s + 2 * rotg) & 31)) * 1024)); };
#pragma unroll
    for (int s = 0; s < (EXPW ? 32 : 10); ++s) { ldy(s); SB(); }
    u32x2 stv[8];
#pragma unroll
    for (int q = 0; q < 8; ++q) stv[q] = ld64_sc1(rws, (u32)(tok * 8 + h * 2048), WS_ST + (u32)((tb32 * 16 + q) * 256));
    SB();
    u32 mz, sz; float r2;
    auto act_store = [&](const f32x16& ac, int pp) {
      u32x4 o;
#pragma unroll
      for (int e = 0; e < 4; ++e) {
        const f32x2 gsc = f32x2{ac[2 * e], ac[2 * e + 1]} * f32x2{r2, r2};
        const f32x2 av = f32x2{fmaxf(gsc[0], 0.f), fmaxf(gsc[1], 0.f)} * f32x2{ac[8 + 2 * e], ac[9 + 2 * e]};
        o[e] = pk2(av[0], av[1]);
      }
      st128_sc1(o, rws, (u32)(lane * 16), WS_ACT + (u32)((tb32 * 112 + 7 * jt + pp) * 1024));
    };
    {
      f32x16 acc[4];
#pragma unroll
      for (int q = 0; q < 4; ++q)
#pragma unroll
        for (int r = 0; r < 16; ++r) acc[q][r] = 0.f;
      LAS char* pa[4];
#pragma unroll
      for (int pp = 0; pp < 4; ++pp) pa[pp] = L + (p1base + (((u32)pp ^ p1x) * 32));
      u32x4 wf[2][4], fx, fxp;
      auto ldw = [&](int st, int bi) {
#pragma unroll
        for (int pp = 0; pp < 4; ++pp) { if (st == 16) pa[pp] = opq(pa[pp] + 8 * L_P1B); wf[bi][pp] = trd2(pa[pp] + ((st >> 1) & 7) * L_P1B + (st & 1) * 2048, 1024); }
      };
      ldw(0, 0); SB();
#pragma unroll
      for (int st = 0; st < 32; ++st) {
        if (st == 15) WACC(25, bar());
        if (st == 12) {
      float s1 = 0.f, s2 = 0.f;
#pragma unroll
      for (int q = 0; q < 8; ++q) { s1 += f_(stv[q][0]); s2 += f_(stv[q][1]); }
      s1 = swap32_add(s1, s1); s2 = swap32_add(s2, s2);
      const float md = s1 * (1.f / 512.f);
      const float ve = fmaxf(s2 * (1.f / 512.f) - md * md, 0.f) + 1e-5f;
      const float sg = __builtin_amdgcn_sqrtf(ve); r2 = __builtin_amdgcn_rcpf(ve);
      const u32 hi = pk2(-md, sg), lo = pk2(-md - bflo(hi), sg - bfhi(hi));
      const u32 mm = (hi & 0xffffu) | (lo << 16), ss = (hi >> 16) | (lo & 0xffff0000u);
      mz = h ? 0u : mm; sz = h ? 0u : ss;
      asm volatile("" : "+v"(mz), "+v"(sz), "+v"(r2));
    }
        if (st + 1 < 32) ldw(st + 1, (st + 1) & 1);
        if (OPT & 2) { if (2 * st + 10 < 32) { ldy(2 * st + 10); SB(); ldy(2 * st + 11); } }
        else if (!EXPW && st + 10 < 32) ldy(st + 10);
#pragma unroll
        for (int pp = 0; pp < 4; ++pp) acc[pp] = mfma32(wf[st & 1][pp], yf[st], acc[pp]);
#pragma unroll
        for (int pp = 0; pp < 4; ++pp) { SGB_MFMA(1); SGB_DSR(2); }
        SB();
        if (st >= 24 && st < 28) fx = *(const LAS u32x4*)(L + L_FIX + (st - 24) * 1024 + lane * 16), SB();
        if (st >= 25 && st < 29) acc[st - 25 < 4 && st >= 25 ? st - 25 : 0] = mfma32(fxp, u32x4{mz, mz, sz, sz}, acc[st - 25 < 4 && st >= 25 ? st - 25 : 0]), SB();
        fxp = fx;
      }
      STAMP(15);
#pragma unroll
      for (int pp = 0; pp < 4; ++pp) act_store(acc[pp], p0 + pp);
      WACC(26, bar());
    }
    u32x4 af[16][2]; u32x2 fl;
    auto lda = [&](int n) { const int s = n >> 1, t2 = n & 1; af[s][t2] = ld128_sc1(rws, (u32)(lane * 16), WS_ACT + (u32)(((2 * tt + (t2 ^ tb)) * 112 + 28 * cw + 8 * (((s >> 2) + rotd) & 3) + (s & 3)) * 1024)); };
    {
      f32x16 acc[3], z16;
#pragma unroll
      for (int r = 0; r < 16; ++r) z16[r] = 0.f;
#pragma unroll
      for (int pp = 0; pp < 3; ++pp) acc[pp] = mfma32(*(const LAS u32x4*)(L + L_FIX + (4 + pp) * 1024 + lane * 16), u32x4{mz, mz, sz, sz}, z16);
      LAS char* pb = L + p2base;
      u32x4 wf[2][3];
      auto ldw = [&](int st, int bi) {
        if (st == 16) pb = opq(pb + 8 * L_P2B);
#pragma unroll
        for (int pp = 0; pp < 3; ++pp) wf[bi][pp] = trd2(pb + ((st >> 1) & 7) * L_P2B + (st & 1) * 1536 + pp * 32, 768);
      };
      ldw(0, 0); SB();
#pragma unroll
      for (int st = 0; st < 32; ++st) {
        if (st == S0P) WACC(30, pubbar<0>(16, rws, WS_FA + (u32)(T * 2048 + jt * 8), tlo, thi, tid));
        if (st == 15 || st == 23) WACC(27, bar());
        if (st == FA1P) fl = peek_flags<4>(rws, WS_FA + (u32)(T * 2048 + ((tt & 1) * 8 + cb) * 128 + cw * 32), lane);
        if (st == FA1P + 4) { STAMP(23); WACC(28, wait_flags_from<4>(fl, rws, WS_FA + (u32)(T * 2048 + ((tt & 1) * 8 + cb) * 128 + cw * 32), lane, tlo, thi)); }
        if (st + 1 < 32) ldw(st + 1, (st + 1) & 1);
        if (st >= FA1P + 4) { const int k = st - FA1P - 4; if (A1R == 1) lda(k); else if (A1R == 2) { lda(2 * k); SB(); lda(2 * k + 1); } else { lda(3 * (k >> 1) + (k & 1) * 2); if (!(k & 1)) { SB(); lda(3 * (k >> 1) + 1); } } }
#pragma unroll
        for (int pp = 0; pp < 3; ++pp) acc[pp] = mfma32(wf[st & 1][pp], yf[st], acc[pp]);
#pragma unroll
        for (int pp = 0; pp < 3; ++pp) { SGB_MFMA(1); SGB_DSR(2); }
        SB();
      }
      WACC(27, bar());
      STAMP(16);
#pragma unroll
      for (int pp = 0; pp < 3; ++pp) act_store(acc[pp], pp >= p0 ? pp + 4 : pp);
#pragma unroll
      for (int n = A1N; n < 24; ++n) { lda(n); SB(); }
    }
    SB();
    {
      f32x16 acc[4];
#pragma unroll
      for (int q = 0; q < 4; ++q)
#pragma unroll
        for (int r = 0; r < 16; ++r) acc[q][r] = 0.f;
      LAS char* d0 = L + (cw * 8192 + (4 * h + q4) * 128 + mrow * 32 + (lane & 3) * 8);
      LAS char* da = d0 + ((q4 >> 1) ^ kh) * 64; LAS char* db = d0 + ((q4 >> 1) ^ kh ^ 1) * 64;
      u32x4 wl[2][2], a2[12][2];
      auto lda2 = [&](int n) {
        const int s2 = n >> 1, t2 = n & 1, jj = (s2 / 3 + rotd) & 3, pp = s2 % 3;
        a2[s2][t2] = ld128_sc1(rws, (u32)(lane * 16), WS_ACT + (u32)(((2 * tt + (t2 ^ tb)) * 112 + 28 * cw + 7 * jj + (pp >= jj ? pp + 4 : pp)) * 1024));
      };
      LAS char* const dah = opq(da + 65536); LAS char* const dbh = opq(db + 65536);
      auto ldw = [&](int s, int bi) { const u32 o = wd_unit_off(s >> 2) + (s & 3) * 2048; wl[bi][0] = trd2(o < 65536 ? da + o : dah + (o - 65536), 1024); wl[bi][1] = trd2(o < 65536 ? db + o : dbh + (o - 65536), 1024); };
      ldw(0, 0); SB();
#pragma unroll
      for (int s = 0; s < 28; ++s) {
        if (s == 3) WACC(30, pubbar<(A1N < 24 ? 24 - A1N : 0) + (A1N <= 24 ? 3 : (A1N < 27 ? 27 - A1N : 0))>(16, rws, WS_FA2 + (u32)(T * 2048 + jt * 8), tlo, thi, tid));
        if ((s & 3) == 3 && s > 3 && s < 24) WACC(29, bar());
        if (!D0 && s == 12) {
          STAMP(17);
          wait_flags_from<4>(fl, rws, WS_FA2 + (u32)(T * 2048 + ((tt & 1) * 8 + cb) * 128 + cw * 32), lane, tlo, thi);
          STAMP(21);
#pragma unroll
          for (int n = 0; n < 12; ++n) { lda2(n); SB(); }
          SB();
        }
        if (s < 8 && 24 + s >= A1N) lda(24 + s);
        if (!D0 && s == 8) fl = peek_flags<4>(rws, WS_FA2 + (u32)(T * 2048 + ((tt & 1) * 8 + cb) * 128 + cw * 32), lane);
        if (!D0 && s >= 13 && s < 19) { lda2(12 + 2 * (s - 13)); SB(); lda2(13 + 2 * (s - 13)); }
        if (s + 1 < 28) ldw(s + 1, (s + 1) & 1);
        const u32x4 b0 = s < 16 ? af[s < 16 ? s : 0][0] : a2[s < 16 ? 0 : s - 16][0], b1 = s < 16 ? af[s < 16 ? s : 0][1] : a2[s < 16 ? 0 : s - 16][1];
        acc[0] = mfma32(wl[s & 1][0], b0, acc[0]); acc[2] = mfma32(wl[s & 1][1], b0, acc[2]);
        acc[1] = mfma32(wl[s & 1][0], b1, acc[1]); acc[3] = mfma32(wl[s & 1][1], b1, acc[3]);
        SGB_MFMA(1); SGB_DSR(2); SGB_MFMA(1); SGB_DSR(2); SGB_MFMA(2);
        SB();
      }
      STAMP(22);
      int ln2 = lane; asm volatile("" : "+v"(ln2));
#pragma unroll
      for (int i = 1; i < 4; ++i)
#pragma unroll
        for (int q = 0; q < 4; ++q) *(LAS f32x4*)(L + L_RED4 + ((i ^ cw) * 3 + i - 1) * 4096 + q * 1024 + ln2 * 16) = f32x4{acc[i][4 * q], acc[i][4 * q + 1], acc[i][4 * q + 2], acc[i][4 * q + 3]};
      const f32x16 mine = acc[0];
      bar();
      u32 ob[8];
#pragma unroll
      for (int q = 0; q < 4; ++q) {
        const LAS char* rp = L + L_RED4 + cw * 3 * 4096 + q * 1024 + ln2 * 16;
        const f32x4 r0 = *(const LAS f32x4*)rp, r1 = *(const LAS f32x4*)(rp + 4096), r2_ = *(const LAS f32x4*)(rp + 8192);
        const f32x4 gb4 = *(const LAS f32x4*)(L + L_GBV + (32 * kh + 8 * q + 4 * (ln2 >> 5)) * 4);
        float v[4];
#pragma unroll
        for (int e = 0; e < 4; ++e) v[e] = (mine[4 * q + e] + r0[e]) + (r1[e] + r2_[e]);
        ob[2 * q] = pk2(fmaf(gb4[0], v[0], bflo(x1p[2 * q])), fmaf(gb4[1], v[1], bfhi(x1p[2 * q])));
        ob[2 * q + 1] = pk2(fmaf(gb4[2], v[2], bflo(x1p[2 * q + 1])), fmaf(gb4[3], v[3], bfhi(x1p[2 * q + 1])));
      }
#pragma unroll
      for (int m = 0; m < 2; ++m) {
        auto r0 = __builtin_amdgcn_permlane32_swap(ob[4 * m], ob[4 * m + 2], false, false);
        auto r1 = __builtin_amdgcn_permlane32_swap(ob[4 * m + 1], ob[4 * m + 3], false, false);
        const u32 a0 = r0[0], b0 = r0[1], a1 = r1[0], b1 = r1[1];
        __builtin_amdgcn_raw_buffer_store_b128(u32x4{a0, a1, b0, b1}, rout, (u32)((64 * tt + 32 * tb + (ln2 & 31)) * 1024 + (64 * cb + 32 * kh + 16 * m + 8 * (ln2 >> 5)) * 2), 0, 0);
      }
    }
    STAMP(18);
  }
}

__global__ __launch_bounds__(512, 2) void fused_block(Params p) {
  extern __shared__ __attribute__((aligned(16))) char smem_[];
  asm volatile("" :: "s"(p.x), "s"(p.wqkv), "s"(p.wo), "s"(p.wg), "s"(p.wu), "s"(p.wd), "s"(p.c), "s"(p.wmod), "s"(p.out), "s"(p.ws));
  int w = __builtin_amdgcn_readfirstlane(threadIdx.x >> 6);
  u32x4 wr[18]; u32 tlo, thi;
  front(p, (LAS char*)smem_, threadIdx.x, w, blockIdx.x, wr, tlo, thi STAMP_ARG);
  if (w >= 4) {
    int b = blockIdx.x, tid = threadIdx.x; asm volatile("; H role" : "+s"(b), "+s"(w)); asm volatile("; H role" : "+v"(tid));
    role_body<false>(p, (LAS char*)smem_, tid, w, b, wr, tlo, thi STAMP_ARG);
  } else {
    int b = blockIdx.x, tid = threadIdx.x; asm volatile("; C role" : "+s"(b), "+s"(w)); asm volatile("; C role" : "+v"(tid));
    role_body<true>(p, (LAS char*)smem_, tid, w, b, wr, tlo, thi STAMP_ARG);
  }
}

extern "C" void kernel_launch(void* const* d_in, const int* in_sizes, int n_in, void* d_out, int out_size, void* d_ws, size_t ws_size, hipStream_t stream) {
  Params p;
  p.x = d_in[0]; p.wqkv = d_in[1]; p.wo = d_in[2]; p.wg = d_in[4]; p.wu = d_in[5]; p.wd = d_in[6]; p.c = d_in[7]; p.wmod = d_in[8];
  p.out = d_out; p.ws = d_ws;
  (void)hipFuncSetAttribute((const void*)fused_block, hipFuncAttributeMaxDynamicSharedMemorySize, LDS_BYTES);
  fused_block<<<256, 512, LDS_BYTES, stream>>>(p);
}
```

```cpp
#include <hip/hip_runtime.h>
#include <stdint.h>

typedef unsigned short u16;
typedef unsigned u32;
typedef unsigned long long u64;
typedef u32 u32x2 __attribute__((ext_vector_type(2)));
typedef u32 u32x4 __attribute__((ext_vector_type(4)));
typedef short s16x4 __attribute__((ext_vector_type(4)));
typedef short s16x8 __attribute__((ext_vector_type(8)));
typedef __bf16 bf16x2 __attribute__((ext_vector_type(2)));
typedef __bf16 bf16x8 __attribute__((ext_vector_type(8)));
typedef float f32x2 __attribute__((ext_vector_type(2)));
typedef float f32x4 __attribute__((ext_vector_type(4)));
typedef float f32x16 __attribute__((ext_vector_type(16)));
typedef __amdgpu_buffer_rsrc_t rsrc_t;
#define LAS __attribute__((address_space(3)))
#define DI __device__ __forceinline__

#define EXPW 0
#define TOUCH 30
#define THR 4
constexpr unsigned CODE_BYTES = 40000;
#define NTX 0
#define FA1P 16
#define OPT 0
#define ELIM 0
#define S0P 3
#define A1R 1
#define A1N (A1R == 1 ? 28 - FA1P : A1R == 2 ? 2 * (28 - FA1P) : 3 * (28 - FA1P) / 2)
#define W1T 0
#define D0 0
#define A2C 9
#define LITE 0
#define MIM 16
#define DIAG 0
#define XMAP 2

constexpr int S_ = 2048, D_ = 512, F_ = 1792;
constexpr u32 WS_E = 0, WS_MODG = 393216, WS_FU = 57344, WS_FO = 8192, WS_FY = 983040, WS_FA = 327680, WS_FA2 = 360448, WS_DBG = 65536;
constexpr u32 WS_MU = 499712;
constexpr u32 WS_UT = 524288;
constexpr u32 WS_O = 1u << 20, WS_Y = 3u << 20, WS_ST = 5u << 20, WS_ACT = 6u << 20, WS_BYTES = 13u << 20;
constexpr u32 L_AIMG = 0, L_RW = 81920, L_W1S = 12288;
constexpr u32 L_LNP = 73728, L_PRED = L_LNP + 6400, L_RSNB = L_PRED + 1088;
constexpr u32 L_XRES = 65536, L_WO = 73728, L_RED2 = 139264;
constexpr u32 L_P1B = 8192, L_P2B = 6400, L_P2M = 3200;
constexpr u32 L_RED4 = 65536;
constexpr u32 L_MISC = 155648, L_G1 = L_MISC, L_S1 = L_MISC + 2048, L_FIX = L_MISC;
constexpr u32 L_SL = L_MISC + 4096, L_SCRAP = L_MISC + 6656;
constexpr u32 L_MU1 = L_MISC + 7168, L_GA = L_MU1 + 256, L_G2C = L_GA + 256, L_GBV = L_G2C + 256;
constexpr u32 LDS_BYTES = 163840;
__host__ __device__ constexpr u32 wd_unit_off(int u) { return u == 0 || u == 4 ? 102400u : (u == 1 || u == 5 ? 0u : (u == 2 || u == 6 ? 32768u : 65536u)); }

template <int N> struct ic { static constexpr int value = N; };
struct Params { const void *x, *wqkv, *wo, *wg, *wu, *wd, *c, *wmod; void *out; void *ws; };

DI rsrc_t mk(const void* p, u32 bytes) { return __builtin_amdgcn_make_buffer_rsrc((void*)p, 0, bytes, 0x00020000); }
DI float bflo(u32 v) { return __builtin_bit_cast(float, v << 16); }
DI float bfhi(u32 v) { return __builtin_bit_cast(float, v & 0xffff0000u); }
DI u32 pk2(float a, float b) { bf16x2 r; r[0] = (__bf16)a; r[1] = (__bf16)b; return __builtin_bit_cast(u32, r); }
DI float rbf(float a) { return bflo(pk2(a, a)); }
DI f32x4 mfma16(u32x4 a, u32x4 b, f32x4 c) { return __builtin_amdgcn_mfma_f32_16x16x32_bf16(__builtin_bit_cast(bf16x8, a), __builtin_bit_cast(bf16x8, b), c, 0, 0, 0); }
DI f32x4 mfma16k16(u32x2 a, u32x2 b, f32x4 c) { return __builtin_amdgcn_mfma_f32_16x16x16bf16_1k(__builtin_bit_cast(s16x4, a), __builtin_bit_cast(s16x4, b), c, 0, 0, 0); }
DI f32x16 mfma32(u32x4 a, u32x4 b, f32x16 c) { return __builtin_amdgcn_mfma_f32_32x32x16_bf16(__builtin_bit_cast(bf16x8, a), __builtin_bit_cast(bf16x8, b), c, 0, 0, 0); }
DI u32x2 trd(LAS char* p) { return __builtin_bit_cast(u32x2, __builtin_amdgcn_ds_read_tr16_b64_v4i16((LAS s16x4*)p)); }
DI u32x4 trd2(LAS char* p, int d) { u32x2 a = trd(p), b = trd(p + d); return u32x4{a[0], a[1], b[0], b[1]}; }
DI LAS char* opq(LAS char* p) { asm volatile("" : "+v"(p)); return p; }
DI void dma16(rsrc_t rs, u32 voff, u32 soff, u32 lds) {
  asm volatile("s_mov_b32 m0, %0\n\ts_nop 0\n\tbuffer_load_dwordx4 %1, %2, %3 offen lds" :: "s"(lds), "v"(voff), "s"(rs), "s"(soff) : "m0", "memory");
}
template <int OFF> DI void ald128(u32x4& d, rsrc_t rs, u32 voff, u32 soff) {
  asm volatile("buffer_load_dwordx4 %0, %1, %2, %3 offen offset:%c4" : "=v"(d) : "v"(voff), "s"(rs), "s"(soff), "i"(OFF) : "memory");
}
DI void ald128_nt(u32x4& d, rsrc_t rs, u32 voff, u32 soff) {
  asm volatile("buffer_load_dwordx4 %0, %1, %2, %3 offen" : "=v"(d) : "v"(voff), "s"(rs), "s"(soff) : "memory");
}
template <int OFF> DI void ald128_sc1(u32x4& d, rsrc_t rs, u32 voff, u32 soff) {
  asm volatile("buffer_load_dwordx4 %0, %1, %2, %3 offen offset:%c4 sc1" : "=v"(d) : "v"(voff), "s"(rs), "s"(soff), "i"(OFF) : "memory");
}
DI void ald64_sc1(u32x2& d, rsrc_t rs, u32 voff, u32 soff) {
  asm volatile("buffer_load_dwordx2 %0, %1, %2, %3 offen sc1" : "=v"(d) : "v"(voff), "s"(rs), "s"(soff) : "memory");
}
DI void ald64(u32x2& d, rsrc_t rs, u32 voff, u32 soff) {
  asm volatile("buffer_load_dwordx2 %0, %1, %2, %3 offen" : "=v"(d) : "v"(voff), "s"(rs), "s"(soff) : "memory");
}
DI void warm(u32 lds, rsrc_t rs, u32 voff, u32 soff) {
  asm volatile("s_mov_b32 m0, %0\n\ts_nop 0\n\tbuffer_load_dword %1, %2, %3 offen lds" :: "s"(lds), "v"(voff), "s"(rs), "s"(soff) : "m0", "memory");
}
template <int N> DI void waitv() { asm volatile("s_waitcnt vmcnt(%c0)" :: "i"(N) : "memory"); }
template <int N> DI void waitv4(u32x4& a, u32x4& b, u32x4& c, u32x4& d) { asm volatile("s_waitcnt vmcnt(%c4)" : "+v"(a), "+v"(b), "+v"(c), "+v"(d) : "i"(N) : "memory"); }
template <int N> DI void waitv1(u32x4& a) { asm volatile("s_waitcnt vmcnt(%c1)" : "+v"(a) : "i"(N) : "memory"); }
DI void bar() { __builtin_amdgcn_sched_barrier(0); asm volatile("s_waitcnt lgkmcnt(0)\n\ts_barrier" ::: "memory"); __builtin_amdgcn_sched_barrier(0); }
DI u32x4 ld128_sc1(rsrc_t rs, u32 voff, u32 soff) { return __builtin_amdgcn_raw_buffer_load_b128(rs, voff, soff, 16); }
DI u32x2 ld64_sc1(rsrc_t rs, u32 voff, u32 soff) { return __builtin_amdgcn_raw_buffer_load_b64(rs, voff, soff, 16); }
DI void st128_sc1(u32x4 v, rsrc_t rs, u32 voff, u32 soff) { __builtin_amdgcn_raw_buffer_store_b128(v, rs, voff, soff, 16); }
DI void st64_sc1(u32x2 v, rsrc_t rs, u32 voff, u32 soff) { __builtin_amdgcn_raw_buffer_store_b64(v, rs, voff, soff, 16); }
DI float f_(u32 v) { return __builtin_bit_cast(float, v); }
DI u32 u_(float v) { return __builtin_bit_cast(u32, v); }
DI float swap16_add(float a, float b) { auto r = __builtin_amdgcn_permlane16_swap(u_(a), u_(b), false, false); u32 r0 = r[0], r1 = r[1]; return f_(r0) + f_(r1); }
DI float swap32_add(float a, float b) { auto r = __builtin_amdgcn_permlane32_swap(u_(a), u_(b), false, false); u32 r0 = r[0], r1 = r[1]; return f_(r0) + f_(r1); }
DI float swap16_max(float a) { auto r = __builtin_amdgcn_permlane16_swap(u_(a), u_(a), false, false); u32 r0 = r[0], r1 = r[1]; return fmaxf(f_(r0), f_(r1)); }
DI float swap32_max(float a) { auto r = __builtin_amdgcn_permlane32_swap(u_(a), u_(a), false, false); u32 r0 = r[0], r1 = r[1]; return fmaxf(f_(r0), f_(r1)); }
DI float mx2(float a, float b) { return __builtin_amdgcn_fmed3f(a, b, __builtin_inff()); }
template <int CTRL> DI float dpp_add(float a) { return a + f_(__builtin_amdgcn_update_dpp(0, u_(a), CTRL, 0xf, 0xf, true)); }
DI float dot2(u32 a, u32 b, float c) { return __builtin_amdgcn_fdot2_f32_bf16(__builtin_bit_cast(bf16x2, a), __builtin_bit_cast(bf16x2, b), c, false); }

constexpr u64 SPIN_TICKS = 20000;
DI bool spin_out(u64 t0) { __builtin_amdgcn_s_sleep(1); return (__builtin_amdgcn_s_memrealtime() - t0) > SPIN_TICKS; }

#define SB() __builtin_amdgcn_sched_barrier(0)
#define SGB_MFMA(n) __builtin_amdgcn_sched_group_barrier(0x8, n, 0)
#define SGB_DSR(n) __builtin_amdgcn_sched_group_barrier(0x100, n, 0)
#define SGB_DSW(n) __builtin_amdgcn_sched_group_barrier(0x200, n, 0)
#define STAMP(i) do {} while (0)
#define WACC(i, code) do { code; } while (0)

template <int YOUNGER> DI void pubbar(int NC, rsrc_t rws, u32 flag_off, u32 tlo, u32 thi, int tid) {
  waitv<YOUNGER>(); bar();
  if (tid < NC) st64_sc1(u32x2{tlo, thi}, rws, (u32)tid * 128u, flag_off);
}
template <int NF> DI u32x2 peek_flags(rsrc_t rws, u32 off, int lane) { return ld64_sc1(rws, (u32)(lane & (NF - 1)) * 8u, off); }
template <int NF> DI void wait_flags(rsrc_t rws, u32 off, int lane, u32 tlo, u32 thi) {
  const u32 vo = (u32)(lane & (NF - 1)) * 8u;
  const u64 t0 = __builtin_amdgcn_s_memrealtime();
  for (;;) {
    u32x2 f = ld64_sc1(rws, vo, off);
    if (__all(f[0] == tlo && f[1] == thi)) break;
    if (spin_out(t0)) break;
  }
  __builtin_amdgcn_fence(__ATOMIC_ACQUIRE, "wavefront");
}
DI bool gran_ok(u32x4 g, u32 glo, u32 ghi) { return (g[0] & 0xffu) == glo && g[1] == ghi && (g[2] & 0xffu) == glo && g[3] == ghi; }

#define STAMP_PARAM
#define STAMP_ARG
template <int NF> DI void wait_flags_from(u32x2 seen, rsrc_t rws, u32 off, int lane, u32 tlo, u32 thi) {
  if (!__all(seen[0] == tlo && seen[1] == thi)) wait_flags<NF>(rws, off, lane, tlo, thi);
  __builtin_amdgcn_fence(__ATOMIC_ACQUIRE, "wavefront");
}
struct Gath { u32x4 q[4]; };
DI void gath_load(Gath& G, rsrc_t rws, u32 voff, u32 soff) {
#pragma unroll
  for (int k = 0; k < 4; ++k) G.q[k] = ld128_sc1(rws, voff, WS_MODG + soff + (u32)k * 24576u);
}
DI bool gath_ok(const Gath& G, u32 glo, u32 ghi) { return gran_ok(G.q[0], glo, ghi) && gran_ok(G.q[1], glo, ghi) && gran_ok(G.q[2], glo, ghi) && gran_ok(G.q[3], glo, ghi); }
DI u32 gath_val(const Gath& G) {
  const float a = (f_(G.q[0][0] & 0xffffff00u) + f_(G.q[1][0] & 0xffffff00u)) + (f_(G.q[2][0] & 0xffffff00u) + f_(G.q[3][0] & 0xffffff00u));
  const float b = (f_(G.q[0][2] & 0xffffff00u) + f_(G.q[1][2] & 0xffffff00u)) + (f_(G.q[2][2] & 0xffffff00u) + f_(G.q[3][2] & 0xffffff00u));
  return pk2(a, b);
}
DI void mod_issue(u32x4 (&wm)[4], u32x2& cv, rsrc_t rwm, rsrc_t rc, int mq, int mblk, int cw, int lane) {
    const int r0 = 128 * mq + 32 * cw;
#pragma unroll
    for (int i = 0; i < 4; ++i) ald128_nt(wm[i], rwm, (u32)((lane >> 3) * 4 * 6144 + (lane & 7) * 16), (u32)((r0 + i) * 6144 + mblk * 128));
    ald64(cv, rc, (u32)((lane >> 3) * 8), (u32)(r0 * 2));
}
DI void mod_finish(LAS char* L, u32 pred, const u32x4 (&wm)[4], const u32x2& cv, u32 tlo, u32 thi, rsrc_t rws, int mq, int mblk, int cw, int lane) {
    const int g = lane >> 4, i16 = lane & 15;
    float a8[8];
#pragma unroll
    for (int e = 0; e < 8; ++e) a8[e] = 0.f;
#pragma unroll
    for (int i = 0; i < 4; ++i) {
      const float cf = (i & 1) ? bfhi(cv[i >> 1]) : bflo(cv[i >> 1]);
      const float sv = rbf(cf * __builtin_amdgcn_rcpf(1.f + __expf(-cf)));
#pragma unroll
      for (int e = 0; e < 4; ++e) { a8[2 * e] = fmaf(sv, bflo(wm[i][e]), a8[2 * e]); a8[2 * e + 1] = fmaf(sv, bfhi(wm[i][e]), a8[2 * e + 1]); }
    }
#pragma unroll
    for (int e = 0; e < 8; ++e) a8[e] = dpp_add<0x128>(a8[e]);
    float b4[4], b2[2];
#pragma unroll
    for (int e = 0; e < 4; ++e) b4[e] = swap16_add(a8[e], a8[e + 4]);
#pragma unroll
    for (int e = 0; e < 2; ++e) b2[e] = swap32_add(b4[e], b4[e + 2]);
    if (i16 < 8) *(LAS f32x2*)(L + pred + cw * 256 + (i16 * 8 + (g & 1) * 4 + (g >> 1) * 2) * 4) = f32x2{b2[0], b2[1]};
    asm volatile("s_waitcnt lgkmcnt(0)" ::: "memory");
    if (lane == 0) *(volatile LAS u32x2*)(L + pred + 1024 + cw * 8) = u32x2{tlo, thi};
    if (cw == 0) {
      const u64 t0 = __builtin_amdgcn_s_memrealtime();
      for (;;) {
        const u32x2 tg = *(volatile LAS u32x2*)(L + pred + 1024 + (lane & 3) * 8);
        if (__all(tg[0] == tlo && tg[1] == thi)) break;
        if ((__builtin_amdgcn_s_memrealtime() - t0) > SPIN_TICKS) break;
      }
      asm volatile("" ::: "memory");
      LAS float* s = (LAS float*)(L + pred) + lane;
      const float v = (s[0] + s[64]) + (s[128] + s[192]);
      st64_sc1(u32x2{((u_(v) + 0x80u) & 0xffffff00u) | (tlo & 0xffu), (tlo >> 8) | (thi << 24)}, rws, (u32)(lane * 8), WS_MODG + (u32)(mq * 24576 + mblk * 512));
    }
}
DI void front(const Params& p, LAS char* L, const int tid, const int w, const int b, u32x4 (&wr)[18], u32& tlo, u32& thi STAMP_PARAM) {
  const bool isC = w < 4;
  f32x2 xh[10][4];
  const u32 Lb = (u32)(size_t)L;
  const int lane = tid & 63, cw = w & 3;
  const int xcd = b & 7, idx = b >> 3;
  const int tt = 8 * (xcd >> 1) + (idx >> 2), hgl = idx & 3, hg = 4 * (xcd & 1) + hgl;
  const int jt = 8 * (xcd & 1) + 4 * (tt & 1) + hgl;
  const int T = tt >> 1, cb = hg;
  STAMP(0);
  const rsrc_t rx = mk(p.x, S_ * D_ * 2), rw1 = mk(p.wqkv, D_ * 3 * D_ * 2), rwo = mk(p.wo, D_ * D_ * 2);
  const rsrc_t rwg = mk(p.wg, D_ * F_ * 2), rwu = mk(p.wu, D_ * F_ * 2), rwd = mk(p.wd, F_ * D_ * 2);
  const rsrc_t rc = mk(p.c, D_ * 2), rwm = mk(p.wmod, D_ * 6 * D_ * 2), rout = mk(p.out, S_ * D_ * 2), rws = mk(p.ws, WS_BYTES);
  const int g = lane >> 4, i16 = lane & 15, h = lane >> 5, tok = lane & 31, t4 = tid & 255;

  const int nrot = 2 * hgl, rot = 2 * (tt & 7);
  const u32 w1v = (u32)((lane >> 3) * 3072 + (((((lane >> 1) & 3) ^ ((lane >> 4) & 1)) ^ ((cw & 1) << 1)) * 32) + (lane & 1) * 16);
  auto w1slot = [](int n) { return n < 6 ? L_RW + n * L_W1S : (n < 12 ? (n - 6) * L_W1S : L_RW + (n - 12) * L_W1S); };
  {
    u64 pc; asm volatile("s_getpc_b64 %0" : "=s"(pc));
    const rsrc_t rcode = mk((const void*)(pc & ~(u64)255), CODE_BYTES);
    warm(Lb + L_SCRAP, rcode, (u32)((idx * 8 + w) * 2 + (lane & 1)) * 128u, 0u);
  }
  u32x2 ev; ald64_sc1(ev, rws, 0u, WS_E);
  const bool prod = isC && idx < 8;
  const int mq = idx & 3, mblk = xcd * 2 + (idx >> 2);
  auto dma_w1 = [&](int i) {
    const int n = (i < 9 ? 6 : 0) + 2 * ((i % 9) / 3) + (w >> 2), pp = i % 3;
    dma16(rw1, w1v, (u32)((32 * ((n + rot) & 15) + 8 * cw) * 3072 + pp * 1024 + hg * 128), Lb + (u32)((i < 9 ? (n - 6) * L_W1S : L_RW + n * L_W1S) + pp * 4096 + cw * 1024));
  };
  u32x2 cv; u32x4 wm[4];
  if (prod) mod_issue(wm, cv, rwm, rc, mq, mblk, cw, lane);
  u32x4 xr[10];
#pragma unroll
  for (int n = 0; n < 10; ++n) {
    int r8 = n + nrot; r8 = r8 >= 10 ? r8 - 10 : r8;
    int row = 64 * tt - 16 + 8 * r8 + (lane >> 3); row = row < 0 ? 0 : row;
    ald128_nt(xr[n], rx, (u32)(row * 1024 + (lane & 7) * 16), (u32)(w * 128));
  }
  constexpr int Y0 = 0;
  STAMP(1);
  if (prod) {
    asm volatile("s_waitcnt vmcnt(%c6)" : "+v"(wm[0]), "+v"(wm[1]), "+v"(wm[2]), "+v"(wm[3]), "+v"(cv), "+v"(ev) : "i"(10 + Y0) : "memory");
    const u64 e1 = (((u64)__builtin_amdgcn_readfirstlane(ev[1]) << 32) | (u64)__builtin_amdgcn_readfirstlane(ev[0])) + 1ull;
    mod_finish(L, L_PRED, wm, cv, (u32)e1, (u32)(e1 >> 32), rws, mq, mblk, cw, lane);
  }
  STAMP(2);
  STAMP(3);
  {
    const int c8 = lane & 7, r8l = lane >> 3;
    float pv[10];
#pragma unroll
    for (int n = 0; n < 10; ++n) {
      waitv1<9>(xr[n]);
#pragma unroll
      for (int e = 0; e < 4; ++e) xh[n][e] = f32x2{bflo(xr[n][e]), bfhi(xr[n][e])};
      const f32x2 a1 = (xh[n][0] + xh[n][1]) + (xh[n][2] + xh[n][3]);
      const f32x2 a2 = __builtin_elementwise_fma(xh[n][0], xh[n][0], xh[n][1] * xh[n][1]) + __builtin_elementwise_fma(xh[n][2], xh[n][2], xh[n][3] * xh[n][3]);
      const float s1 = a1[0] + a1[1], s2 = a2[0] + a2[1];
      const float keep = (c8 & 1) ? s2 : s1, give = (c8 & 1) ? s1 : s2;
      float v = keep + f_(__builtin_amdgcn_update_dpp(0, u_(give), 0xB1, 0xf, 0xf, true));
      v = dpp_add<0x4E>(v);
      pv[n] = v + f_(__builtin_amdgcn_update_dpp(0, u_(v), 0x104, 0xf, 0xf, true));
      asm volatile("" : "+v"(pv[n])); dma_w1(n); SB();
    }
    asm volatile("" : "+v"(ev));
    {
      const u64 e1 = (((u64)__builtin_amdgcn_readfirstlane(ev[1]) << 32) | (u64)__builtin_amdgcn_readfirstlane(ev[0])) + 1ull;
      tlo = (u32)e1; thi = (u32)(e1 >> 32);
    }
    {
    const rsrc_t rt = w == 0 ? rwo : w == 1 ? rwg : w == 2 ? rwu : w == 3 ? rwd : w == 4 ? rout : rws;
    warm(Lb + L_SCRAP, rt, (u32)lane << 20, 0u);
  }

    if (c8 < 2) {
#pragma unroll
      for (int n = 0; n < 10; ++n) *(LAS float*)(L + L_LNP + (n * 8 + r8l) * 80 + (w * 2 + c8) * 4) = pv[n];
    }
    bar();
    f32x4 qa[2][4];
#pragma unroll
    for (int k = 0; k < 2; ++k)
#pragma unroll
      for (int e = 0; e < 4; ++e) qa[k][e] = *(const LAS f32x4*)(L + L_LNP + (k ? 64 + i16 : lane) * 80 + e * 16);
    dma_w1(10);
    float mus[2];
#pragma unroll
    for (int k = 0; k < 2; ++k) {
      const f32x4 a = (qa[k][0] + qa[k][1]) + (qa[k][2] + qa[k][3]);
      const float mu = (a[0] + a[2]) * (1.f / 512.f);
      const float var = fmaxf((a[1] + a[3]) * (1.f / 512.f) - mu * mu, 0.f);
      const float rs = __builtin_amdgcn_rcpf(rbf(__builtin_amdgcn_sqrtf(rbf(rbf(var) + 1e-5f))));
      *(LAS f32x2*)(L + L_RSNB + (k ? 64 + i16 : lane) * 8) = f32x2{rs, -rbf(mu) * rs};
      mus[k] = mu;
    }
    asm volatile("" ::: "memory");
    f32x2 rn[10];
#pragma unroll
    for (int n = 0; n < 10; ++n) rn[n] = *(const LAS f32x2*)(L + L_RSNB + (n * 8 + r8l) * 8);
    if (w == 0) {
#pragma unroll
      for (int k = 0; k < 2; ++k) { int r = (k ? 64 + i16 : lane) + 8 * nrot; r = r >= 80 ? r - 80 : r; ((LAS float*)(L + L_MU1 - 64))[r] = mus[k]; }
    }
#pragma unroll
    for (int n = 0; n < 10; ++n) {
#pragma unroll
      for (int e = 0; e < 4; ++e) xh[n][e] = __builtin_elementwise_fma(xh[n][e], f32x2{rn[n][0], rn[n][0]}, f32x2{rn[n][1], rn[n][1]});
      if (n < 7) { asm volatile("" : "+v"(xh[n][0]), "+v"(xh[n][1]), "+v"(xh[n][2]), "+v"(xh[n][3])); dma_w1(11 + n); SB(); }
    }
  }
#pragma unroll
  for (int n = 0; n < 10; ++n) asm volatile("" : "+v"(xh[n][0]), "+v"(xh[n][1]), "+v"(xh[n][2]), "+v"(xh[n][3]));
  STAMP(4);
  const u32 glo = tlo & 0xffu, ghi = (tlo >> 8) | (thi << 24);
  const u32 gvo = (u32)(w * 512 + tok * 16 + h * 4096);
  Gath g1_;
#pragma unroll
  for (int k = 0; k < 4; ++k) ald128_sc1<0>(g1_.q[k], rws, gvo, WS_MODG + (u32)k * 24576u);
  waitv<13>();
  bar();
  if (isC) {
    LAS char* w1rd = L + ((8 * g + (i16 >> 2)) * 128 + ((cw ^ (((i16 >> 3) & 1) | ((g & 1) << 1))) * 32) + (lane & 3) * 8);

#pragma unroll
    for (int n = 0; n < 6; ++n)
#pragma unroll
      for (int pp = 0; pp < 3; ++pp) wr[3 * n + pp] = trd2(w1rd + n * L_W1S + pp * 4096, 512);
  }
  STAMP(5);
  waitv4<0>(g1_.q[0], g1_.q[1], g1_.q[2], g1_.q[3]);
  if (!__all(gath_ok(g1_, glo, ghi))) {
    const u64 t0 = __builtin_amdgcn_s_memrealtime();
    for (;;) {
      gath_load(g1_, rws, gvo, 0u);
      if (__all(gath_ok(g1_, glo, ghi))) break;
      if (spin_out(t0)) break;
    }
  }
  {
    const u32 v2 = gath_val(g1_);
    const float a = bflo(v2), c = bfhi(v2);
    *(LAS f32x2*)(L + (h ? L_G1 : L_S1) + (w * 32 + tok) * 8) = h ? f32x2{rbf(1.f + a), rbf(1.f + c)} : f32x2{a, c};
  }
  STAMP(6);
  bar();
  {
    const int c8 = lane & 7, r8l = lane >> 3;
    const LAS f32x4* gp = (const LAS f32x4*)(L + L_G1 + (w * 64 + c8 * 8) * 4);
    const LAS f32x4* sp = (const LAS f32x4*)(L + L_S1 + (w * 64 + c8 * 8) * 4);
    const f32x4 g0 = gp[0], g1 = gp[1], s0 = sp[0], s1 = sp[1];
    LAS char* ab = L + L_AIMG + (2 * w + (c8 >> 2)) * 1024 + (c8 & 3) * 256 + ((r8l ^ c8) * 16);
#pragma unroll
    for (int n = 0; n < 10; ++n) {
      const f32x2 h0 = __builtin_elementwise_fma(xh[n][0], f32x2{g0[0], g0[1]}, f32x2{s0[0], s0[1]}), h1 = __builtin_elementwise_fma(xh[n][1], f32x2{g0[2], g0[3]}, f32x2{s0[2], s0[3]});
      const f32x2 h2 = __builtin_elementwise_fma(xh[n][2], f32x2{g1[0], g1[1]}, f32x2{s1[0], s1[1]}), h3 = __builtin_elementwise_fma(xh[n][3], f32x2{g1[2], g1[3]}, f32x2{s1[2], s1[3]});
      const u32x4 o = u32x4{pk2(h0[0], h0[1]), pk2(h1[0], h1[1]), pk2(h2[0], h2[1]), pk2(h3[0], h3[1])};
      int r8 = n + nrot; r8 = r8 >= 10 ? r8 - 10 : r8;
      *(LAS u32x4*)(ab + (r8 >> 1) * 16384 + (r8 & 1) * 128) = o;
    }
  }
  bar();
  STAMP(7);
}

template <bool isC> DI void role_body(const Params& p, LAS char* L, const int tid, const int w, const int b, u32x4 (&wr)[18], const u32 tlo, const u32 thi STAMP_PARAM) {
  const u32 Lb = (u32)(size_t)L;
  const int lane = tid & 63, cw = w & 3;
  const int xcd = b & 7, idx = b >> 3;
  const int tt = 8 * (xcd >> 1) + (idx >> 2), hgl = idx & 3, hg = 4 * (xcd & 1) + hgl;
  const int jt = 8 * (xcd & 1) + 4 * (tt & 1) + hgl;
  const int T = tt >> 1, cb = hg;
  const rsrc_t rx = mk(p.x, S_ * D_ * 2), rw1 = mk(p.wqkv, D_ * 3 * D_ * 2), rwo = mk(p.wo, D_ * D_ * 2);
  const rsrc_t rwg = mk(p.wg, D_ * F_ * 2), rwu = mk(p.wu, D_ * F_ * 2), rwd = mk(p.wd, F_ * D_ * 2);
  const rsrc_t rc = mk(p.c, D_ * 2), rwm = mk(p.wmod, D_ * 6 * D_ * 2), rout = mk(p.out, S_ * D_ * 2), rws = mk(p.ws, WS_BYTES);
  const int g = lane >> 4, i16 = lane & 15, h = lane >> 5, tok = lane & 31, t4 = tid & 255;

  const int nrot = 2 * hgl, rot = 2 * (tt & 7);
  const u32 w1v = (u32)((lane >> 3) * 3072 + (((((lane >> 1) & 3) ^ ((lane >> 4) & 1)) ^ ((cw & 1) << 1)) * 32) + (lane & 1) * 16);
  auto w1slot = [](int n) { return n < 6 ? L_RW + n * L_W1S : (n < 12 ? (n - 6) * L_W1S : L_RW + (n - 12) * L_W1S); };
  LAS char* w1rd = L + ((8 * g + (i16 >> 2)) * 128 + ((cw ^ (((i16 >> 3) & 1) | ((g & 1) << 1))) * 32) + (lane & 3) * 8);
  const u32 glo = tlo & 0xffu, ghi = (tlo >> 8) | (thi << 24);
  const int p0 = jt & 3, rotg = (4 * (T & 3) + 2 * (jt & 7)) & 15, rotd = (tt + hgl) & 3;
  const u32 wov = (u32)((lane >> 3) * 1024 + ((((lane >> 2) & 1) ^ ((lane >> 4) & 1)) * 64) + (lane & 3) * 16);
  const int mrow = (lane >> 4) & 1, q4 = i16 >> 2;
  const u32 p1base = (u32)(mrow * 4096 + (4 * h + q4) * 128 + (lane & 3) * 8), p1x = (u32)((q4 >> 1) | (mrow << 1));
  const u32 p2base = (u32)(mrow * L_P2M + (4 * h + q4) * 96 + (lane & 3) * 8);

  if constexpr (!isC) {
    const bool prod2 = idx >= 8 && idx < 24;
    const int mq = idx & 3, mblk = 16 + xcd * 4 + ((idx - 8) >> 2);
    u32x2 cv; u32x4 wm[4];
    if (prod2) mod_issue(wm, cv, rwm, rc, mq, mblk, cw, lane);
    bar();
#pragma unroll 1
    for (int n = 0; n < 4; ++n)
#pragma unroll
      for (int pp = 0; pp < 3; ++pp) dma16(rw1, w1v, (u32)((32 * ((12 + n + rot) & 15) + 8 * cw) * 3072 + pp * 1024 + hg * 128), Lb + (u32)(L_RW + n * L_W1S + pp * 4096 + cw * 1024));
    warm(Lb + L_SCRAP, cw == 0 ? rwg : cw == 1 ? rwu : cw == 2 ? rwd : rwo, (u32)(b + 256 * lane) * 128u, 0u);
    if (prod2) {
      asm volatile("s_waitcnt vmcnt(13)" : "+v"(wm[0]), "+v"(wm[1]), "+v"(wm[2]), "+v"(wm[3]), "+v"(cv) :: "memory");
      mod_finish(L, L_G1, wm, cv, tlo, thi, rws, mq, mblk, cw, lane);
    }
    if (prod2 && cw == 0) waitv<2>(); else waitv<1>();
    bar();
    STAMP(8);
    STAMP(9);
    bar();
    const u32 gch = h ? (u32)((cw == 0 ? 1024 : cw == 1 ? 2048 : 2560) + 64 * cb + 2 * tok) : (u32)((lane & 16 ? 1536 : 2048) + 32 * T + 2 * i16);
    Gath g2_;
#pragma unroll
    for (int k = 0; k < 4; ++k) ald128_sc1<0>(g2_.q[k], rws, gch * 8u, WS_MODG + (u32)k * 24576u);
    const rsrc_t rgu = (cw & 1) ? rwu : rwg;
    const int uc = 7 * (cw >> 1) + ((lane & 7) < 7 ? (lane & 7) : 6);
    u32x4 uw[4];
    u32 uso = (u32)(T * (32 * 3584) + jt * 224); asm volatile("" : "+s"(uso));
#pragma unroll
    for (int i = 0; i < 4; ++i) ald128<0>(uw[i], rgu, (u32)((lane >> 3) * 3584 + uc * 16), uso + (u32)(8 * i * 3584));
#pragma unroll 1
    for (int q = 0; q < 16; ++q) { const int qq = (q + rot) & 15; dma16(rwo, wov, (u32)((128 * cw + 8 * qq) * 1024 + cb * 128), Lb + (u32)(L_WO + (16 * cw + qq) * 1024)); }
#pragma unroll
    for (int q = 0; q < 2; ++q) dma16(rx, (u32)((lane >> 3) * 1024 + (lane & 7) * 16), (u32)((64 * tt + 16 * cw + 8 * q) * 1024 + cb * 128), Lb + (u32)(L_XRES + (2 * cw + q) * 1024));
    const u32 p1v = (u32)((lane >> 3) * 3584 + ((((lane >> 1) & 3) ^ ((lane >> 4) & 1)) * 32) + (lane & 1) * 16);
    auto dma_p1 = [&](int k) {
      const u32 so = (u32)((32 * ((k + rotg) & 15) + 8 * cw) * 3584 + jt * 224 + p0 * 32);
      dma16(rwg, p1v, so, Lb + (u32)(k * L_P1B + cw * 1024));
      dma16(rwu, p1v ^ 64u, so, Lb + (u32)(k * L_P1B + 4096 + cw * 1024));
    };
    dma_p1(0); dma_p1(1);
    bar();
    {
      waitv4<26>(g2_.q[0], g2_.q[1], g2_.q[2], g2_.q[3]);
      if (!__all(gath_ok(g2_, glo, ghi))) {
        const u64 t0 = __builtin_amdgcn_s_memrealtime();
        for (;;) {
          gath_load(g2_, rws, gch * 8u, 0u);
          if (__all(gath_ok(g2_, glo, ghi))) break;
          if (spin_out(t0)) break;
        }
      }
      const u32 v2 = gath_val(g2_);
      const bool one = h ? cw == 1 : !(lane & 16);
      const float a = bflo(v2), c = bfhi(v2);
      const u32 dst = h ? (cw == 0 ? L_GA : cw == 1 ? L_G2C : cw == 2 ? L_GBV : L_SCRAP) + tok * 8 : L_SL + cw * 256 + tok * 8;
      *(LAS f32x2*)(L + dst) = one ? f32x2{rbf(1.f + a), rbf(1.f + c)} : f32x2{a, c};
    }
    {
      waitv4<22>(uw[0], uw[1], uw[2], uw[3]);
      float a[16];
#pragma unroll
      for (int e = 0; e < 16; ++e) a[e] = 0.f;
#pragma unroll
      for (int i = 0; i < 4; ++i) {
        const float gk = ((const LAS float*)(L + L_SL + cw * 256))[8 * i + (lane >> 3)], sk = ((const LAS float*)(L + L_SL + cw * 256))[32 + 8 * i + (lane >> 3)];
#pragma unroll
        for (int e = 0; e < 4; ++e) {
          const float lo = bflo(uw[i][e]), hi = bfhi(uw[i][e]);
          a[2 * e] = fmaf(gk, lo, a[2 * e]); a[2 * e + 1] = fmaf(gk, hi, a[2 * e + 1]);
          a[8 + 2 * e] = fmaf(sk, lo, a[8 + 2 * e]); a[9 + 2 * e] = fmaf(sk, hi, a[9 + 2 * e]);
        }
      }
#pragma unroll
      for (int e = 0; e < 16; ++e) a[e] = dpp_add<0x128>(a[e]);
      float b8[8], ut4[4];
#pragma unroll
      for (int e = 0; e < 8; ++e) b8[e] = swap16_add(a[e], a[e + 8]);
#pragma unroll
      for (int e = 0; e < 4; ++e) ut4[e] = swap32_add(b8[e], b8[e + 4]);
      auto r0 = __builtin_amdgcn_permlane16_swap(u_(ut4[0]), u_(ut4[2]), false, false);
      auto r1 = __builtin_amdgcn_permlane16_swap(u_(ut4[1]), u_(ut4[3]), false, false);
      const u32 a0 = r0[0], b0 = r0[1], a1 = r1[0], b1 = r1[1];
      const int pr = uc >> 1, slot = pr >= p0 && pr < p0 + 4 ? pr - p0 : (pr < p0 ? 4 + pr : pr);
      if (i16 < 7) st128_sc1(u32x4{a0, b0, a1, b1}, rws, (u32)((slot * 32 + (cw & 1) * 16 + (uc & 1) * 8 + h * 4 + (g & 1) * 2) * 8), WS_UT + (u32)((jt * 16 + T) * 1792));
    }
    waitv<4>(); STAMP(10); bar();
#pragma unroll 1
    for (int k = 2; k < 8; ++k) dma_p1(k);
    waitv<12>(); STAMP(11); bar();
    if (tid == 256) st64_sc1(u32x2{tlo, thi}, rws, 0u, WS_FU + (u32)(jt * 256 + T * 8));
#pragma unroll 1
    for (int k = 8; k < 16; ++k) dma_p1(k);
    waitv<16>(); bar();
    u32 p2v[3];
#pragma unroll
    for (int i = 0; i < 3; ++i) { const int n = 64 * i + lane, row = n / 6, c = n - row * 6; p2v[i] = (u32)(row * 3584 + c * 16 + ((c >> 1) >= p0 ? 128 : 0)); }
    auto dma_p2 = [&](int k0, int k1) {
      if (ELIM & 1) return;
#pragma unroll 1
      for (int k = k0; k < k1; ++k) {
        const int pos = 2 * k + (cw >> 1);
#pragma unroll
        for (int i = 0; i < 3; ++i) dma16(rgu, p2v[i], (u32)(((pos + rotg) & 15) * 32 * 3584 + jt * 224), Lb + (u32)(pos * L_P2B + (cw & 1) * L_P2M + i * 1024));
      }
    };
    auto dma_wd = [&](int u) {
      if (ELIM & 4) return;
#pragma unroll
      for (int q = 0; q < 8; ++q) {
        const int n = 8 * (u - 4) + q;
        const int jj = ((u < 4 ? u : n / 6) + rotd) & 3, r6 = n % 6;
        const int row = u < 4 ? 112 * jj + 16 * jj + 8 * q : 112 * jj + 8 * r6 + ((r6 >> 1) >= jj ? 64 : 0);
        dma16(rwd, wov, (u32)((448 * cw + row) * 1024 + cb * 128), Lb + (u32)(wd_unit_off(u) + cw * 8192 + q * 1024));
      }
    };
    {
      wait_flags<16>(rws, WS_FU + (u32)(jt * 256), lane, tlo, thi);
      u32x2 pt[16];
#pragma unroll
      for (int k = 0; k < 16; ++k) ald64_sc1(pt[k], rws, (u32)(t4 < 224 ? t4 : 0) * 8u, WS_UT + (u32)((jt * 16 + k) * 1792));
      SB();
      float uu = 0.f, tt_ = 0.f;
      waitv<0>();
#pragma unroll
      for (int k = 0; k < 16; ++k) { asm volatile("" : "+v"(pt[k])); uu += f_(pt[k][0]); tt_ += f_(pt[k][1]); }
      const u32 hi = pk2(uu, tt_), lo = pk2(uu - bflo(hi), tt_ - bfhi(hi));
      const u32 u1 = hi & 0xffffu, t1 = hi >> 16, u2 = lo & 0xffffu, t2 = lo >> 16;
      const u32x4 o = u32x4{u1 | (u1 << 16), u2 | (u2 << 16), t1 | (t1 << 16), t2 | (t2 << 16)};
      if (t4 < 224) { LAS char* d = L + L_FIX + (t4 >> 5) * 1024 + (t4 & 31) * 16; *(LAS u32x4*)d = o; *(LAS u32x4*)(d + 512) = o; }
    }
    STAMP(12); bar();
    dma_p2(0, 5);
    if (b == 0 && cw == 3) {
      const u64 t0 = __builtin_amdgcn_s_memrealtime();
      for (;;) {
        bool ok = true;
#pragma unroll
        for (int k = 0; k < 4; ++k) { const int f = 64 * k + lane; const u32x2 v = ld64_sc1(rws, (u32)((f >> 3) * 1024 + (f & 7) * 8), WS_FO); ok = ok && v[0] == tlo && v[1] == thi; }
        if (__all(ok)) break;
        if (spin_out(t0)) break;
      }
      if (lane == 0) st64_sc1(u32x2{tlo, thi}, rws, 0u, WS_E);
    }
    waitv<0>(); STAMP(15); bar();
    dma_p2(5, 8);
    bar();
    dma_wd(0);
    waitv<8>(); STAMP(16); bar();
    dma_wd(1);
    bar();
    dma_wd(2);
    waitv<16>(); bar();
    dma_wd(3);
    waitv<16>(); STAMP(17); bar();
    dma_wd(4);
    waitv<16>(); bar();
    dma_wd(5);
    waitv<16>(); bar();
    dma_wd(6);
    waitv<16>(); STAMP(18); bar();
    waitv<8>(); bar();
    waitv<0>(); bar();
    bar();
  } else {
    f32x4 m0, m1, m0f;
#pragma unroll
    for (int r = 0; r < 4; ++r) {
      const int d1 = i16 - 4 * g - r, d0 = d1 + 16;
      m0[r] = (d0 <= 14 && !(d0 & 1)) ? 0.f : -1e30f;
      m1[r] = (d1 >= 0 && d1 <= 14 && !(d1 & 1)) ? 0.f : -1e30f;
      m0f[r] = tt == 0 ? -1e30f : m0[r];
    }
    f32x4 Kt[5], Qt[4], Vn[5];
#pragma unroll
    for (int r = 0; r < 5; ++r) { Kt[r] = f32x4{0, 0, 0, 0}; Vn[r] = f32x4{0, 0, 0, 0}; if (r < 4) Qt[r] = f32x4{0, 0, 0, 0}; }
    {
      u32x4 a[2][5], wl[2][3];
      LAS char* const w1a = opq(w1rd + L_RW); LAS char* const w1b = opq(w1rd + L_RW + 4 * L_W1S);
      auto ld = [&](int n, int bi) {
        LAS char* ab = L + L_AIMG + ((n + rot) & 15) * 1024 + g * 256 + ((i16 ^ (((n & 1) << 2) | g)) * 16);
#pragma unroll
        for (int rb = 0; rb < 5; ++rb) a[bi][rb] = *(const LAS u32x4*)(ab + rb * 16384);
        if (n < 6 || n >= 12) {
#pragma unroll
          for (int pp = 0; pp < 3; ++pp) wl[bi][pp] = trd2((n % 6 < 4 ? w1a : w1b) + (n % 6 % 4) * L_W1S + pp * 4096, 512);
        }
      };
      ld(0, 0); SB();
#pragma unroll
      for (int n = 0; n < 16; ++n) {
        if (n == 3) bar();
        if (n == 8) asm volatile("" : "+v"(m0), "+v"(m1), "+v"(m0f));
        if (n == 11) WACC(24, bar());
        if (n < 15) ld(n + 1, (n + 1) & 1);
        const bool rg = n >= 6 && n < 12;
        const u32x4 wq = rg ? wr[rg ? 3 * (n - 6) : 0] : wl[n & 1][0], wk = rg ? wr[rg ? 3 * (n - 6) + 1 : 0] : wl[n & 1][1], wv = rg ? wr[rg ? 3 * (n - 6) + 2 : 0] : wl[n & 1][2];
#pragma unroll
        for (int rb = 0; rb < 5; ++rb) {
          Kt[rb] = mfma16(wk, a[n & 1][rb], Kt[rb]);
          Vn[rb] = mfma16(a[n & 1][rb], wv, Vn[rb]);
          if (rb > 0) Qt[rb - 1] = mfma16(wq, a[n & 1][rb], Qt[rb - 1]);
        }
#pragma unroll
        for (int k = 0; k < 11; ++k) { SGB_MFMA(1); SGB_DSR(1); }
        SGB_MFMA(3);
        SB();
      }
    }
    bar();
    STAMP(8);
    const int tb = cw & 1, kh = cw >> 1;
    {
      u32x2 kp[5], vp[5], qp[4];
#pragma unroll
      for (int r = 0; r < 5; ++r) { kp[r] = u32x2{pk2(Kt[r][0], Kt[r][1]), pk2(Kt[r][2], Kt[r][3])}; if (r < 4) qp[r] = u32x2{pk2(Qt[r][0], Qt[r][1]), pk2(Qt[r][2], Qt[r][3])}; }
      f32x4 s0[4], s1[4];
#pragma unroll
      for (int qb = 0; qb < 4; ++qb) { s0[qb] = mfma16k16(kp[qb], qp[qb], qb == 0 ? m0f : m0); s1[qb] = mfma16k16(kp[qb + 1], qp[qb], m1); }
#pragma unroll
      for (int r = 0; r < 5; ++r) vp[r] = u32x2{pk2(Vn[r][0], Vn[r][1]), pk2(Vn[r][2], Vn[r][3])};
      float mx[4];
#pragma unroll
      for (int qb = 0; qb < 4; ++qb) mx[qb] = mx2(mx2(mx2(s0[qb][0], s0[qb][1]), mx2(s0[qb][2], s0[qb][3])), mx2(mx2(s1[qb][0], s1[qb][1]), mx2(s1[qb][2], s1[qb][3])));
#pragma unroll
      for (int qb = 0; qb < 4; ++qb) { auto r = __builtin_amdgcn_permlane16_swap(u_(mx[qb]), u_(mx[qb]), false, false); const u32 r0 = r[0], r1 = r[1]; mx[qb] = mx2(f_(r0), f_(r1)); }
#pragma unroll
      for (int qb = 0; qb < 4; ++qb) { auto r = __builtin_amdgcn_permlane32_swap(u_(mx[qb]), u_(mx[qb]), false, false); const u32 r0 = r[0], r1 = r[1]; mx[qb] = mx2(f_(r0), f_(r1)); }
      const float cs = 0.25f * 1.44269504088896f;
      const u32x4 ones = u32x4{0x3f803f80u, 0x3f803f80u, 0x3f803f80u, 0x3f803f80u};
      const f32x4 z = f32x4{0, 0, 0, 0};
      f32x4 ot[4], sm[4];
#pragma unroll
      for (int qb = 0; qb < 4; ++qb) {
        const float nm = -mx[qb] * cs;
        float e0[4], e1[4];
#pragma unroll
        for (int r = 0; r < 4; ++r) { e0[r] = __builtin_amdgcn_exp2f(fmaf(s0[qb][r], cs, nm)); e1[r] = __builtin_amdgcn_exp2f(fmaf(s1[qb][r], cs, nm)); }
        const u32x4 pf = u32x4{pk2(e0[0], e0[1]), pk2(e0[2], e0[3]), pk2(e1[0], e1[1]), pk2(e1[2], e1[3])};
        sm[qb] = mfma16(ones, pf, z);
        ot[qb] = mfma16(u32x4{vp[qb][0], vp[qb][1], vp[qb + 1][0], vp[qb + 1][1]}, pf, z);
      }
      u32x2 op[4];
#pragma unroll
      for (int qb = 0; qb < 4; ++qb) {
        const float ri = __builtin_amdgcn_rcpf(sm[qb][0]);
        op[qb] = u32x2{pk2(ot[qb][0] * ri, ot[qb][1] * ri), pk2(ot[qb][2] * ri, ot[qb][3] * ri)};
      }
#pragma unroll
      for (int t2 = 0; t2 < 2; ++t2) {
        auto r0 = __builtin_amdgcn_permlane16_swap(op[2 * t2][0], op[2 * t2 + 1][0], false, false);
        auto r1 = __builtin_amdgcn_permlane16_swap(op[2 * t2][1], op[2 * t2 + 1][1], false, false);
        const u32 a0 = r0[0], b0 = r0[1], a1 = r1[0], b1 = r1[1];
        st128_sc1(u32x4{a0, a1, b0, b1}, rws, (u32)(lane * 16), WS_O + (u32)(((2 * tt + t2) * 32 + 4 * hg + cw) * 1024));
      }
    }
    STAMP(9);
    pubbar<0>(8, rws, WS_FO + (u32)(tt * 1024 + hg * 8), tlo, thi, tid);
    STAMP(10);
    wait_flags<8>(rws, WS_FO + (u32)(tt * 1024 + cb * 128), lane, tlo, thi);
    STAMP(11);
    u32 x1p[8];
    {
      u32x4 of[16];
      const u32 ob = WS_O + (u32)(((2 * tt + tb) * 32 + 16 * kh) * 1024);
#pragma unroll
      for (int s = 0; s < 16; ++s) { of[s] = ld128_sc1(rws, (u32)(lane * 16), ob + (u32)(((s + 4 * hgl) & 15) * 1024)); SB(); }
      SB();
      WACC(24, bar());
      LAS char* q0 = L + (L_WO + (256 * kh + 8 * h + q4) * 128 + mrow * 32 + (lane & 3) * 8);
      LAS char* qa = q0 + ((q4 >> 1) ^ kh) * 64; LAS char* qb = q0 + ((q4 >> 1) ^ kh ^ 1) * 64;
      f32x16 a0, a1;
#pragma unroll
      for (int r = 0; r < 16; ++r) { a0[r] = 0.f; a1[r] = 0.f; }
      u32x4 wl[2][2];
      wl[0][0] = trd2(qa + hgl * 8192, 512); wl[0][1] = trd2(qb + hgl * 8192, 512); SB();
#pragma unroll
      for (int s = 0; s < 16; ++s) {
        if (s < 15) { const int o2 = ((((s + 1) >> 2) + hgl) & 3) * 8192 + ((s + 1) & 3) * 2048; wl[(s + 1) & 1][0] = trd2(qa + o2, 512); wl[(s + 1) & 1][1] = trd2(qb + o2, 512); }
        a0 = mfma32(wl[s & 1][0], of[s], a0); a1 = mfma32(wl[s & 1][1], of[s], a1);
        SGB_MFMA(1); SGB_DSR(2); SGB_MFMA(1); SGB_DSR(2);
        SB();
      }
      u32x2 xres[4]; f32x4 ga4[4], g24[4];
#pragma unroll
      for (int q = 0; q < 4; ++q) {
        xres[q] = *(const LAS u32x2*)(L + L_XRES + (32 * tb + tok) * 128 + (32 * kh + 8 * q + 4 * h) * 2);
        ga4[q] = *(const LAS f32x4*)(L + L_GA + (32 * kh + 8 * q + 4 * h) * 4); g24[q] = *(const LAS f32x4*)(L + L_G2C + (32 * kh + 8 * q + 4 * h) * 4);
      }
      const float mu1 = ((const LAS float*)(L + L_MU1))[32 * tb + tok];
#pragma unroll
      for (int q = 0; q < 4; ++q) *(LAS f32x4*)(L + L_RED2 + cw * 4096 + q * 1024 + lane * 16) = f32x4{a1[4 * q], a1[4 * q + 1], a1[4 * q + 2], a1[4 * q + 3]};
      f32x2 xa[8];
#pragma unroll
      for (int q = 0; q < 4; ++q) {
        xa[2 * q] = __builtin_elementwise_fma(f32x2{ga4[q][0], ga4[q][1]}, f32x2{a0[4 * q], a0[4 * q + 1]}, f32x2{bflo(xres[q][0]), bfhi(xres[q][0])});
        xa[2 * q + 1] = __builtin_elementwise_fma(f32x2{ga4[q][2], ga4[q][3]}, f32x2{a0[4 * q + 2], a0[4 * q + 3]}, f32x2{bflo(xres[q][1]), bfhi(xres[q][1])});
      }
#pragma unroll
      for (int q = 0; q < 8; q += 4) asm volatile("" : "+v"(xa[q]), "+v"(xa[q + 1]), "+v"(xa[q + 2]), "+v"(xa[q + 3]));
      STAMP(19);
      bar();
      STAMP(20);
      f32x2 sv1 = f32x2{0.f, 0.f}, sv2 = f32x2{0.f, 0.f}; u32 yp[8];
#pragma unroll
      for (int q = 0; q < 4; ++q) {
        const f32x4 o4 = *(const LAS f32x4*)(L + L_RED2 + (cw ^ 2) * 4096 + q * 1024 + lane * 16);
#pragma unroll
        for (int e = 0; e < 2; ++e) {
          const f32x2 v = __builtin_elementwise_fma(f32x2{ga4[q][2 * e], ga4[q][2 * e + 1]}, f32x2{o4[2 * e], o4[2 * e + 1]}, xa[2 * q + e]);
          x1p[2 * q + e] = pk2(v[0], v[1]);
          const f32x2 d = f32x2{bflo(x1p[2 * q + e]), bfhi(x1p[2 * q + e])} - f32x2{mu1, mu1};
          sv1 += d; sv2 = __builtin_elementwise_fma(d, d, sv2);
          const f32x2 y = d * f32x2{g24[q][2 * e], g24[q][2 * e + 1]};
          yp[2 * q + e] = pk2(y[0], y[1]);
        }
      }
      float s1 = sv1[0] + sv1[1], s2 = sv2[0] + sv2[1];
      const u32 yb = WS_Y + (u32)(((2 * tt + tb) * 32 + 4 * cb + 2 * kh) * 1024);
      st128_sc1(u32x4{yp[0], yp[1], yp[2], yp[3]}, rws, (u32)(lane * 16), yb);
      st128_sc1(u32x4{yp[4], yp[5], yp[6], yp[7]}, rws, (u32)(lane * 16), yb + 1024);
      s1 = swap32_add(s1, s1); s2 = swap32_add(s2, s2);
      if (lane < 32 && cb == 0 && kh == 0) __builtin_amdgcn_raw_buffer_store_b32(u_(mu1), rws, (u32)(tok * 4), WS_MU + (u32)((2 * tt + tb) * 128), 16);
      if (lane < 32) st64_sc1(u32x2{u_(s1), u_(s2)}, rws, (u32)(tok * 8), WS_ST + (u32)(((2 * tt + tb) * 16 + 2 * cb + kh) * 256));
    }
#pragma unroll
    for (int q = 0; q < 8; q += 4) asm volatile("" : "+v"(x1p[q]), "+v"(x1p[q + 1]), "+v"(x1p[q + 2]), "+v"(x1p[q + 3]));
    STAMP(12);
    pubbar<0>(16, rws, WS_FY + (u32)(tt * 2048 + cb * 8), tlo, thi, tid);
    STAMP(13);
    wait_flags<8>(rws, WS_FY + (u32)((2 * T + (cw >> 1)) * 2048 + jt * 128), lane, tlo, thi);
    STAMP(14);
    const int tb32 = 4 * T + cw;
    u32x4 yf[32];
    auto ldy = [&](int s) { yf[s] = ld128_sc1(rws, (u32)(lane * 16), WS_Y + (u32)((tb32 * 32 + ((s + 2 * rotg) & 31)) * 1024)); };
#pragma unroll
    for (int s = 0; s < (EXPW ? 32 : 10); ++s) { ldy(s); SB(); }
    const u32 stv8 = __builtin_amdgcn_raw_buffer_load_b32(rws, (u32)(tok * 4), WS_MU + (u32)(tb32 * 128), 16);
    u32x2 stv[8];
#pragma unroll
    for (int q = 0; q < 8; ++q) stv[q] = ld64_sc1(rws, (u32)(tok * 8 + h * 2048), WS_ST + (u32)((tb32 * 16 + q) * 256));
    SB();
    u32 mz, sz; float r2;
    auto act_store = [&](const f32x16& ac, int pp) {
      const float r1 = __builtin_amdgcn_sqrtf(r2);
      u32x4 o;
#pragma unroll
      for (int e = 0; e < 4; ++e) {
        const f32x2 gsc = f32x2{ac[2 * e], ac[2 * e + 1]} * f32x2{r2, r2};
        const f32x2 av = f32x2{fmaxf(gsc[0], 0.f), fmaxf(gsc[1], 0.f)} * f32x2{ac[8 + 2 * e], ac[9 + 2 * e]};
        o[e] = pk2(av[0], av[1]);
      }
      st128_sc1(o, rws, (u32)(lane * 16), WS_ACT + (u32)((tb32 * 112 + 7 * jt + pp) * 1024));
    };
    {
      f32x16 acc[4];
#pragma unroll
      for (int q = 0; q < 4; ++q)
#pragma unroll
        for (int r = 0; r < 16; ++r) acc[q][r] = 0.f;
      LAS char* pa[4];
#pragma unroll
      for (int pp = 0; pp < 4; ++pp) pa[pp] = L + (p1base + (((u32)pp ^ p1x) * 32));
      u32x4 wf[2][4], fx, fxp;
      auto ldw = [&](int st, int bi) {
#pragma unroll
        for (int pp = 0; pp < 4; ++pp) { if (st == 16) pa[pp] = opq(pa[pp] + 8 * L_P1B); wf[bi][pp] = trd2(pa[pp] + ((st >> 1) & 7) * L_P1B + (st & 1) * 2048, 1024); }
      };
      ldw(0, 0); SB();
#pragma unroll
      for (int st = 0; st < 32; ++st) {
        if (st == 15) WACC(25, bar());
        if (st == 12) {
      float s1 = 0.f, s2 = 0.f;
#pragma unroll
      for (int q = 0; q < 8; ++q) { s1 += f_(stv[q][0]); s2 += f_(stv[q][1]); }
      s1 = swap32_add(s1, s1); s2 = swap32_add(s2, s2);
      const float md0 = s1 * (1.f / 512.f), mu1t = f_(stv8);
      const float sg = rbf(__builtin_amdgcn_sqrtf(rbf(rbf(fmaxf(s2 * (1.f / 512.f) - md0 * md0, 0.f)) + 1e-5f)));
      const float md = rbf(md0 + mu1t) - mu1t;
      r2 = __builtin_amdgcn_rcpf(sg * sg);
      const u32 hi = pk2(-md, sg), lo = pk2(-md - bflo(hi), sg - bfhi(hi));
      const u32 mm = (hi & 0xffffu) | (lo << 16), ss = (hi >> 16) | (lo & 0xffff0000u);
      mz = h ? 0u : mm; sz = h ? 0u : ss;
      asm volatile("" : "+v"(mz), "+v"(sz), "+v"(r2));
    }
        if (st + 1 < 32) ldw(st + 1, (st + 1) & 1);
        if (OPT & 2) { if (2 * st + 10 < 32) { ldy(2 * st + 10); SB(); ldy(2 * st + 11); } }
        else if (!EXPW && st + 10 < 32) ldy(st + 10);
#pragma unroll
        for (int pp = 0; pp < 4; ++pp) acc[pp] = mfma32(wf[st & 1][pp], yf[st], acc[pp]);
#pragma unroll
        for (int pp = 0; pp < 4; ++pp) { SGB_MFMA(1); SGB_DSR(2); }
        SB();
        if (st >= 24 && st < 28) fx = *(const LAS u32x4*)(L + L_FIX + (st - 24) * 1024 + lane * 16), SB();
        if (st >= 25 && st < 29) acc[st - 25 < 4 && st >= 25 ? st - 25 : 0] = mfma32(fxp, u32x4{mz, mz, sz, sz}, acc[st - 25 < 4 && st >= 25 ? st - 25 : 0]), SB();
        fxp = fx;
      }
      STAMP(15);
#pragma unroll
      for (int pp = 0; pp < 4; ++pp) act_store(acc[pp], p0 + pp);
      WACC(26, bar());
    }
    u32x4 af[16][2]; u32x2 fl;
    auto lda = [&](int n) { const int s = n >> 1, t2 = n & 1; af[s][t2] = ld128_sc1(rws, (u32)(lane * 16), WS_ACT + (u32)(((2 * tt + (t2 ^ tb)) * 112 + 28 * cw + 8 * (((s >> 2) + rotd) & 3) + (s & 3)) * 1024)); };
    {
      f32x16 acc[3], z16;
#pragma unroll
      for (int r = 0; r < 16; ++r) z16[r] = 0.f;
#pragma unroll
      for (int pp = 0; pp < 3; ++pp) acc[pp] = mfma32(*(const LAS u32x4*)(L + L_FIX + (4 + pp) * 1024 + lane * 16), u32x4{mz, mz, sz, sz}, z16);
      LAS char* pb = L + p2base;
      u32x4 wf[2][3];
      auto ldw = [&](int st, int bi) {
        if (st == 16) pb = opq(pb + 8 * L_P2B);
#pragma unroll
        for (int pp = 0; pp < 3; ++pp) wf[bi][pp] = trd2(pb + ((st >> 1) & 7) * L_P2B + (st & 1) * 1536 + pp * 32, 768);
      };
      ldw(0, 0); SB();
#pragma unroll
      for (int st = 0; st < 32; ++st) {
        if (st == S0P) WACC(30, pubbar<0>(16, rws, WS_FA + (u32)(T * 2048 + jt * 8), tlo, thi, tid));
        if (st == 15 || st == 23) WACC(27, bar());
        if (st == FA1P) fl = peek_flags<4>(rws, WS_FA + (u32)(T * 2048 + ((tt & 1) * 8 + cb) * 128 + cw * 32), lane);
        if (st == FA1P + 4) { STAMP(23); WACC(28, wait_flags_from<4>(fl, rws, WS_FA + (u32)(T * 2048 + ((tt & 1) * 8 + cb) * 128 + cw * 32), lane, tlo, thi)); }
        if (st + 1 < 32) ldw(st + 1, (st + 1) & 1);
        if (st >= FA1P + 4) { const int k = st - FA1P - 4; if (A1R == 1) lda(k); else if (A1R == 2) { lda(2 * k); SB(); lda(2 * k + 1); } else { lda(3 * (k >> 1) + (k & 1) * 2); if (!(k & 1)) { SB(); lda(3 * (k >> 1) + 1); } } }
#pragma unroll
        for (int pp = 0; pp < 3; ++pp) acc[pp] = mfma32(wf[st & 1][pp], yf[st], acc[pp]);
#pragma unroll
        for (int pp = 0; pp < 3; ++pp) { SGB_MFMA(1); SGB_DSR(2); }
        SB();
      }
      WACC(27, bar());
      STAMP(16);
#pragma unroll
      for (int pp = 0; pp < 3; ++pp) act_store(acc[pp], pp >= p0 ? pp + 4 : pp);
#pragma unroll
      for (int n = A1N; n < 24; ++n) { lda(n); SB(); }
    }
    SB();
    {
      f32x16 acc[4];
#pragma unroll
      for (int q = 0; q < 4; ++q)
#pragma unroll
        for (int r = 0; r < 16; ++r) acc[q][r] = 0.f;
      LAS char* d0 = L + (cw * 8192 + (4 * h + q4) * 128 + mrow * 32 + (lane & 3) * 8);
      LAS char* da = d0 + ((q4 >> 1) ^ kh) * 64; LAS char* db = d0 + ((q4 >> 1) ^ kh ^ 1) * 64;
      u32x4 wl[2][2], a2[12][2];
      auto lda2 = [&](int n) {
        const int s2 = n >> 1, t2 = n & 1, jj = (s2 / 3 + rotd) & 3, pp = s2 % 3;
        a2[s2][t2] = ld128_sc1(rws, (u32)(lane * 16), WS_ACT + (u32)(((2 * tt + (t2 ^ tb)) * 112 + 28 * cw + 7 * jj + (pp >= jj ? pp + 4 : pp)) * 1024));
      };
      LAS char* const dah = opq(da + 65536); LAS char* const dbh = opq(db + 65536);
      auto ldw = [&](int s, int bi) { const u32 o = wd_unit_off(s >> 2) + (s & 3) * 2048; wl[bi][0] = trd2(o < 65536 ? da + o : dah + (o - 65536), 1024); wl[bi][1] = trd2(o < 65536 ? db + o : dbh + (o - 65536), 1024); };
      ldw(0, 0); SB();
#pragma unroll
      for (int s = 0; s < 28; ++s) {
        if (s == 3) WACC(30, pubbar<(A1N < 24 ? 24 - A1N : 0) + (A1N <= 24 ? 3 : (A1N < 27 ? 27 - A1N : 0))>(16, rws, WS_FA2 + (u32)(T * 2048 + jt * 8), tlo, thi, tid));
        if ((s & 3) == 3 && s > 3 && s < 24) WACC(29, bar());
        if (!D0 && s == 12) {
          STAMP(17);
          wait_flags_from<4>(fl, rws, WS_FA2 + (u32)(T * 2048 + ((tt & 1) * 8 + cb) * 128 + cw * 32), lane, tlo, thi);
          STAMP(21);
#pragma unroll
          for (int n = 0; n < 12; ++n) { lda2(n); SB(); }
          SB();
        }
        if (s < 8 && 24 + s >= A1N) lda(24 + s);
        if (!D0 && s == 8) fl = peek_flags<4>(rws, WS_FA2 + (u32)(T * 2048 + ((tt & 1) * 8 + cb) * 128 + cw * 32), lane);
        if (!D0 && s >= 13 && s < 19) { lda2(12 + 2 * (s - 13)); SB(); lda2(13 + 2 * (s - 13)); }
        if (s + 1 < 28) ldw(s + 1, (s + 1) & 1);
        const u32x4 b0 = s < 16 ? af[s < 16 ? s : 0][0] : a2[s < 16 ? 0 : s - 16][0], b1 = s < 16 ? af[s < 16 ? s : 0][1] : a2[s < 16 ? 0 : s - 16][1];
        acc[0] = mfma32(wl[s & 1][0], b0, acc[0]); acc[2] = mfma32(wl[s & 1][1], b0, acc[2]);
        acc[1] = mfma32(wl[s & 1][0], b1, acc[1]); acc[3] = mfma32(wl[s & 1][1], b1, acc[3]);
        SGB_MFMA(1); SGB_DSR(2); SGB_MFMA(1); SGB_DSR(2); SGB_MFMA(2);
        SB();
      }
      STAMP(22);
      int ln2 = lane; asm volatile("" : "+v"(ln2));
#pragma unroll
      for (int i = 1; i < 4; ++i)
#pragma unroll
        for (int q = 0; q < 4; ++q) *(LAS f32x4*)(L + L_RED4 + ((i ^ cw) * 3 + i - 1) * 4096 + q * 1024 + ln2 * 16) = f32x4{acc[i][4 * q], acc[i][4 * q + 1], acc[i][4 * q + 2], acc[i][4 * q + 3]};
      f32x4 gb4[4]; f32x2 xa[8];
#pragma unroll
      for (int q = 0; q < 4; ++q) {
        gb4[q] = *(const LAS f32x4*)(L + L_GBV + (32 * kh + 8 * q + 4 * (ln2 >> 5)) * 4);
#pragma unroll
        for (int e = 0; e < 2; ++e)
          xa[2 * q + e] = __builtin_elementwise_fma(f32x2{gb4[q][2 * e], gb4[q][2 * e + 1]}, f32x2{acc[0][4 * q + 2 * e], acc[0][4 * q + 2 * e + 1]}, f32x2{bflo(x1p[2 * q + e]), bfhi(x1p[2 * q + e])});
      }
#pragma unroll
      for (int q = 0; q < 8; q += 4) asm volatile("" : "+v"(xa[q]), "+v"(xa[q + 1]), "+v"(xa[q + 2]), "+v"(xa[q + 3]));
      bar();
      u32 ob[8];
#pragma unroll
      for (int q = 0; q < 4; ++q) {
        const LAS char* rp = L + L_RED4 + cw * 3 * 4096 + q * 1024 + ln2 * 16;
        const f32x4 rs = (*(const LAS f32x4*)rp + *(const LAS f32x4*)(rp + 4096)) + *(const LAS f32x4*)(rp + 8192);
#pragma unroll
        for (int e = 0; e < 2; ++e) {
          const f32x2 v = __builtin_elementwise_fma(f32x2{gb4[q][2 * e], gb4[q][2 * e + 1]}, f32x2{rs[2 * e], rs[2 * e + 1]}, xa[2 * q + e]);
          ob[2 * q + e] = pk2(v[0], v[1]);
        }
      }
#pragma unroll
      for (int m = 0; m < 2; ++m) {
        auto r0 = __builtin_amdgcn_permlane32_swap(ob[4 * m], ob[4 * m + 2], false, false);
        auto r1 = __builtin_amdgcn_permlane32_swap(ob[4 * m + 1], ob[4 * m + 3], false, false);
        const u32 a0 = r0[0], b0 = r0[1], a1 = r1[0], b1 = r1[1];
        __builtin_amdgcn_raw_buffer_store_b128(u32x4{a0, a1, b0, b1}, rout, (u32)((64 * tt + 32 * tb + (ln2 & 31)) * 1024 + (64 * cb + 32 * kh + 16 * m + 8 * (ln2 >> 5)) * 2), 0, 0);
      }
    }
    STAMP(18);
  }
}

__global__ __launch_bounds__(512, 2) void fused_block(Params p) {
  extern __shared__ __attribute__((aligned(16))) char smem_[];
  asm volatile("" :: "s"(p.x), "s"(p.wqkv), "s"(p.wo), "s"(p.wg), "s"(p.wu), "s"(p.wd), "s"(p.c), "s"(p.wmod), "s"(p.out), "s"(p.ws));
  int w = __builtin_amdgcn_readfirstlane(threadIdx.x >> 6);
  u32x4 wr[18]; u32 tlo, thi;
  front(p, (LAS char*)smem_, threadIdx.x, w, blockIdx.x, wr, tlo, thi STAMP_ARG);
  if (w >= 4) {
    int b = blockIdx.x, tid = threadIdx.x; asm volatile("; H role" : "+s"(b), "+s"(w)); asm volatile("; H role" : "+v"(tid));
    role_body<false>(p, (LAS char*)smem_, tid, w, b, wr, tlo, thi STAMP_ARG);
  } else {
    int b = blockIdx.x, tid = threadIdx.x; asm volatile("; C role" : "+s"(b), "+s"(w)); asm volatile("; C role" : "+v"(tid));
    role_body<true>(p, (LAS char*)smem_, tid, w, b, wr, tlo, thi STAMP_ARG);
  }
}

extern "C" void kernel_launch(void* const* d_in, const int* in_sizes, int n_in, void* d_out, int out_size, void* d_ws, size_t ws_size, hipStream_t stream) {
  Params p;
  p.x = d_in[0]; p.wqkv = d_in[1]; p.wo = d_in[2]; p.wg = d_in[4]; p.wu = d_in[5]; p.wd = d_in[6]; p.c = d_in[7]; p.wmod = d_in[8];
  p.out = d_out; p.ws = d_ws;
  (void)hipFuncSetAttribute((const void*)fused_block, hipFuncAttributeMaxDynamicSharedMemorySize, LDS_BYTES);
  fused_block<<<256, 512, LDS_BYTES, stream>>>(p);
}
```

```cpp
#include <hip/hip_runtime.h>
#include <stdint.h>

typedef unsigned short u16;
typedef unsigned u32;
typedef unsigned long long u64;
typedef u32 u32x2 __attribute__((ext_vector_type(2)));
typedef u32 u32x4 __attribute__((ext_vector_type(4)));
typedef short s16x4 __attribute__((ext_vector_type(4)));
typedef short s16x8 __attribute__((ext_vector_type(8)));
typedef __bf16 bf16x2 __attribute__((ext_vector_type(2)));
typedef __bf16 bf16x8 __attribute__((ext_vector_type(8)));
typedef float f32x2 __attribute__((ext_vector_type(2)));
typedef float f32x4 __attribute__((ext_vector_type(4)));
typedef float f32x16 __attribute__((ext_vector_type(16)));
typedef __amdgpu_buffer_rsrc_t rsrc_t;
#define LAS __attribute__((address_space(3)))
#define DI __device__ __forceinline__

#define EXPW 0
#define TOUCH 6
#define THR 4
constexpr unsigned CODE_BYTES = 40000;
#define NTX 0
#define FA1P 16
#define OPT 0
#define ELIM 0
#define S0P 3
#define A1R 1
#define A1N (A1R == 1 ? 28 - FA1P : A1R == 2 ? 2 * (28 - FA1P) : 3 * (28 - FA1P) / 2)
#define W1T 0
#define D0 0
#define A2C 9
#define LITE 0
#define MIM 16
#define MALLT 0
#define FIXP 1
#define DIAG 0
#define XMAP 2

constexpr int S_ = 2048, D_ = 512, F_ = 1792;
constexpr u32 WS_E = 0, WS_MODG = 393216, WS_FU = 57344, WS_FO = 8192, WS_FY = 983040, WS_FA = 327680, WS_FA2 = 360448, WS_DBG = 65536;
constexpr u32 WS_MU = 499712;
constexpr u32 WS_UT = 524288;
constexpr u32 WS_O = 1u << 20, WS_Y = 3u << 20, WS_ST = 5u << 20, WS_ACT = 6u << 20, WS_BYTES = 13u << 20;
constexpr u32 L_AIMG = 0, L_RW = 81920, L_W1S = 12288;
constexpr u32 L_LNP = 73728, L_PRED = L_LNP + 6400, L_RSNB = L_PRED + 1088;
constexpr u32 L_XRES = 65536, L_WO = 73728, L_RED2 = 139264;
constexpr u32 L_P1B = 8192, L_P2B = 6400, L_P2M = 3200;
constexpr u32 L_RED4 = 65536;
constexpr u32 L_MISC = 155648, L_G1 = L_MISC, L_S1 = L_MISC + 2048, L_FIX = L_MISC;
constexpr u32 L_SL = L_MISC + 4096, L_SCRAP = L_MISC + 6656;
constexpr u32 L_MU1 = L_MISC + 7168, L_GA = L_MU1 + 256, L_G2C = L_GA + 256, L_GBV = L_G2C + 256;
constexpr u32 LDS_BYTES = 163840;
__host__ __device__ constexpr u32 wd_unit_off(int u) { return u == 0 || u == 4 ? 102400u : (u == 1 || u == 5 ? 0u : (u == 2 || u == 6 ? 32768u : 65536u)); }

template <int N> struct ic { static constexpr int value = N; };
struct Params { const void *x, *wqkv, *wo, *wg, *wu, *wd, *c, *wmod; void *out; void *ws; };

DI rsrc_t mk(const void* p, u32 bytes) { return __builtin_amdgcn_make_buffer_rsrc((void*)p, 0, bytes, 0x00020000); }
DI float bflo(u32 v) { return __builtin_bit_cast(float, v << 16); }
DI float bfhi(u32 v) { return __builtin_bit_cast(float, v & 0xffff0000u); }
DI u32 pk2(float a, float b) { bf16x2 r; r[0] = (__bf16)a; r[1] = (__bf16)b; return __builtin_bit_cast(u32, r); }
DI float rbf(float a) { return bflo(pk2(a, a)); }
DI f32x4 mfma16(u32x4 a, u32x4 b, f32x4 c) { return __builtin_amdgcn_mfma_f32_16x16x32_bf16(__builtin_bit_cast(bf16x8, a), __builtin_bit_cast(bf16x8, b), c, 0, 0, 0); }
DI f32x4 mfma16k16(u32x2 a, u32x2 b, f32x4 c) { return __builtin_amdgcn_mfma_f32_16x16x16bf16_1k(__builtin_bit_cast(s16x4, a), __builtin_bit_cast(s16x4, b), c, 0, 0, 0); }
DI f32x16 mfma32(u32x4 a, u32x4 b, f32x16 c) { return __builtin_amdgcn_mfma_f32_32x32x16_bf16(__builtin_bit_cast(bf16x8, a), __builtin_bit_cast(bf16x8, b), c, 0, 0, 0); }
DI u32x2 trd(LAS char* p) { return __builtin_bit_cast(u32x2, __builtin_amdgcn_ds_read_tr16_b64_v4i16((LAS s16x4*)p)); }
DI u32x4 trd2(LAS char* p, int d) { u32x2 a = trd(p), b = trd(p + d); return u32x4{a[0], a[1], b[0], b[1]}; }
DI LAS char* opq(LAS char* p) { asm volatile("" : "+v"(p)); return p; }
DI void dma16(rsrc_t rs, u32 voff, u32 soff, u32 lds) {
  asm volatile("s_mov_b32 m0, %0\n\ts_nop 0\n\tbuffer_load_dwordx4 %1, %2, %3 offen lds" :: "s"(lds), "v"(voff), "s"(rs), "s"(soff) : "m0", "memory");
}
template <int OFF> DI void ald128(u32x4& d, rsrc_t rs, u32 voff, u32 soff) {
  asm volatile("buffer_load_dwordx4 %0, %1, %2, %3 offen offset:%c4" : "=v"(d) : "v"(voff), "s"(rs), "s"(soff), "i"(OFF) : "memory");
}
DI void ald128_nt(u32x4& d, rsrc_t rs, u32 voff, u32 soff) {
  asm volatile("buffer_load_dwordx4 %0, %1, %2, %3 offen" : "=v"(d) : "v"(voff), "s"(rs), "s"(soff) : "memory");
}
template <int OFF> DI void ald128_sc1(u32x4& d, rsrc_t rs, u32 voff, u32 soff) {
  asm volatile("buffer_load_dwordx4 %0, %1, %2, %3 offen offset:%c4 sc1" : "=v"(d) : "v"(voff), "s"(rs), "s"(soff), "i"(OFF) : "memory");
}
DI void ald64_sc1(u32x2& d, rsrc_t rs, u32 voff, u32 soff) {
  asm volatile("buffer_load_dwordx2 %0, %1, %2, %3 offen sc1" : "=v"(d) : "v"(voff), "s"(rs), "s"(soff) : "memory");
}
DI void ald64(u32x2& d, rsrc_t rs, u32 voff, u32 soff) {
  asm volatile("buffer_load_dwordx2 %0, %1, %2, %3 offen" : "=v"(d) : "v"(voff), "s"(rs), "s"(soff) : "memory");
}
DI void warm(u32 lds, rsrc_t rs, u32 voff, u32 soff) {
  asm volatile("s_mov_b32 m0, %0\n\ts_nop 0\n\tbuffer_load_dword %1, %2, %3 offen lds" :: "s"(lds), "v"(voff), "s"(rs), "s"(soff) : "m0", "memory");
}
template <int N> DI void waitv() { asm volatile("s_waitcnt vmcnt(%c0)" :: "i"(N) : "memory"); }
template <int N> DI void waitv4(u32x4& a, u32x4& b, u32x4& c, u32x4& d) { asm volatile("s_waitcnt vmcnt(%c4)" : "+v"(a), "+v"(b), "+v"(c), "+v"(d) : "i"(N) : "memory"); }
template <int N> DI void waitv1(u32x4& a) { asm volatile("s_waitcnt vmcnt(%c1)" : "+v"(a) : "i"(N) : "memory"); }
DI void bar() { __builtin_amdgcn_sched_barrier(0); asm volatile("s_waitcnt lgkmcnt(0)\n\ts_barrier" ::: "memory"); __builtin_amdgcn_sched_barrier(0); }
DI u32x4 ld128_sc1(rsrc_t rs, u32 voff, u32 soff) { return __builtin_amdgcn_raw_buffer_load_b128(rs, voff, soff, 16); }
DI u32x2 ld64_sc1(rsrc_t rs, u32 voff, u32 soff) { return __builtin_amdgcn_raw_buffer_load_b64(rs, voff, soff, 16); }
DI void st128_sc1(u32x4 v, rsrc_t rs, u32 voff, u32 soff) { __builtin_amdgcn_raw_buffer_store_b128(v, rs, voff, soff, 16); }
DI void st64_sc1(u32x2 v, rsrc_t rs, u32 voff, u32 soff) { __builtin_amdgcn_raw_buffer_store_b64(v, rs, voff, soff, 16); }
DI float f_(u32 v) { return __builtin_bit_cast(float, v); }
DI u32 u_(float v) { return __builtin_bit_cast(u32, v); }
DI float swap16_add(float a, float b) { auto r = __builtin_amdgcn_permlane16_swap(u_(a), u_(b), false, false); u32 r0 = r[0], r1 = r[1]; return f_(r0) + f_(r1); }
DI float swap32_add(float a, float b) { auto r = __builtin_amdgcn_permlane32_swap(u_(a), u_(b), false, false); u32 r0 = r[0], r1 = r[1]; return f_(r0) + f_(r1); }
DI float swap16_max(float a) { auto r = __builtin_amdgcn_permlane16_swap(u_(a), u_(a), false, false); u32 r0 = r[0], r1 = r[1]; return fmaxf(f_(r0), f_(r1)); }
DI float swap32_max(float a) { auto r = __builtin_amdgcn_permlane32_swap(u_(a), u_(a), false, false); u32 r0 = r[0], r1 = r[1]; return fmaxf(f_(r0), f_(r1)); }
DI float mx2(float a, float b) { return __builtin_amdgcn_fmed3f(a, b, __builtin_inff()); }
template <int CTRL> DI float dpp_add(float a) { return a + f_(__builtin_amdgcn_update_dpp(0, u_(a), CTRL, 0xf, 0xf, true)); }
DI float dot2(u32 a, u32 b, float c) { return __builtin_amdgcn_fdot2_f32_bf16(__builtin_bit_cast(bf16x2, a), __builtin_bit_cast(bf16x2, b), c, false); }

constexpr u64 SPIN_TICKS = 20000;
DI bool spin_out(u64 t0) { __builtin_amdgcn_s_sleep(1); return (__builtin_amdgcn_s_memrealtime() - t0) > SPIN_TICKS; }

#define SB() __builtin_amdgcn_sched_barrier(0)
#define SGB_MFMA(n) __builtin_amdgcn_sched_group_barrier(0x8, n, 0)
#define SGB_DSR(n) __builtin_amdgcn_sched_group_barrier(0x100, n, 0)
#define SGB_DSW(n) __builtin_amdgcn_sched_group_barrier(0x200, n, 0)
#define STAMP(i) do {} while (0)
#define WACC(i, code) do { code; } while (0)

template <int YOUNGER> DI void pubbar(int NC, rsrc_t rws, u32 flag_off, u32 tlo, u32 thi, int tid) {
  waitv<YOUNGER>(); bar();
  if (tid < NC) st64_sc1(u32x2{tlo, thi}, rws, (u32)tid * 128u, flag_off);
}
template <int NF> DI u32x2 peek_flags(rsrc_t rws, u32 off, int lane) { return ld64_sc1(rws, (u32)(lane & (NF - 1)) * 8u, off); }
template <int NF> DI void wait_flags(rsrc_t rws, u32 off, int lane, u32 tlo, u32 thi) {
  const u32 vo = (u32)(lane & (NF - 1)) * 8u;
  const u64 t0 = __builtin_amdgcn_s_memrealtime();
  for (;;) {
    u32x2 f = ld64_sc1(rws, vo, off);
    if (__all(f[0] == tlo && f[1] == thi)) break;
    if (spin_out(t0)) break;
  }
  __builtin_amdgcn_fence(__ATOMIC_ACQUIRE, "wavefront");
}
DI bool gran_ok(u32x4 g, u32 glo, u32 ghi) { return (g[0] & 0xffu) == glo && g[1] == ghi && (g[2] & 0xffu) == glo && g[3] == ghi; }

#define STAMP_PARAM
#define STAMP_ARG
template <int NF> DI void wait_flags_from(u32x2 seen, rsrc_t rws, u32 off, int lane, u32 tlo, u32 thi) {
  if (!__all(seen[0] == tlo && seen[1] == thi)) wait_flags<NF>(rws, off, lane, tlo, thi);
  __builtin_amdgcn_fence(__ATOMIC_ACQUIRE, "wavefront");
}
struct Gath { u32x4 q[4]; };
DI void gath_load(Gath& G, rsrc_t rws, u32 voff, u32 soff) {
#pragma unroll
  for (int k = 0; k < 4; ++k) G.q[k] = ld128_sc1(rws, voff, WS_MODG + soff + (u32)k * 24576u);
}
DI bool gath_ok(const Gath& G, u32 glo, u32 ghi) { return gran_ok(G.q[0], glo, ghi) && gran_ok(G.q[1], glo, ghi) && gran_ok(G.q[2], glo, ghi) && gran_ok(G.q[3], glo, ghi); }
DI u32 gath_val(const Gath& G) {
  const float a = (f_(G.q[0][0] & 0xffffff00u) + f_(G.q[1][0] & 0xffffff00u)) + (f_(G.q[2][0] & 0xffffff00u) + f_(G.q[3][0] & 0xffffff00u));
  const float b = (f_(G.q[0][2] & 0xffffff00u) + f_(G.q[1][2] & 0xffffff00u)) + (f_(G.q[2][2] & 0xffffff00u) + f_(G.q[3][2] & 0xffffff00u));
  return pk2(a, b);
}
DI void mod_issue(u32x4 (&wm)[4], u32x2& cv, rsrc_t rwm, rsrc_t rc, int mq, int mblk, int cw, int lane) {
    const int r0 = 128 * mq + 32 * cw;
#pragma unroll
    for (int i = 0; i < 4; ++i) ald128_nt(wm[i], rwm, (u32)((lane >> 3) * 4 * 6144 + (lane & 7) * 16), (u32)((r0 + i) * 6144 + mblk * 128));
    ald64(cv, rc, (u32)((lane >> 3) * 8), (u32)(r0 * 2));
}
DI void mod_finish(LAS char* L, u32 pred, const u32x4 (&wm)[4], const u32x2& cv, u32 tlo, u32 thi, rsrc_t rws, int mq, int mblk, int cw, int lane) {
    const int g = lane >> 4, i16 = lane & 15;
    float a8[8];
#pragma unroll
    for (int e = 0; e < 8; ++e) a8[e] = 0.f;
#pragma unroll
    for (int i = 0; i < 4; ++i) {
      const float cf = (i & 1) ? bfhi(cv[i >> 1]) : bflo(cv[i >> 1]);
      const float sv = rbf(cf * __builtin_amdgcn_rcpf(1.f + __expf(-cf)));
#pragma unroll
      for (int e = 0; e < 4; ++e) { a8[2 * e] = fmaf(sv, bflo(wm[i][e]), a8[2 * e]); a8[2 * e + 1] = fmaf(sv, bfhi(wm[i][e]), a8[2 * e + 1]); }
    }
#pragma unroll
    for (int e = 0; e < 8; ++e) a8[e] = dpp_add<0x128>(a8[e]);
    float b4[4], b2[2];
#pragma unroll
    for (int e = 0; e < 4; ++e) b4[e] = swap16_add(a8[e], a8[e + 4]);
#pragma unroll
    for (int e = 0; e < 2; ++e) b2[e] = swap32_add(b4[e], b4[e + 2]);
    if (i16 < 8) *(LAS f32x2*)(L + pred + cw * 256 + (i16 * 8 + (g & 1) * 4 + (g >> 1) * 2) * 4) = f32x2{b2[0], b2[1]};
    asm volatile("s_waitcnt lgkmcnt(0)" ::: "memory");
    if (lane == 0) *(volatile LAS u32x2*)(L + pred + 1024 + cw * 8) = u32x2{tlo, thi};
    if (cw == 0) {
      const u64 t0 = __builtin_amdgcn_s_memrealtime();
      for (;;) {
        const u32x2 tg = *(volatile LAS u32x2*)(L + pred + 1024 + (lane & 3) * 8);
        if (__all(tg[0] == tlo && tg[1] == thi)) break;
        if ((__builtin_amdgcn_s_memrealtime() - t0) > SPIN_TICKS) break;
      }
      asm volatile("" ::: "memory");
      LAS float* s = (LAS float*)(L + pred) + lane;
      const float v = (s[0] + s[64]) + (s[128] + s[192]);
      st64_sc1(u32x2{((u_(v) + 0x80u) & 0xffffff00u) | (tlo & 0xffu), (tlo >> 8) | (thi << 24)}, rws, (u32)(lane * 8), WS_MODG + (u32)(mq * 24576 + mblk * 512));
    }
}
DI void front(const Params& p, LAS char* L, const int tid, const int w, const int b, u32x4 (&wr)[18], u32& tlo, u32& thi STAMP_PARAM) {
  const bool isC = w < 4;
  f32x2 xh[10][4];
  const u32 Lb = (u32)(size_t)L;
  const int lane = tid & 63, cw = w & 3;
  const int xcd = b & 7, idx = b >> 3;
  const int tt = 8 * (xcd >> 1) + (idx >> 2), hgl = idx & 3, hg = 4 * (xcd & 1) + hgl;
  const int jt = 8 * (xcd & 1) + 4 * (tt & 1) + hgl;
  const int T = tt >> 1, cb = hg;
  STAMP(0);
  const rsrc_t rx = mk(p.x, S_ * D_ * 2), rw1 = mk(p.wqkv, D_ * 3 * D_ * 2), rwo = mk(p.wo, D_ * D_ * 2);
  const rsrc_t rwg = mk(p.wg, D_ * F_ * 2), rwu = mk(p.wu, D_ * F_ * 2), rwd = mk(p.wd, F_ * D_ * 2);
  const rsrc_t rc = mk(p.c, D_ * 2), rwm = mk(p.wmod, D_ * 6 * D_ * 2), rout = mk(p.out, S_ * D_ * 2), rws = mk(p.ws, WS_BYTES);
  const int g = lane >> 4, i16 = lane & 15, h = lane >> 5, tok = lane & 31, t4 = tid & 255;

  const int nrot = 2 * hgl, rot = 2 * (tt & 7);
  const u32 w1v = (u32)((lane >> 3) * 3072 + (((((lane >> 1) & 3) ^ ((lane >> 4) & 1)) ^ ((cw & 1) << 1)) * 32) + (lane & 1) * 16);
  auto w1slot = [](int n) { return n < 6 ? L_RW + n * L_W1S : (n < 12 ? (n - 6) * L_W1S : L_RW + (n - 12) * L_W1S); };
  {
    u64 pc; u32 len;
    asm volatile("s_getpc_b64 %0\n.Lfb_pc:\n\ts_mov_b32 %1, .Lfb_end-.Lfb_pc" : "=&s"(pc), "=s"(len));
    const rsrc_t rcode = mk((const void*)pc, len);
    warm(Lb + L_SCRAP, rcode, (u32)((idx * 8 + w) * 2 + (lane & 1)) * 128u, 0u);
  }
  u32x2 ev; ald64_sc1(ev, rws, 0u, WS_E);
  const bool prod = isC && idx < 8, prod2 = isC && idx >= 8 && idx < 24;
  const int mq = idx & 3, mblk = idx < 8 ? xcd * 2 + (idx >> 2) : 16 + xcd * 4 + ((idx - 8) >> 2);
  auto dma_w1 = [&](int i) {
    const int n = (i < 9 ? 6 : 0) + 2 * ((i % 9) / 3) + (w >> 2), pp = i % 3;
    dma16(rw1, w1v, (u32)((32 * ((n + rot) & 15) + 8 * cw) * 3072 + pp * 1024 + hg * 128), Lb + (u32)((i < 9 ? (n - 6) * L_W1S : L_RW + n * L_W1S) + pp * 4096 + cw * 1024));
  };
  u32x2 cv; u32x4 wm[4];
  if (prod) mod_issue(wm, cv, rwm, rc, mq, mblk, cw, lane);
  u32x4 xr[10];
#pragma unroll
  for (int n = 0; n < 10; ++n) {
    int r8 = n + nrot; r8 = r8 >= 10 ? r8 - 10 : r8;
    int row = 64 * tt - 16 + 8 * r8 + (lane >> 3); row = row < 0 ? 0 : row;
    ald128_nt(xr[n], rx, (u32)(row * 1024 + (lane & 7) * 16), (u32)(w * 128));
  }
  constexpr int Y0 = 0;
  STAMP(1);
  if (prod) {
    asm volatile("s_waitcnt vmcnt(%c6)" : "+v"(wm[0]), "+v"(wm[1]), "+v"(wm[2]), "+v"(wm[3]), "+v"(cv), "+v"(ev) : "i"(10 + Y0) : "memory");
    const u64 e1 = (((u64)__builtin_amdgcn_readfirstlane(ev[1]) << 32) | (u64)__builtin_amdgcn_readfirstlane(ev[0])) + 1ull;
    mod_finish(L, L_PRED, wm, cv, (u32)e1, (u32)(e1 >> 32), rws, mq, mblk, cw, lane);
  }
  STAMP(2);
  STAMP(3);
  {
    const int c8 = lane & 7, r8l = lane >> 3;
    float pv[10];
#pragma unroll
    for (int n = 0; n < 10; ++n) {
      waitv1<9>(xr[n]);
#pragma unroll
      for (int e = 0; e < 4; ++e) xh[n][e] = f32x2{bflo(xr[n][e]), bfhi(xr[n][e])};
      const f32x2 a1 = (xh[n][0] + xh[n][1]) + (xh[n][2] + xh[n][3]);
      const f32x2 a2 = __builtin_elementwise_fma(xh[n][0], xh[n][0], xh[n][1] * xh[n][1]) + __builtin_elementwise_fma(xh[n][2], xh[n][2], xh[n][3] * xh[n][3]);
      const float s1 = a1[0] + a1[1], s2 = a2[0] + a2[1];
      const float keep = (c8 & 1) ? s2 : s1, give = (c8 & 1) ? s1 : s2;
      float v = keep + f_(__builtin_amdgcn_update_dpp(0, u_(give), 0xB1, 0xf, 0xf, true));
      v = dpp_add<0x4E>(v);
      pv[n] = v + f_(__builtin_amdgcn_update_dpp(0, u_(v), 0x104, 0xf, 0xf, true));
      asm volatile("" : "+v"(pv[n])); dma_w1(n); SB();
    }
    asm volatile("" : "+v"(ev));
    {
      const u64 e1 = (((u64)__builtin_amdgcn_readfirstlane(ev[1]) << 32) | (u64)__builtin_amdgcn_readfirstlane(ev[0])) + 1ull;
      tlo = (u32)e1; thi = (u32)(e1 >> 32);
    }
    if (prod2) mod_issue(wm, cv, rwm, rc, mq, mblk, cw, lane);
    {
    const rsrc_t rt = w == 0 ? rwo : w == 1 ? rwg : w == 2 ? rwu : w == 3 ? rwd : w == 4 ? rout : rws;
    warm(Lb + L_SCRAP, rt, (u32)lane << 20, 0u);
  }

    if (c8 < 2) {
#pragma unroll
      for (int n = 0; n < 10; ++n) *(LAS float*)(L + L_LNP + (n * 8 + r8l) * 80 + (w * 2 + c8) * 4) = pv[n];
    }
    bar();
    f32x4 qa[2][4];
#pragma unroll
    for (int k = 0; k < 2; ++k)
#pragma unroll
      for (int e = 0; e < 4; ++e) qa[k][e] = *(const LAS f32x4*)(L + L_LNP + (k ? 64 + i16 : lane) * 80 + e * 16);
    dma_w1(10);
    float mus[2];
#pragma unroll
    for (int k = 0; k < 2; ++k) {
      const f32x4 a = (qa[k][0] + qa[k][1]) + (qa[k][2] + qa[k][3]);
      const float mu = (a[0] + a[2]) * (1.f / 512.f);
      const float var = fmaxf((a[1] + a[3]) * (1.f / 512.f) - mu * mu, 0.f);
      const float rs = __builtin_amdgcn_rcpf(rbf(__builtin_amdgcn_sqrtf(rbf(rbf(var) + 1e-5f))));
      *(LAS f32x2*)(L + L_RSNB + (k ? 64 + i16 : lane) * 8) = f32x2{rs, -rbf(mu) * rs};
      mus[k] = mu;
    }
    asm volatile("" ::: "memory");
    f32x2 rn[10];
#pragma unroll
    for (int n = 0; n < 10; ++n) rn[n] = *(const LAS f32x2*)(L + L_RSNB + (n * 8 + r8l) * 8);
    if (w == 0) {
#pragma unroll
      for (int k = 0; k < 2; ++k) { int r = (k ? 64 + i16 : lane) + 8 * nrot; r = r >= 80 ? r - 80 : r; ((LAS float*)(L + L_MU1 - 64))[r] = mus[k]; }
    }
#pragma unroll
    for (int n = 0; n < 10; ++n) {
#pragma unroll
      for (int e = 0; e < 4; ++e) xh[n][e] = __builtin_elementwise_fma(xh[n][e], f32x2{rn[n][0], rn[n][0]}, f32x2{rn[n][1], rn[n][1]});
      if (n < 7) { asm volatile("" : "+v"(xh[n][0]), "+v"(xh[n][1]), "+v"(xh[n][2]), "+v"(xh[n][3])); dma_w1(11 + n); SB(); }
    }
  }
#pragma unroll
  for (int n = 0; n < 10; ++n) asm volatile("" : "+v"(xh[n][0]), "+v"(xh[n][1]), "+v"(xh[n][2]), "+v"(xh[n][3]));
  STAMP(4);
  const u32 glo = tlo & 0xffu, ghi = (tlo >> 8) | (thi << 24);
  const u32 gvo = (u32)(w * 512 + tok * 16 + h * 4096);
  Gath g1_;
#pragma unroll
  for (int k = 0; k < 4; ++k) ald128_sc1<0>(g1_.q[k], rws, gvo, WS_MODG + (u32)k * 24576u);
  if (prod2) {
    asm volatile("s_waitcnt vmcnt(12)" : "+v"(wm[0]), "+v"(wm[1]), "+v"(wm[2]), "+v"(wm[3]), "+v"(cv) :: "memory");
    mod_finish(L, L_PRED, wm, cv, tlo, thi, rws, mq, mblk, cw, lane);
  }
  waitv<13>();
  bar();
  if (isC) {
    LAS char* w1rd = L + ((8 * g + (i16 >> 2)) * 128 + ((cw ^ (((i16 >> 3) & 1) | ((g & 1) << 1))) * 32) + (lane & 3) * 8);

#pragma unroll
    for (int n = 0; n < 6; ++n)
#pragma unroll
      for (int pp = 0; pp < 3; ++pp) wr[3 * n + pp] = trd2(w1rd + n * L_W1S + pp * 4096, 512);
  }
  STAMP(5);
  waitv4<0>(g1_.q[0], g1_.q[1], g1_.q[2], g1_.q[3]);
  if (!__all(gath_ok(g1_, glo, ghi))) {
    const u64 t0 = __builtin_amdgcn_s_memrealtime();
    for (;;) {
      gath_load(g1_, rws, gvo, 0u);
      if (__all(gath_ok(g1_, glo, ghi))) break;
      if (spin_out(t0)) break;
    }
  }
  {
    const u32 v2 = gath_val(g1_);
    const float a = bflo(v2), c = bfhi(v2);
    *(LAS f32x2*)(L + (h ? L_G1 : L_S1) + (w * 32 + tok) * 8) = h ? f32x2{rbf(1.f + a), rbf(1.f + c)} : f32x2{a, c};
  }
  STAMP(6);
  bar();
  {
    const int c8 = lane & 7, r8l = lane >> 3;
    const LAS f32x4* gp = (const LAS f32x4*)(L + L_G1 + (w * 64 + c8 * 8) * 4);
    const LAS f32x4* sp = (const LAS f32x4*)(L + L_S1 + (w * 64 + c8 * 8) * 4);
    const f32x4 g0 = gp[0], g1 = gp[1], s0 = sp[0], s1 = sp[1];
    LAS char* ab = L + L_AIMG + (2 * w + (c8 >> 2)) * 1024 + (c8 & 3) * 256 + ((r8l ^ c8) * 16);
#pragma unroll
    for (int n = 0; n < 10; ++n) {
      const f32x2 h0 = __builtin_elementwise_fma(xh[n][0], f32x2{g0[0], g0[1]}, f32x2{s0[0], s0[1]}), h1 = __builtin_elementwise_fma(xh[n][1], f32x2{g0[2], g0[3]}, f32x2{s0[2], s0[3]});
      const f32x2 h2 = __builtin_elementwise_fma(xh[n][2], f32x2{g1[0], g1[1]}, f32x2{s1[0], s1[1]}), h3 = __builtin_elementwise_fma(xh[n][3], f32x2{g1[2], g1[3]}, f32x2{s1[2], s1[3]});
      const u32x4 o = u32x4{pk2(h0[0], h0[1]), pk2(h1[0], h1[1]), pk2(h2[0], h2[1]), pk2(h3[0], h3[1])};
      int r8 = n + nrot; r8 = r8 >= 10 ? r8 - 10 : r8;
      *(LAS u32x4*)(ab + (r8 >> 1) * 16384 + (r8 & 1) * 128) = o;
    }
  }
  bar();
  STAMP(7);
}

template <bool isC> DI void role_body(const Params& p, LAS char* L, const int tid, const int w, const int b, u32x4 (&wr)[18], const u32 tlo, const u32 thi STAMP_PARAM) {
  const u32 Lb = (u32)(size_t)L;
  const int lane = tid & 63, cw = w & 3;
  const int xcd = b & 7, idx = b >> 3;
  const int tt = 8 * (xcd >> 1) + (idx >> 2), hgl = idx & 3, hg = 4 * (xcd & 1) + hgl;
  const int jt = 8 * (xcd & 1) + 4 * (tt & 1) + hgl;
  const int T = tt >> 1, cb = hg;
  const rsrc_t rx = mk(p.x, S_ * D_ * 2), rw1 = mk(p.wqkv, D_ * 3 * D_ * 2), rwo = mk(p.wo, D_ * D_ * 2);
  const rsrc_t rwg = mk(p.wg, D_ * F_ * 2), rwu = mk(p.wu, D_ * F_ * 2), rwd = mk(p.wd, F_ * D_ * 2);
  const rsrc_t rc = mk(p.c, D_ * 2), rwm = mk(p.wmod, D_ * 6 * D_ * 2), rout = mk(p.out, S_ * D_ * 2), rws = mk(p.ws, WS_BYTES);
  const int g = lane >> 4, i16 = lane & 15, h = lane >> 5, tok = lane & 31, t4 = tid & 255;

  const int nrot = 2 * hgl, rot = 2 * (tt & 7);
  const u32 w1v = (u32)((lane >> 3) * 3072 + (((((lane >> 1) & 3) ^ ((lane >> 4) & 1)) ^ ((cw & 1) << 1)) * 32) + (lane & 1) * 16);
  auto w1slot = [](int n) { return n < 6 ? L_RW + n * L_W1S : (n < 12 ? (n - 6) * L_W1S : L_RW + (n - 12) * L_W1S); };
  LAS char* w1rd = L + ((8 * g + (i16 >> 2)) * 128 + ((cw ^ (((i16 >> 3) & 1) | ((g & 1) << 1))) * 32) + (lane & 3) * 8);
  const u32 glo = tlo & 0xffu, ghi = (tlo >> 8) | (thi << 24);
  const int p0 = jt & 3, rotg = (4 * (T & 3) + 2 * (jt & 7)) & 15, rotd = (tt + hgl) & 3;
  const u32 wov = (u32)((lane >> 3) * 1024 + ((((lane >> 2) & 1) ^ ((lane >> 4) & 1)) * 64) + (lane & 3) * 16);
  const int mrow = (lane >> 4) & 1, q4 = i16 >> 2;
  const u32 p1base = (u32)(mrow * 4096 + (4 * h + q4) * 128 + (lane & 3) * 8), p1x = (u32)((q4 >> 1) | (mrow << 1));
  const u32 p2base = (u32)(mrow * L_P2M + (4 * h + q4) * 96 + (lane & 3) * 8);

  if constexpr (!isC) {
    const u32 gch = h ? (u32)((cw == 0 ? 1024 : cw == 1 ? 2048 : 2560) + 64 * cb + 2 * tok) : (u32)((lane & 16 ? 1536 : 2048) + 32 * T + 2 * i16);
    Gath g2_;
#pragma unroll
    for (int k = 0; k < 4; ++k) ald128_sc1<0>(g2_.q[k], rws, gch * 8u, WS_MODG + (u32)k * 24576u);
    const rsrc_t rgu = (cw & 1) ? rwu : rwg;
    const int uc = 7 * (cw >> 1) + ((lane & 7) < 7 ? (lane & 7) : 6);
    u32x4 uw[4];
    u32 uso = (u32)(T * (32 * 3584) + jt * 224); asm volatile("" : "+s"(uso));
#pragma unroll
    for (int i = 0; i < 4; ++i) ald128<0>(uw[i], rgu, (u32)((lane >> 3) * 3584 + uc * 16), uso + (u32)(8 * i * 3584));
    bar();
#pragma unroll 1
    for (int n = 0; n < 4; ++n)
#pragma unroll
      for (int pp = 0; pp < 3; ++pp) dma16(rw1, w1v, (u32)((32 * ((12 + n + rot) & 15) + 8 * cw) * 3072 + pp * 1024 + hg * 128), Lb + (u32)(L_RW + n * L_W1S + pp * 4096 + cw * 1024));
    {
      waitv4<16>(g2_.q[0], g2_.q[1], g2_.q[2], g2_.q[3]);
      if (!__all(gath_ok(g2_, glo, ghi))) {
        const u64 t0 = __builtin_amdgcn_s_memrealtime();
        for (;;) {
          gath_load(g2_, rws, gch * 8u, 0u);
          if (__all(gath_ok(g2_, glo, ghi))) break;
          if (spin_out(t0)) break;
        }
      }
      const u32 v2 = gath_val(g2_);
      const bool one = h ? cw == 1 : !(lane & 16);
      const float a = bflo(v2), c = bfhi(v2);
      const u32 dst = h ? (cw == 0 ? L_GA : cw == 1 ? L_G2C : cw == 2 ? L_GBV : L_SCRAP) + tok * 8 : L_SL + cw * 256 + tok * 8;
      *(LAS f32x2*)(L + dst) = one ? f32x2{rbf(1.f + a), rbf(1.f + c)} : f32x2{a, c};
    }
    waitv<0>(); bar();
    STAMP(8);
    {
      asm volatile("" : "+v"(uw[0]), "+v"(uw[1]), "+v"(uw[2]), "+v"(uw[3]));
      float a[16];
#pragma unroll
      for (int e = 0; e < 16; ++e) a[e] = 0.f;
#pragma unroll
      for (int i = 0; i < 4; ++i) {
        const float gk = ((const LAS float*)(L + L_SL + cw * 256))[8 * i + (lane >> 3)], sk = ((const LAS float*)(L + L_SL + cw * 256))[32 + 8 * i + (lane >> 3)];
#pragma unroll
        for (int e = 0; e < 4; ++e) {
          const float lo = bflo(uw[i][e]), hi = bfhi(uw[i][e]);
          a[2 * e] = fmaf(gk, lo, a[2 * e]); a[2 * e + 1] = fmaf(gk, hi, a[2 * e + 1]);
          a[8 + 2 * e] = fmaf(sk, lo, a[8 + 2 * e]); a[9 + 2 * e] = fmaf(sk, hi, a[9 + 2 * e]);
        }
      }
#pragma unroll
      for (int e = 0; e < 16; ++e) a[e] = dpp_add<0x128>(a[e]);
      float b8[8], ut4[4];
#pragma unroll
      for (int e = 0; e < 8; ++e) b8[e] = swap16_add(a[e], a[e + 8]);
#pragma unroll
      for (int e = 0; e < 4; ++e) ut4[e] = swap32_add(b8[e], b8[e + 4]);
      auto r0 = __builtin_amdgcn_permlane16_swap(u_(ut4[0]), u_(ut4[2]), false, false);
      auto r1 = __builtin_amdgcn_permlane16_swap(u_(ut4[1]), u_(ut4[3]), false, false);
      const u32 a0 = r0[0], b0 = r0[1], a1 = r1[0], b1 = r1[1];
      const int pr = uc >> 1, slot = pr >= p0 && pr < p0 + 4 ? pr - p0 : (pr < p0 ? 4 + pr : pr);
      if (i16 < 7) st128_sc1(u32x4{a0, b0, a1, b1}, rws, (u32)((slot * 32 + (cw & 1) * 16 + (uc & 1) * 8 + h * 4 + (g & 1) * 2) * 8), WS_UT + (u32)((jt * 16 + T) * 1792));
    }
    STAMP(9);
    bar();
#pragma unroll 1
    for (int q = 0; q < 16; ++q) { const int qq = (q + rot) & 15; dma16(rwo, wov, (u32)((128 * cw + 8 * qq) * 1024 + cb * 128), Lb + (u32)(L_WO + (16 * cw + qq) * 1024)); }
#pragma unroll
    for (int q = 0; q < 2; ++q) dma16(rx, (u32)((lane >> 3) * 1024 + (lane & 7) * 16), (u32)((64 * tt + 16 * cw + 8 * q) * 1024 + cb * 128), Lb + (u32)(L_XRES + (2 * cw + q) * 1024));
    const u32 p1v = (u32)((lane >> 3) * 3584 + ((((lane >> 1) & 3) ^ ((lane >> 4) & 1)) * 32) + (lane & 1) * 16);
    auto dma_p1 = [&](int k) {
      const u32 so = (u32)((32 * ((k + rotg) & 15) + 8 * cw) * 3584 + jt * 224 + p0 * 32);
      dma16(rwg, p1v, so, Lb + (u32)(k * L_P1B + cw * 1024));
      dma16(rwu, p1v ^ 64u, so, Lb + (u32)(k * L_P1B + 4096 + cw * 1024));
    };
    dma_p1(0); dma_p1(1);
    waitv<22>(); bar();
    if (tid == 256) st64_sc1(u32x2{tlo, thi}, rws, 0u, WS_FU + (u32)(jt * 256 + T * 8));
    waitv<4>(); STAMP(10); bar();
#pragma unroll 1
    for (int k = 2; k < 8; ++k) dma_p1(k);
    STAMP(11); bar();
#pragma unroll 1
    for (int k = 8; k < 16; ++k) dma_p1(k);
    waitv<16>(); bar();
    {
      wait_flags<16>(rws, WS_FU + (u32)(jt * 256), lane, tlo, thi);
      u32x2 pt[16];
#pragma unroll
      for (int k = 0; k < 16; ++k) ald64_sc1(pt[k], rws, (u32)(t4 < 224 ? t4 : 0) * 8u, WS_UT + (u32)((jt * 16 + k) * 1792));
      SB();
      float uu = 0.f, tt_ = 0.f;
      waitv<0>();
#pragma unroll
      for (int k = 0; k < 16; ++k) { asm volatile("" : "+v"(pt[k])); uu += f_(pt[k][0]); tt_ += f_(pt[k][1]); }
      const u32 hi = pk2(uu, tt_), lo = pk2(uu - bflo(hi), tt_ - bfhi(hi));
      const u32 u1 = hi & 0xffffu, t1 = hi >> 16, u2 = lo & 0xffffu, t2 = lo >> 16;
      const u32x4 o = u32x4{u1 | (u1 << 16), u2 | (u2 << 16), t1 | (t1 << 16), t2 | (t2 << 16)};
      if (t4 < 224) { LAS char* d = L + L_FIX + (t4 >> 5) * 1024 + (t4 & 31) * 16; *(LAS u32x4*)d = o; *(LAS u32x4*)(d + 512) = o; }
    }
    u32 p2v[3];
#pragma unroll
    for (int i = 0; i < 3; ++i) { const int n = 64 * i + lane, row = n / 6, c = n - row * 6; p2v[i] = (u32)(row * 3584 + c * 16 + ((c >> 1) >= p0 ? 128 : 0)); }
    auto dma_p2 = [&](int k0, int k1) {
      if (ELIM & 1) return;
#pragma unroll 1
      for (int k = k0; k < k1; ++k) {
        const int pos = 2 * k + (cw >> 1);
#pragma unroll
        for (int i = 0; i < 3; ++i) dma16(rgu, p2v[i], (u32)(((pos + rotg) & 15) * 32 * 3584 + jt * 224), Lb + (u32)(pos * L_P2B + (cw & 1) * L_P2M + i * 1024));
      }
    };
    auto dma_wd = [&](int u) {
      if (ELIM & 4) return;
#pragma unroll
      for (int q = 0; q < 8; ++q) {
        const int n = 8 * (u - 4) + q;
        const int jj = ((u < 4 ? u : n / 6) + rotd) & 3, r6 = n % 6;
        const int row = u < 4 ? 112 * jj + 16 * jj + 8 * q : 112 * jj + 8 * r6 + ((r6 >> 1) >= jj ? 64 : 0);
        dma16(rwd, wov, (u32)((448 * cw + row) * 1024 + cb * 128), Lb + (u32)(wd_unit_off(u) + cw * 8192 + q * 1024));
      }
    };
    waitv<0>();
    STAMP(12); bar();
    dma_p2(0, 5);
    if (b == 0 && cw == 3) {
      const u64 t0 = __builtin_amdgcn_s_memrealtime();
      for (;;) {
        bool ok = true;
#pragma unroll
        for (int k = 0; k < 4; ++k) { const int f = 64 * k + lane; const u32x2 v = ld64_sc1(rws, (u32)((f >> 3) * 1024 + (f & 7) * 8), WS_FO); ok = ok && v[0] == tlo && v[1] == thi; }
        if (__all(ok)) break;
        if (spin_out(t0)) break;
      }
      if (lane == 0) st64_sc1(u32x2{tlo, thi}, rws, 0u, WS_E);
    }
    waitv<0>(); STAMP(15); bar();
    dma_p2(5, 8);
    bar();
    dma_wd(0);
    waitv<8>(); STAMP(16); bar();
    dma_wd(1);
    bar();
    dma_wd(2);
    waitv<16>(); bar();
    dma_wd(3);
    waitv<16>(); STAMP(17); bar();
    dma_wd(4);
    waitv<16>(); bar();
    dma_wd(5);
    waitv<16>(); bar();
    dma_wd(6);
    waitv<16>(); STAMP(18); bar();
    waitv<8>(); bar();
    waitv<0>(); bar();
    bar();
    asm volatile(".Lfb_end:");
  } else {
    f32x4 m0, m1, m0f;
#pragma unroll
    for (int r = 0; r < 4; ++r) {
      const int d1 = i16 - 4 * g - r, d0 = d1 + 16;
      m0[r] = (d0 <= 14 && !(d0 & 1)) ? 0.f : -1e30f;
      m1[r] = (d1 >= 0 && d1 <= 14 && !(d1 & 1)) ? 0.f : -1e30f;
      m0f[r] = tt == 0 ? -1e30f : m0[r];
    }
    f32x4 Kt[5], Qt[4], Vn[5];
#pragma unroll
    for (int r = 0; r < 5; ++r) { Kt[r] = f32x4{0, 0, 0, 0}; Vn[r] = f32x4{0, 0, 0, 0}; if (r < 4) Qt[r] = f32x4{0, 0, 0, 0}; }
    {
      u32x4 a[2][5], wl[2][3];
      LAS char* const w1a = opq(w1rd + L_RW); LAS char* const w1b = opq(w1rd + L_RW + 4 * L_W1S);
      auto ld = [&](int n, int bi) {
        LAS char* ab = L + L_AIMG + ((n + rot) & 15) * 1024 + g * 256 + ((i16 ^ (((n & 1) << 2) | g)) * 16);
#pragma unroll
        for (int rb = 0; rb < 5; ++rb) a[bi][rb] = *(const LAS u32x4*)(ab + rb * 16384);
        if (n < 6 || n >= 12) {
#pragma unroll
          for (int pp = 0; pp < 3; ++pp) wl[bi][pp] = trd2((n % 6 < 4 ? w1a : w1b) + (n % 6 % 4) * L_W1S + pp * 4096, 512);
        }
      };
      ld(0, 0); SB();
#pragma unroll
      for (int n = 0; n < 16; ++n) {
        if (n == 3) bar();
        if (n == 8) asm volatile("" : "+v"(m0), "+v"(m1), "+v"(m0f));
        if (n == 11) WACC(24, bar());
        if (n < 15) ld(n + 1, (n + 1) & 1);
        const bool rg = n >= 6 && n < 12;
        const u32x4 wq = rg ? wr[rg ? 3 * (n - 6) : 0] : wl[n & 1][0], wk = rg ? wr[rg ? 3 * (n - 6) + 1 : 0] : wl[n & 1][1], wv = rg ? wr[rg ? 3 * (n - 6) + 2 : 0] : wl[n & 1][2];
#pragma unroll
        for (int rb = 0; rb < 5; ++rb) {
          Kt[rb] = mfma16(wk, a[n & 1][rb], Kt[rb]);
          Vn[rb] = mfma16(a[n & 1][rb], wv, Vn[rb]);
          if (rb > 0) Qt[rb - 1] = mfma16(wq, a[n & 1][rb], Qt[rb - 1]);
        }
#pragma unroll
        for (int k = 0; k < 11; ++k) { SGB_MFMA(1); SGB_DSR(1); }
        SGB_MFMA(3);
        SB();
      }
    }
    bar();
    STAMP(8);
    const int tb = cw & 1, kh = cw >> 1;
    {
      u32x2 kp[5], vp[5], qp[4];
#pragma unroll
      for (int r = 0; r < 5; ++r) { kp[r] = u32x2{pk2(Kt[r][0], Kt[r][1]), pk2(Kt[r][2], Kt[r][3])}; if (r < 4) qp[r] = u32x2{pk2(Qt[r][0], Qt[r][1]), pk2(Qt[r][2], Qt[r][3])}; }
      f32x4 s0[4], s1[4];
#pragma unroll
      for (int qb = 0; qb < 4; ++qb) { s0[qb] = mfma16k16(kp[qb], qp[qb], qb == 0 ? m0f : m0); s1[qb] = mfma16k16(kp[qb + 1], qp[qb], m1); }
#pragma unroll
      for (int r = 0; r < 5; ++r) vp[r] = u32x2{pk2(Vn[r][0], Vn[r][1]), pk2(Vn[r][2], Vn[r][3])};
      float mx[4];
#pragma unroll
      for (int qb = 0; qb < 4; ++qb) mx[qb] = mx2(mx2(mx2(s0[qb][0], s0[qb][1]), mx2(s0[qb][2], s0[qb][3])), mx2(mx2(s1[qb][0], s1[qb][1]), mx2(s1[qb][2], s1[qb][3])));
#pragma unroll
      for (int qb = 0; qb < 4; ++qb) { auto r = __builtin_amdgcn_permlane16_swap(u_(mx[qb]), u_(mx[qb]), false, false); const u32 r0 = r[0], r1 = r[1]; mx[qb] = mx2(f_(r0), f_(r1)); }
#pragma unroll
      for (int qb = 0; qb < 4; ++qb) { auto r = __builtin_amdgcn_permlane32_swap(u_(mx[qb]), u_(mx[qb]), false, false); const u32 r0 = r[0], r1 = r[1]; mx[qb] = mx2(f_(r0), f_(r1)); }
      const float cs = 0.25f * 1.44269504088896f;
      const u32x4 ones = u32x4{0x3f803f80u, 0x3f803f80u, 0x3f803f80u, 0x3f803f80u};
      const f32x4 z = f32x4{0, 0, 0, 0};
      f32x4 ot[4], sm[4];
#pragma unroll
      for (int qb = 0; qb < 4; ++qb) {
        const float nm = -mx[qb] * cs;
        float e0[4], e1[4];
#pragma unroll
        for (int r = 0; r < 4; ++r) { e0[r] = __builtin_amdgcn_exp2f(fmaf(s0[qb][r], cs, nm)); e1[r] = __builtin_amdgcn_exp2f(fmaf(s1[qb][r], cs, nm)); }
        const u32x4 pf = u32x4{pk2(e0[0], e0[1]), pk2(e0[2], e0[3]), pk2(e1[0], e1[1]), pk2(e1[2], e1[3])};
        sm[qb] = mfma16(ones, pf, z);
        ot[qb] = mfma16(u32x4{vp[qb][0], vp[qb][1], vp[qb + 1][0], vp[qb + 1][1]}, pf, z);
      }
      u32x2 op[4];
#pragma unroll
      for (int qb = 0; qb < 4; ++qb) {
        const float ri = __builtin_amdgcn_rcpf(sm[qb][0]);
        op[qb] = u32x2{pk2(ot[qb][0] * ri, ot[qb][1] * ri), pk2(ot[qb][2] * ri, ot[qb][3] * ri)};
      }
#pragma unroll
      for (int t2 = 0; t2 < 2; ++t2) {
        auto r0 = __builtin_amdgcn_permlane16_swap(op[2 * t2][0], op[2 * t2 + 1][0], false, false);
        auto r1 = __builtin_amdgcn_permlane16_swap(op[2 * t2][1], op[2 * t2 + 1][1], false, false);
        const u32 a0 = r0[0], b0 = r0[1], a1 = r1[0], b1 = r1[1];
        st128_sc1(u32x4{a0, a1, b0, b1}, rws, (u32)(lane * 16), WS_O + (u32)(((2 * tt + t2) * 32 + 4 * hg + cw) * 1024));
      }
    }
    STAMP(9);
    pubbar<0>(8, rws, WS_FO + (u32)(tt * 1024 + hg * 8), tlo, thi, tid);
    STAMP(10);
    wait_flags<8>(rws, WS_FO + (u32)(tt * 1024 + cb * 128), lane, tlo, thi);
    STAMP(11);
    u32 x1p[8];
    {
      u32x4 of[16];
      const u32 ob = WS_O + (u32)(((2 * tt + tb) * 32 + 16 * kh) * 1024);
#pragma unroll
      for (int s = 0; s < 16; ++s) { of[s] = ld128_sc1(rws, (u32)(lane * 16), ob + (u32)(((s + 4 * hgl) & 15) * 1024)); SB(); }
      SB();
      WACC(24, bar());
      LAS char* q0 = L + (L_WO + (256 * kh + 8 * h + q4) * 128 + mrow * 32 + (lane & 3) * 8);
      LAS char* qa = q0 + ((q4 >> 1) ^ kh) * 64; LAS char* qb = q0 + ((q4 >> 1) ^ kh ^ 1) * 64;
      f32x16 a0, a1;
#pragma unroll
      for (int r = 0; r < 16; ++r) { a0[r] = 0.f; a1[r] = 0.f; }
      u32x4 wl[2][2];
      wl[0][0] = trd2(qa + hgl * 8192, 512); wl[0][1] = trd2(qb + hgl * 8192, 512); SB();
#pragma unroll
      for (int s = 0; s < 16; ++s) {
        if (s < 15) { const int o2 = ((((s + 1) >> 2) + hgl) & 3) * 8192 + ((s + 1) & 3) * 2048; wl[(s + 1) & 1][0] = trd2(qa + o2, 512); wl[(s + 1) & 1][1] = trd2(qb + o2, 512); }
        a0 = mfma32(wl[s & 1][0], of[s], a0); a1 = mfma32(wl[s & 1][1], of[s], a1);
        SGB_MFMA(1); SGB_DSR(2); SGB_MFMA(1); SGB_DSR(2);
        SB();
      }
      u32x2 xres[4]; f32x4 ga4[4], g24[4];
#pragma unroll
      for (int q = 0; q < 4; ++q) {
        xres[q] = *(const LAS u32x2*)(L + L_XRES + (32 * tb + tok) * 128 + (32 * kh + 8 * q + 4 * h) * 2);
        ga4[q] = *(const LAS f32x4*)(L + L_GA + (32 * kh + 8 * q + 4 * h) * 4); g24[q] = *(const LAS f32x4*)(L + L_G2C + (32 * kh + 8 * q + 4 * h) * 4);
      }
      const float mu1 = ((const LAS float*)(L + L_MU1))[32 * tb + tok];
#pragma unroll
      for (int q = 0; q < 4; ++q) *(LAS f32x4*)(L + L_RED2 + cw * 4096 + q * 1024 + lane * 16) = f32x4{a1[4 * q], a1[4 * q + 1], a1[4 * q + 2], a1[4 * q + 3]};
      f32x2 xa[8];
#pragma unroll
      for (int q = 0; q < 4; ++q) {
        xa[2 * q] = __builtin_elementwise_fma(f32x2{ga4[q][0], ga4[q][1]}, f32x2{a0[4 * q], a0[4 * q + 1]}, f32x2{bflo(xres[q][0]), bfhi(xres[q][0])});
        xa[2 * q + 1] = __builtin_elementwise_fma(f32x2{ga4[q][2], ga4[q][3]}, f32x2{a0[4 * q + 2], a0[4 * q + 3]}, f32x2{bflo(xres[q][1]), bfhi(xres[q][1])});
      }
#pragma unroll
      for (int q = 0; q < 8; q += 4) asm volatile("" : "+v"(xa[q]), "+v"(xa[q + 1]), "+v"(xa[q + 2]), "+v"(xa[q + 3]));
      STAMP(19);
      bar();
      STAMP(20);
      f32x2 sv1 = f32x2{0.f, 0.f}, sv2 = f32x2{0.f, 0.f}; u32 yp[8];
#pragma unroll
      for (int q = 0; q < 4; ++q) {
        const f32x4 o4 = *(const LAS f32x4*)(L + L_RED2 + (cw ^ 2) * 4096 + q * 1024 + lane * 16);
#pragma unroll
        for (int e = 0; e < 2; ++e) {
          const f32x2 v = __builtin_elementwise_fma(f32x2{ga4[q][2 * e], ga4[q][2 * e + 1]}, f32x2{o4[2 * e], o4[2 * e + 1]}, xa[2 * q + e]);
          x1p[2 * q + e] = pk2(v[0], v[1]);
          const f32x2 d = f32x2{bflo(x1p[2 * q + e]), bfhi(x1p[2 * q + e])} - f32x2{mu1, mu1};
          sv1 += d; sv2 = __builtin_elementwise_fma(d, d, sv2);
          const f32x2 y = d * f32x2{g24[q][2 * e], g24[q][2 * e + 1]};
          yp[2 * q + e] = pk2(y[0], y[1]);
        }
      }
      float s1 = sv1[0] + sv1[1], s2 = sv2[0] + sv2[1];
      const u32 yb = WS_Y + (u32)(((2 * tt + tb) * 32 + 4 * cb + 2 * kh) * 1024);
      st128_sc1(u32x4{yp[0], yp[1], yp[2], yp[3]}, rws, (u32)(lane * 16), yb);
      st128_sc1(u32x4{yp[4], yp[5], yp[6], yp[7]}, rws, (u32)(lane * 16), yb + 1024);
      s1 = swap32_add(s1, s1); s2 = swap32_add(s2, s2);
      if (lane < 32 && cb == 0 && kh == 0) __builtin_amdgcn_raw_buffer_store_b32(u_(mu1), rws, (u32)(tok * 4), WS_MU + (u32)((2 * tt + tb) * 128), 16);
      if (lane < 32) st64_sc1(u32x2{u_(s1), u_(s2)}, rws, (u32)(tok * 8), WS_ST + (u32)(((2 * tt + tb) * 16 + 2 * cb + kh) * 256));
    }
#pragma unroll
    for (int q = 0; q < 8; q += 4) asm volatile("" : "+v"(x1p[q]), "+v"(x1p[q + 1]), "+v"(x1p[q + 2]), "+v"(x1p[q + 3]));
    STAMP(12);
    pubbar<0>(16, rws, WS_FY + (u32)(tt * 2048 + cb * 8), tlo, thi, tid);
    STAMP(13);
    wait_flags<8>(rws, WS_FY + (u32)((2 * T + (cw >> 1)) * 2048 + jt * 128), lane, tlo, thi);
    STAMP(14);
    const int tb32 = 4 * T + cw;
    u32x4 yf[32];
    auto ldy = [&](int s) { yf[s] = ld128_sc1(rws, (u32)(lane * 16), WS_Y + (u32)((tb32 * 32 + ((s + 2 * rotg) & 31)) * 1024)); };
#pragma unroll
    for (int s = 0; s < (EXPW ? 32 : 10); ++s) { ldy(s); SB(); }
    const u32 stv8 = __builtin_amdgcn_raw_buffer_load_b32(rws, (u32)(tok * 4), WS_MU + (u32)(tb32 * 128), 16);
    u32x2 stv[8];
#pragma unroll
    for (int q = 0; q < 8; ++q) stv[q] = ld64_sc1(rws, (u32)(tok * 8 + h * 2048), WS_ST + (u32)((tb32 * 16 + q) * 256));
    SB();
    u32 mz, sz; float r2;
    auto act_store = [&](const f32x16& ac, int pp) {
      const float r1 = __builtin_amdgcn_sqrtf(r2);
      u32x4 o;
#pragma unroll
      for (int e = 0; e < 4; ++e) {
        const f32x2 gsc = f32x2{ac[2 * e], ac[2 * e + 1]} * f32x2{r2, r2};
        const f32x2 av = f32x2{fmaxf(gsc[0], 0.f), fmaxf(gsc[1], 0.f)} * f32x2{ac[8 + 2 * e], ac[9 + 2 * e]};
        o[e] = pk2(av[0], av[1]);
      }
      st128_sc1(o, rws, (u32)(lane * 16), WS_ACT + (u32)((tb32 * 112 + 7 * jt + pp) * 1024));
    };
    {
      f32x16 acc[4];
#pragma unroll
      for (int q = 0; q < 4; ++q)
#pragma unroll
        for (int r = 0; r < 16; ++r) acc[q][r] = 0.f;
      LAS char* pa[4];
#pragma unroll
      for (int pp = 0; pp < 4; ++pp) pa[pp] = L + (p1base + (((u32)pp ^ p1x) * 32));
      u32x4 wf[2][4], fx, fxp;
      auto ldw = [&](int st, int bi) {
#pragma unroll
        for (int pp = 0; pp < 4; ++pp) { if (st == 16) pa[pp] = opq(pa[pp] + 8 * L_P1B); wf[bi][pp] = trd2(pa[pp] + ((st >> 1) & 7) * L_P1B + (st & 1) * 2048, 1024); }
      };
      ldw(0, 0); SB();
#pragma unroll
      for (int st = 0; st < 32; ++st) {
        if (st == 15) WACC(25, bar());
        if (st == 12) {
      float s1 = 0.f, s2 = 0.f;
#pragma unroll
      for (int q = 0; q < 8; ++q) { s1 += f_(stv[q][0]); s2 += f_(stv[q][1]); }
      s1 = swap32_add(s1, s1); s2 = swap32_add(s2, s2);
      const float md0 = s1 * (1.f / 512.f), mu1t = f_(stv8);
      const float sg = rbf(__builtin_amdgcn_sqrtf(rbf(rbf(fmaxf(s2 * (1.f / 512.f) - md0 * md0, 0.f)) + 1e-5f)));
      const float md = rbf(md0 + mu1t) - mu1t;
      r2 = __builtin_amdgcn_rcpf(sg * sg);
      const u32 hi = pk2(-md, sg), lo = pk2(-md - bflo(hi), sg - bfhi(hi));
      const u32 mm = (hi & 0xffffu) | (lo << 16), ss = (hi >> 16) | (lo & 0xffff0000u);
      mz = h ? 0u : mm; sz = h ? 0u : ss;
      asm volatile("" : "+v"(mz), "+v"(sz), "+v"(r2));
    }
        if (st + 1 < 32) ldw(st + 1, (st + 1) & 1);
        if (OPT & 2) { if (2 * st + 10 < 32) { ldy(2 * st + 10); SB(); ldy(2 * st + 11); } }
        else if (!EXPW && st + 10 < 32) ldy(st + 10);
#pragma unroll
        for (int pp = 0; pp < 4; ++pp) acc[pp] = mfma32(wf[st & 1][pp], yf[st], acc[pp]);
#pragma unroll
        for (int pp = 0; pp < 4; ++pp) { SGB_MFMA(1); SGB_DSR(2); }
        SB();
        if (st >= 24 && st < 28) fx = *(const LAS u32x4*)(L + L_FIX + (st - 24) * 1024 + lane * 16), SB();
        if (st >= 25 && st < 29) acc[st - 25 < 4 && st >= 25 ? st - 25 : 0] = mfma32(fxp, u32x4{mz, mz, sz, sz}, acc[st - 25 < 4 && st >= 25 ? st - 25 : 0]), SB();
        fxp = fx;
      }
      STAMP(15);
#pragma unroll
      for (int pp = 0; pp < 4; ++pp) act_store(acc[pp], p0 + pp);
      WACC(26, bar());
    }
    u32x4 af[16][2]; u32x2 fl;
    auto lda = [&](int n) { const int s = n >> 1, t2 = n & 1; af[s][t2] = ld128_sc1(rws, (u32)(lane * 16), WS_ACT + (u32)(((2 * tt + (t2 ^ tb)) * 112 + 28 * cw + 8 * (((s >> 2) + rotd) & 3) + (s & 3)) * 1024)); };
    {
      f32x16 acc[3], z16;
#pragma unroll
      for (int r = 0; r < 16; ++r) z16[r] = 0.f;
#pragma unroll
      for (int pp = 0; pp < 3; ++pp) acc[pp] = mfma32(*(const LAS u32x4*)(L + L_FIX + (4 + pp) * 1024 + lane * 16), u32x4{mz, mz, sz, sz}, z16);
      LAS char* pb = L + p2base;
      u32x4 wf[2][3];
      auto ldw = [&](int st, int bi) {
        if (st == 16) pb = opq(pb + 8 * L_P2B);
#pragma unroll
        for (int pp = 0; pp < 3; ++pp) wf[bi][pp] = trd2(pb + ((st >> 1) & 7) * L_P2B + (st & 1) * 1536 + pp * 32, 768);
      };
      ldw(0, 0); SB();
#pragma unroll
      for (int st = 0; st < 32; ++st) {
        if (st == S0P) WACC(30, pubbar<0>(16, rws, WS_FA + (u32)(T * 2048 + jt * 8), tlo, thi, tid));
        if (st == 15 || st == 23) WACC(27, bar());
        if (st == FA1P) fl = peek_flags<4>(rws, WS_FA + (u32)(T * 2048 + ((tt & 1) * 8 + cb) * 128 + cw * 32), lane);
        if (st == FA1P + 4) { STAMP(23); WACC(28, wait_flags_from<4>(fl, rws, WS_FA + (u32)(T * 2048 + ((tt & 1) * 8 + cb) * 128 + cw * 32), lane, tlo, thi)); }
        if (st + 1 < 32) ldw(st + 1, (st + 1) & 1);
        if (st >= FA1P + 4) { const int k = st - FA1P - 4; if (A1R == 1) lda(k); else if (A1R == 2) { lda(2 * k); SB(); lda(2 * k + 1); } else { lda(3 * (k >> 1) + (k & 1) * 2); if (!(k & 1)) { SB(); lda(3 * (k >> 1) + 1); } } }
#pragma unroll
        for (int pp = 0; pp < 3; ++pp) acc[pp] = mfma32(wf[st & 1][pp], yf[st], acc[pp]);
#pragma unroll
        for (int pp = 0; pp < 3; ++pp) { SGB_MFMA(1); SGB_DSR(2); }
        SB();
      }
      WACC(27, bar());
      STAMP(16);
#pragma unroll
      for (int pp = 0; pp < 3; ++pp) act_store(acc[pp], pp >= p0 ? pp + 4 : pp);
#pragma unroll
      for (int n = A1N; n < 24; ++n) { lda(n); SB(); }
    }
    SB();
    {
      f32x16 acc[4];
#pragma unroll
      for (int q = 0; q < 4; ++q)
#pragma unroll
        for (int r = 0; r < 16; ++r) acc[q][r] = 0.f;
      LAS char* d0 = L + (cw * 8192 + (4 * h + q4) * 128 + mrow * 32 + (lane & 3) * 8);
      LAS char* da = d0 + ((q4 >> 1) ^ kh) * 64; LAS char* db = d0 + ((q4 >> 1) ^ kh ^ 1) * 64;
      u32x4 wl[2][2], a2[12][2];
      auto lda2 = [&](int n) {
        const int s2 = n >> 1, t2 = n & 1, jj = (s2 / 3 + rotd) & 3, pp = s2 % 3;
        a2[s2][t2] = ld128_sc1(rws, (u32)(lane * 16), WS_ACT + (u32)(((2 * tt + (t2 ^ tb)) * 112 + 28 * cw + 7 * jj + (pp >= jj ? pp + 4 : pp)) * 1024));
      };
      LAS char* const dah = opq(da + 65536); LAS char* const dbh = opq(db + 65536);
      auto ldw = [&](int s, int bi) { const u32 o = wd_unit_off(s >> 2) + (s & 3) * 2048; wl[bi][0] = trd2(o < 65536 ? da + o : dah + (o - 65536), 1024); wl[bi][1] = trd2(o < 65536 ? db + o : dbh + (o - 65536), 1024); };
      ldw(0, 0); SB();
#pragma unroll
      for (int s = 0; s < 28; ++s) {
        if (s == 3) WACC(30, pubbar<(A1N < 24 ? 24 - A1N : 0) + (A1N <= 24 ? 3 : (A1N < 27 ? 27 - A1N : 0))>(16, rws, WS_FA2 + (u32)(T * 2048 + jt * 8), tlo, thi, tid));
        if ((s & 3) == 3 && s > 3 && s < 24) WACC(29, bar());
        if (!D0 && s == 12) {
          STAMP(17);
          wait_flags_from<4>(fl, rws, WS_FA2 + (u32)(T * 2048 + ((tt & 1) * 8 + cb) * 128 + cw * 32), lane, tlo, thi);
          STAMP(21);
#pragma unroll
          for (int n = 0; n < 12; ++n) { lda2(n); SB(); }
          SB();
        }
        if (s < 8 && 24 + s >= A1N) lda(24 + s);
        if (!D0 && s == 8) fl = peek_flags<4>(rws, WS_FA2 + (u32)(T * 2048 + ((tt & 1) * 8 + cb) * 128 + cw * 32), lane);
        if (!D0 && s >= 13 && s < 19) { lda2(12 + 2 * (s - 13)); SB(); lda2(13 + 2 * (s - 13)); }
        if (s + 1 < 28) ldw(s + 1, (s + 1) & 1);
        const u32x4 b0 = s < 16 ? af[s < 16 ? s : 0][0] : a2[s < 16 ? 0 : s - 16][0], b1 = s < 16 ? af[s < 16 ? s : 0][1] : a2[s < 16 ? 0 : s - 16][1];
        acc[0] = mfma32(wl[s & 1][0], b0, acc[0]); acc[2] = mfma32(wl[s & 1][1], b0, acc[2]);
        acc[1] = mfma32(wl[s & 1][0], b1, acc[1]); acc[3] = mfma32(wl[s & 1][1], b1, acc[3]);
        SGB_MFMA(1); SGB_DSR(2); SGB_MFMA(1); SGB_DSR(2); SGB_MFMA(2);
        SB();
      }
      STAMP(22);
      int ln2 = lane; asm volatile("" : "+v"(ln2));
#pragma unroll
      for (int i = 1; i < 4; ++i)
#pragma unroll
        for (int q = 0; q < 4; ++q) *(LAS f32x4*)(L + L_RED4 + ((i ^ cw) * 3 + i - 1) * 4096 + q * 1024 + ln2 * 16) = f32x4{acc[i][4 * q], acc[i][4 * q + 1], acc[i][4 * q + 2], acc[i][4 * q + 3]};
      f32x4 gb4[4]; f32x2 xa[8];
#pragma unroll
      for (int q = 0; q < 4; ++q) {
        gb4[q] = *(const LAS f32x4*)(L + L_GBV + (32 * kh + 8 * q + 4 * (ln2 >> 5)) * 4);
#pragma unroll
        for (int e = 0; e < 2; ++e)
          xa[2 * q + e] = __builtin_elementwise_fma(f32x2{gb4[q][2 * e], gb4[q][2 * e + 1]}, f32x2{acc[0][4 * q + 2 * e], acc[0][4 * q + 2 * e + 1]}, f32x2{bflo(x1p[2 * q + e]), bfhi(x1p[2 * q + e])});
      }
#pragma unroll
      for (int q = 0; q < 8; q += 4) asm volatile("" : "+v"(xa[q]), "+v"(xa[q + 1]), "+v"(xa[q + 2]), "+v"(xa[q + 3]));
      bar();
      u32 ob[8];
#pragma unroll
      for (int q = 0; q < 4; ++q) {
        const LAS char* rp = L + L_RED4 + cw * 3 * 4096 + q * 1024 + ln2 * 16;
        const f32x4 rs = (*(const LAS f32x4*)rp + *(const LAS f32x4*)(rp + 4096)) + *(const LAS f32x4*)(rp + 8192);
#pragma unroll
        for (int e = 0; e < 2; ++e) {
          const f32x2 v = __builtin_elementwise_fma(f32x2{gb4[q][2 * e], gb4[q][2 * e + 1]}, f32x2{rs[2 * e], rs[2 * e + 1]}, xa[2 * q + e]);
          ob[2 * q + e] = pk2(v[0], v[1]);
        }
      }
#pragma unroll
      for (int m = 0; m < 2; ++m) {
        auto r0 = __builtin_amdgcn_permlane32_swap(ob[4 * m], ob[4 * m + 2], false, false);
        auto r1 = __builtin_amdgcn_permlane32_swap(ob[4 * m + 1], ob[4 * m + 3], false, false);
        const u32 a0 = r0[0], b0 = r0[1], a1 = r1[0], b1 = r1[1];
        __builtin_amdgcn_raw_buffer_store_b128(u32x4{a0, a1, b0, b1}, rout, (u32)((64 * tt + 32 * tb + (ln2 & 31)) * 1024 + (64 * cb + 32 * kh + 16 * m + 8 * (ln2 >> 5)) * 2), 0, 0);
      }
    }
    STAMP(18);
  }
}

__global__ __launch_bounds__(512, 2) void fused_block(Params p) {
  extern __shared__ __attribute__((aligned(16))) char smem_[];
  asm volatile("" :: "s"(p.x), "s"(p.wqkv), "s"(p.wo), "s"(p.wg), "s"(p.wu), "s"(p.wd), "s"(p.c), "s"(p.wmod), "s"(p.out), "s"(p.ws));
  int w = __builtin_amdgcn_readfirstlane(threadIdx.x >> 6);
  u32x4 wr[18]; u32 tlo, thi;
  front(p, (LAS char*)smem_, threadIdx.x, w, blockIdx.x, wr, tlo, thi STAMP_ARG);
  if (w >= 4) {
    int b = blockIdx.x, tid = threadIdx.x; asm volatile("; H role" : "+s"(b), "+s"(w)); asm volatile("; H role" : "+v"(tid));
    role_body<false>(p, (LAS char*)smem_, tid, w, b, wr, tlo, thi STAMP_ARG);
  } else {
    int b = blockIdx.x, tid = threadIdx.x; asm volatile("; C role" : "+s"(b), "+s"(w)); asm volatile("; C role" : "+v"(tid));
    role_body<true>(p, (LAS char*)smem_, tid, w, b, wr, tlo, thi STAMP_ARG);
  }
}

extern "C" void kernel_launch(void* const* d_in, const int* in_sizes, int n_in, void* d_out, int out_size, void* d_ws, size_t ws_size, hipStream_t stream) {
  Params p;
  p.x = d_in[0]; p.wqkv = d_in[1]; p.wo = d_in[2]; p.wg = d_in[4]; p.wu = d_in[5]; p.wd = d_in[6]; p.c = d_in[7]; p.wmod = d_in[8];
  p.out = d_out; p.ws = d_ws;
  (void)hipFuncSetAttribute((const void*)fused_block, hipFuncAttributeMaxDynamicSharedMemorySize, LDS_BYTES);
  fused_block<<<256, 512, LDS_BYTES, stream>>>(p);
}
```

```cpp
#define DIAG 1
#define LITE 1
#define ENC 0
#define ENCB 100
#include <hip/hip_runtime.h>
#include <stdint.h>

typedef unsigned short u16;
typedef unsigned u32;
typedef unsigned long long u64;
typedef u32 u32x2 __attribute__((ext_vector_type(2)));
typedef u32 u32x4 __attribute__((ext_vector_type(4)));
typedef short s16x4 __attribute__((ext_vector_type(4)));
typedef short s16x8 __attribute__((ext_vector_type(8)));
typedef __bf16 bf16x2 __attribute__((ext_vector_type(2)));
typedef __bf16 bf16x8 __attribute__((ext_vector_type(8)));
typedef float f32x2 __attribute__((ext_vector_type(2)));
typedef float f32x4 __attribute__((ext_vector_type(4)));
typedef float f32x16 __attribute__((ext_vector_type(16)));
typedef __amdgpu_buffer_rsrc_t rsrc_t;
#define LAS __attribute__((address_space(3)))
#define DI __device__ __forceinline__

#define EXPW 0
#define TOUCH 6
#define THR 4
constexpr unsigned CODE_BYTES = 40000;
#define NTX 0
#define FA1P 16
#define OPT 0
#define ELIM 0
#define S0P 3
#define A1R 1
#define A1N (A1R == 1 ? 28 - FA1P : A1R == 2 ? 2 * (28 - FA1P) : 3 * (28 - FA1P) / 2)
#define W1T 0
#define D0 0
#define A2C 9
#define MIM 16
#define MALLT 0
#define FIXP 1
#define XMAP 2

constexpr int S_ = 2048, D_ = 512, F_ = 1792;
constexpr u32 WS_E = 0, WS_MODG = 393216, WS_FU = 57344, WS_FO = 8192, WS_FY = 983040, WS_FA = 327680, WS_FA2 = 360448, WS_DBG = 65536;
constexpr u32 WS_MU = 499712;
constexpr u32 WS_UT = 524288;
constexpr u32 WS_O = 1u << 20, WS_Y = 3u << 20, WS_ST = 5u << 20, WS_ACT = 6u << 20, WS_BYTES = 13u << 20;
constexpr u32 L_AIMG = 0, L_RW = 81920, L_W1S = 12288;
constexpr u32 L_LNP = 73728, L_PRED = L_LNP + 6400, L_RSNB = L_PRED + 1088;
constexpr u32 L_XRES = 65536, L_WO = 73728, L_RED2 = 139264;
constexpr u32 L_P1B = 8192, L_P2B = 6400, L_P2M = 3200;
constexpr u32 L_RED4 = 65536;
constexpr u32 L_MISC = 155648, L_G1 = L_MISC, L_S1 = L_MISC + 2048, L_FIX = L_MISC;
constexpr u32 L_SL = L_MISC + 4096, L_SCRAP = L_MISC + 6656;
constexpr u32 L_MU1 = L_MISC + 7168, L_GA = L_MU1 + 256, L_G2C = L_GA + 256, L_GBV = L_G2C + 256;
constexpr u32 LDS_BYTES = 163840;
__host__ __device__ constexpr u32 wd_unit_off(int u) { return u == 0 || u == 4 ? 102400u : (u == 1 || u == 5 ? 0u : (u == 2 || u == 6 ? 32768u : 65536u)); }

template <int N> struct ic { static constexpr int value = N; };
struct Params { const void *x, *wqkv, *wo, *wg, *wu, *wd, *c, *wmod; void *out; void *ws; };

DI rsrc_t mk(const void* p, u32 bytes) { return __builtin_amdgcn_make_buffer_rsrc((void*)p, 0, bytes, 0x00020000); }
DI float bflo(u32 v) { return __builtin_bit_cast(float, v << 16); }
DI float bfhi(u32 v) { return __builtin_bit_cast(float, v & 0xffff0000u); }
DI u32 pk2(float a, float b) { bf16x2 r; r[0] = (__bf16)a; r[1] = (__bf16)b; return __builtin_bit_cast(u32, r); }
DI float rbf(float a) { return bflo(pk2(a, a)); }
DI f32x4 mfma16(u32x4 a, u32x4 b, f32x4 c) { return __builtin_amdgcn_mfma_f32_16x16x32_bf16(__builtin_bit_cast(bf16x8, a), __builtin_bit_cast(bf16x8, b), c, 0, 0, 0); }
DI f32x4 mfma16k16(u32x2 a, u32x2 b, f32x4 c) { return __builtin_amdgcn_mfma_f32_16x16x16bf16_1k(__builtin_bit_cast(s16x4, a), __builtin_bit_cast(s16x4, b), c, 0, 0, 0); }
DI f32x16 mfma32(u32x4 a, u32x4 b, f32x16 c) { return __builtin_amdgcn_mfma_f32_32x32x16_bf16(__builtin_bit_cast(bf16x8, a), __builtin_bit_cast(bf16x8, b), c, 0, 0, 0); }
DI u32x2 trd(LAS char* p) { return __builtin_bit_cast(u32x2, __builtin_amdgcn_ds_read_tr16_b64_v4i16((LAS s16x4*)p)); }
DI u32x4 trd2(LAS char* p, int d) { u32x2 a = trd(p), b = trd(p + d); return u32x4{a[0], a[1], b[0], b[1]}; }
DI LAS char* opq(LAS char* p) { asm volatile("" : "+v"(p)); return p; }
DI void dma16(rsrc_t rs, u32 voff, u32 soff, u32 lds) {
  asm volatile("s_mov_b32 m0, %0\n\ts_nop 0\n\tbuffer_load_dwordx4 %1, %2, %3 offen lds" :: "s"(lds), "v"(voff), "s"(rs), "s"(soff) : "m0", "memory");
}
template <int OFF> DI void ald128(u32x4& d, rsrc_t rs, u32 voff, u32 soff) {
  asm volatile("buffer_load_dwordx4 %0, %1, %2, %3 offen offset:%c4" : "=v"(d) : "v"(voff), "s"(rs), "s"(soff), "i"(OFF) : "memory");
}
DI void ald128_nt(u32x4& d, rsrc_t rs, u32 voff, u32 soff) {
  asm volatile("buffer_load_dwordx4 %0, %1, %2, %3 offen" : "=v"(d) : "v"(voff), "s"(rs), "s"(soff) : "memory");
}
template <int OFF> DI void ald128_sc1(u32x4& d, rsrc_t rs, u32 voff, u32 soff) {
  asm volatile("buffer_load_dwordx4 %0, %1, %2, %3 offen offset:%c4 sc1" : "=v"(d) : "v"(voff), "s"(rs), "s"(soff), "i"(OFF) : "memory");
}
DI void ald64_sc1(u32x2& d, rsrc_t rs, u32 voff, u32 soff) {
  asm volatile("buffer_load_dwordx2 %0, %1, %2, %3 offen sc1" : "=v"(d) : "v"(voff), "s"(rs), "s"(soff) : "memory");
}
DI void ald64(u32x2& d, rsrc_t rs, u32 voff, u32 soff) {
  asm volatile("buffer_load_dwordx2 %0, %1, %2, %3 offen" : "=v"(d) : "v"(voff), "s"(rs), "s"(soff) : "memory");
}
DI void warm(u32 lds, rsrc_t rs, u32 voff, u32 soff) {
  asm volatile("s_mov_b32 m0, %0\n\ts_nop 0\n\tbuffer_load_dword %1, %2, %3 offen lds" :: "s"(lds), "v"(voff), "s"(rs), "s"(soff) : "m0", "memory");
}
template <int N> DI void waitv() { asm volatile("s_waitcnt vmcnt(%c0)" :: "i"(N) : "memory"); }
template <int N> DI void waitv4(u32x4& a, u32x4& b, u32x4& c, u32x4& d) { asm volatile("s_waitcnt vmcnt(%c4)" : "+v"(a), "+v"(b), "+v"(c), "+v"(d) : "i"(N) : "memory"); }
template <int N> DI void waitv1(u32x4& a) { asm volatile("s_waitcnt vmcnt(%c1)" : "+v"(a) : "i"(N) : "memory"); }
DI void bar() { __builtin_amdgcn_sched_barrier(0); asm volatile("s_waitcnt lgkmcnt(0)\n\ts_barrier" ::: "memory"); __builtin_amdgcn_sched_barrier(0); }
DI u32x4 ld128_sc1(rsrc_t rs, u32 voff, u32 soff) { return __builtin_amdgcn_raw_buffer_load_b128(rs, voff, soff, 16); }
DI u32x2 ld64_sc1(rsrc_t rs, u32 voff, u32 soff) { return __builtin_amdgcn_raw_buffer_load_b64(rs, voff, soff, 16); }
DI void st128_sc1(u32x4 v, rsrc_t rs, u32 voff, u32 soff) { __builtin_amdgcn_raw_buffer_store_b128(v, rs, voff, soff, 16); }
DI void st64_sc1(u32x2 v, rsrc_t rs, u32 voff, u32 soff) { __builtin_amdgcn_raw_buffer_store_b64(v, rs, voff, soff, 16); }
DI float f_(u32 v) { return __builtin_bit_cast(float, v); }
DI u32 u_(float v) { return __builtin_bit_cast(u32, v); }
DI float swap16_add(float a, float b) { auto r = __builtin_amdgcn_permlane16_swap(u_(a), u_(b), false, false); u32 r0 = r[0], r1 = r[1]; return f_(r0) + f_(r1); }
DI float swap32_add(float a, float b) { auto r = __builtin_amdgcn_permlane32_swap(u_(a), u_(b), false, false); u32 r0 = r[0], r1 = r[1]; return f_(r0) + f_(r1); }
DI float swap16_max(float a) { auto r = __builtin_amdgcn_permlane16_swap(u_(a), u_(a), false, false); u32 r0 = r[0], r1 = r[1]; return fmaxf(f_(r0), f_(r1)); }
DI float swap32_max(float a) { auto r = __builtin_amdgcn_permlane32_swap(u_(a), u_(a), false, false); u32 r0 = r[0], r1 = r[1]; return fmaxf(f_(r0), f_(r1)); }
DI float mx2(float a, float b) { return __builtin_amdgcn_fmed3f(a, b, __builtin_inff()); }
template <int CTRL> DI float dpp_add(float a) { return a + f_(__builtin_amdgcn_update_dpp(0, u_(a), CTRL, 0xf, 0xf, true)); }
DI float dot2(u32 a, u32 b, float c) { return __builtin_amdgcn_fdot2_f32_bf16(__builtin_bit_cast(bf16x2, a), __builtin_bit_cast(bf16x2, b), c, false); }

constexpr u64 SPIN_TICKS = 20000;
DI bool spin_out(u64 t0) { __builtin_amdgcn_s_sleep(1); return (__builtin_amdgcn_s_memrealtime() - t0) > SPIN_TICKS; }

#define SB() __builtin_amdgcn_sched_barrier(0)
#define SGB_MFMA(n) __builtin_amdgcn_sched_group_barrier(0x8, n, 0)
#define SGB_DSR(n) __builtin_amdgcn_sched_group_barrier(0x100, n, 0)
#define SGB_DSW(n) __builtin_amdgcn_sched_group_barrier(0x200, n, 0)
#define STAMP(i) do { if ((i) != 0 && (i) != ENC) break; __builtin_amdgcn_sched_barrier(0); u64 t_; asm volatile("s_memrealtime %0\n\ts_waitcnt lgkmcnt(0)" : "=s"(t_) :: "memory"); stamp[i] = (u32)t_; __builtin_amdgcn_sched_barrier(0); } while (0)
#define WACC(i, code) do { code; } while (0)

template <int YOUNGER> DI void pubbar(int NC, rsrc_t rws, u32 flag_off, u32 tlo, u32 thi, int tid) {
  waitv<YOUNGER>(); bar();
  if (tid < NC) st64_sc1(u32x2{tlo, thi}, rws, (u32)tid * 128u, flag_off);
}
template <int NF> DI u32x2 peek_flags(rsrc_t rws, u32 off, int lane) { return ld64_sc1(rws, (u32)(lane & (NF - 1)) * 8u, off); }
template <int NF> DI void wait_flags(rsrc_t rws, u32 off, int lane, u32 tlo, u32 thi) {
  const u32 vo = (u32)(lane & (NF - 1)) * 8u;
  const u64 t0 = __builtin_amdgcn_s_memrealtime();
  for (;;) {
    u32x2 f = ld64_sc1(rws, vo, off);
    if (__all(f[0] == tlo && f[1] == thi)) break;
    if (spin_out(t0)) break;
  }
  __builtin_amdgcn_fence(__ATOMIC_ACQUIRE, "wavefront");
}
DI bool gran_ok(u32x4 g, u32 glo, u32 ghi) { return (g[0] & 0xffu) == glo && g[1] == ghi && (g[2] & 0xffu) == glo && g[3] == ghi; }

#define STAMP_PARAM , u32 (&stamp)[31]
#define STAMP_ARG , stamp
template <int NF> DI void wait_flags_from(u32x2 seen, rsrc_t rws, u32 off, int lane, u32 tlo, u32 thi) {
  if (!__all(seen[0] == tlo && seen[1] == thi)) wait_flags<NF>(rws, off, lane, tlo, thi);
  __builtin_amdgcn_fence(__ATOMIC_ACQUIRE, "wavefront");
}
struct Gath { u32x4 q[4]; };
DI void gath_load(Gath& G, rsrc_t rws, u32 voff, u32 soff) {
#pragma unroll
  for (int k = 0; k < 4; ++k) G.q[k] = ld128_sc1(rws, voff, WS_MODG + soff + (u32)k * 24576u);
}
DI bool gath_ok(const Gath& G, u32 glo, u32 ghi) { return gran_ok(G.q[0], glo, ghi) && gran_ok(G.q[1], glo, ghi) && gran_ok(G.q[2], glo, ghi) && gran_ok(G.q[3], glo, ghi); }
DI u32 gath_val(const Gath& G) {
  const float a = (f_(G.q[0][0] & 0xffffff00u) + f_(G.q[1][0] & 0xffffff00u)) + (f_(G.q[2][0] & 0xffffff00u) + f_(G.q[3][0] & 0xffffff00u));
  const float b = (f_(G.q[0][2] & 0xffffff00u) + f_(G.q[1][2] & 0xffffff00u)) + (f_(G.q[2][2] & 0xffffff00u) + f_(G.q[3][2] & 0xffffff00u));
  return pk2(a, b);
}
DI void mod_issue(u32x4 (&wm)[4], u32x2& cv, rsrc_t rwm, rsrc_t rc, int mq, int mblk, int cw, int lane) {
    const int r0 = 128 * mq + 32 * cw;
#pragma unroll
    for (int i = 0; i < 4; ++i) ald128_nt(wm[i], rwm, (u32)((lane >> 3) * 4 * 6144 + (lane & 7) * 16), (u32)((r0 + i) * 6144 + mblk * 128));
    ald64(cv, rc, (u32)((lane >> 3) * 8), (u32)(r0 * 2));
}
DI void mod_finish(LAS char* L, u32 pred, const u32x4 (&wm)[4], const u32x2& cv, u32 tlo, u32 thi, rsrc_t rws, int mq, int mblk, int cw, int lane) {
    const int g = lane >> 4, i16 = lane & 15;
    float a8[8];
#pragma unroll
    for (int e = 0; e < 8; ++e) a8[e] = 0.f;
#pragma unroll
    for (int i = 0; i < 4; ++i) {
      const float cf = (i & 1) ? bfhi(cv[i >> 1]) : bflo(cv[i >> 1]);
      const float sv = rbf(cf * __builtin_amdgcn_rcpf(1.f + __expf(-cf)));
#pragma unroll
      for (int e = 0; e < 4; ++e) { a8[2 * e] = fmaf(sv, bflo(wm[i][e]), a8[2 * e]); a8[2 * e + 1] = fmaf(sv, bfhi(wm[i][e]), a8[2 * e + 1]); }
    }
#pragma unroll
    for (int e = 0; e < 8; ++e) a8[e] = dpp_add<0x128>(a8[e]);
    float b4[4], b2[2];
#pragma unroll
    for (int e = 0; e < 4; ++e) b4[e] = swap16_add(a8[e], a8[e + 4]);
#pragma unroll
    for (int e = 0; e < 2; ++e) b2[e] = swap32_add(b4[e], b4[e + 2]);
    if (i16 < 8) *(LAS f32x2*)(L + pred + cw * 256 + (i16 * 8 + (g & 1) * 4 + (g >> 1) * 2) * 4) = f32x2{b2[0], b2[1]};
    asm volatile("s_waitcnt lgkmcnt(0)" ::: "memory");
    if (lane == 0) *(volatile LAS u32x2*)(L + pred + 1024 + cw * 8) = u32x2{tlo, thi};
    if (cw == 0) {
      const u64 t0 = __builtin_amdgcn_s_memrealtime();
      for (;;) {
        const u32x2 tg = *(volatile LAS u32x2*)(L + pred + 1024 + (lane & 3) * 8);
        if (__all(tg[0] == tlo && tg[1] == thi)) break;
        if ((__builtin_amdgcn_s_memrealtime() - t0) > SPIN_TICKS) break;
      }
      asm volatile("" ::: "memory");
      LAS float* s = (LAS float*)(L + pred) + lane;
      const float v = (s[0] + s[64]) + (s[128] + s[192]);
      st64_sc1(u32x2{((u_(v) + 0x80u) & 0xffffff00u) | (tlo & 0xffu), (tlo >> 8) | (thi << 24)}, rws, (u32)(lane * 8), WS_MODG + (u32)(mq * 24576 + mblk * 512));
    }
}
DI void front(const Params& p, LAS char* L, const int tid, const int w, const int b, u32x4 (&wr)[18], u32& tlo, u32& thi STAMP_PARAM) {
  const bool isC = w < 4;
  f32x2 xh[10][4];
  const u32 Lb = (u32)(size_t)L;
  const int lane = tid & 63, cw = w & 3;
  const int xcd = b & 7, idx = b >> 3;
  const int tt = 8 * (xcd >> 1) + (idx >> 2), hgl = idx & 3, hg = 4 * (xcd & 1) + hgl;
  const int jt = 8 * (xcd & 1) + 4 * (tt & 1) + hgl;
  const int T = tt >> 1, cb = hg;
  STAMP(0);
  const rsrc_t rx = mk(p.x, S_ * D_ * 2), rw1 = mk(p.wqkv, D_ * 3 * D_ * 2), rwo = mk(p.wo, D_ * D_ * 2);
  const rsrc_t rwg = mk(p.wg, D_ * F_ * 2), rwu = mk(p.wu, D_ * F_ * 2), rwd = mk(p.wd, F_ * D_ * 2);
  const rsrc_t rc = mk(p.c, D_ * 2), rwm = mk(p.wmod, D_ * 6 * D_ * 2), rout = mk(p.out, S_ * D_ * 2), rws = mk(p.ws, WS_BYTES);
  const int g = lane >> 4, i16 = lane & 15, h = lane >> 5, tok = lane & 31, t4 = tid & 255;

  const int nrot = 2 * hgl, rot = 2 * (tt & 7);
  const u32 w1v = (u32)((lane >> 3) * 3072 + (((((lane >> 1) & 3) ^ ((lane >> 4) & 1)) ^ ((cw & 1) << 1)) * 32) + (lane & 1) * 16);
  auto w1slot = [](int n) { return n < 6 ? L_RW + n * L_W1S : (n < 12 ? (n - 6) * L_W1S : L_RW + (n - 12) * L_W1S); };
  {
    u64 pc; u32 len;
    asm volatile("s_getpc_b64 %0\n.Lfb_pc:\n\ts_mov_b32 %1, .Lfb_end-.Lfb_pc" : "=&s"(pc), "=s"(len));
    const rsrc_t rcode = mk((const void*)pc, len);
    warm(Lb + L_SCRAP, rcode, (u32)((idx * 8 + w) * 2 + (lane & 1)) * 128u, 0u);
  }
  u32x2 ev; ald64_sc1(ev, rws, 0u, WS_E);
  const bool prod = isC && idx < 8, prod2 = isC && idx >= 8 && idx < 24;
  const int mq = idx & 3, mblk = idx < 8 ? xcd * 2 + (idx >> 2) : 16 + xcd * 4 + ((idx - 8) >> 2);
  auto dma_w1 = [&](int i) {
    const int n = (i < 9 ? 6 : 0) + 2 * ((i % 9) / 3) + (w >> 2), pp = i % 3;
    dma16(rw1, w1v, (u32)((32 * ((n + rot) & 15) + 8 * cw) * 3072 + pp * 1024 + hg * 128), Lb + (u32)((i < 9 ? (n - 6) * L_W1S : L_RW + n * L_W1S) + pp * 4096 + cw * 1024));
  };
  u32x2 cv; u32x4 wm[4];
  if (prod) mod_issue(wm, cv, rwm, rc, mq, mblk, cw, lane);
  u32x4 xr[10];
#pragma unroll
  for (int n = 0; n < 10; ++n) {
    int r8 = n + nrot; r8 = r8 >= 10 ? r8 - 10 : r8;
    int row = 64 * tt - 16 + 8 * r8 + (lane >> 3); row = row < 0 ? 0 : row;
    ald128_nt(xr[n], rx, (u32)(row * 1024 + (lane & 7) * 16), (u32)(w * 128));
  }
  constexpr int Y0 = 0;
  STAMP(1);
  if (prod) {
    asm volatile("s_waitcnt vmcnt(%c6)" : "+v"(wm[0]), "+v"(wm[1]), "+v"(wm[2]), "+v"(wm[3]), "+v"(cv), "+v"(ev) : "i"(10 + Y0) : "memory");
    const u64 e1 = (((u64)__builtin_amdgcn_readfirstlane(ev[1]) << 32) | (u64)__builtin_amdgcn_readfirstlane(ev[0])) + 1ull;
    mod_finish(L, L_PRED, wm, cv, (u32)e1, (u32)(e1 >> 32), rws, mq, mblk, cw, lane);
  }
  STAMP(2);
  STAMP(3);
  {
    const int c8 = lane & 7, r8l = lane >> 3;
    float pv[10];
#pragma unroll
    for (int n = 0; n < 10; ++n) {
      waitv1<9>(xr[n]);
#pragma unroll
      for (int e = 0; e < 4; ++e) xh[n][e] = f32x2{bflo(xr[n][e]), bfhi(xr[n][e])};
      const f32x2 a1 = (xh[n][0] + xh[n][1]) + (xh[n][2] + xh[n][3]);
      const f32x2 a2 = __builtin_elementwise_fma(xh[n][0], xh[n][0], xh[n][1] * xh[n][1]) + __builtin_elementwise_fma(xh[n][2], xh[n][2], xh[n][3] * xh[n][3]);
      const float s1 = a1[0] + a1[1], s2 = a2[0] + a2[1];
      const float keep = (c8 & 1) ? s2 : s1, give = (c8 & 1) ? s1 : s2;
      float v = keep + f_(__builtin_amdgcn_update_dpp(0, u_(give), 0xB1, 0xf, 0xf, true));
      v = dpp_add<0x4E>(v);
      pv[n] = v + f_(__builtin_amdgcn_update_dpp(0, u_(v), 0x104, 0xf, 0xf, true));
      asm volatile("" : "+v"(pv[n])); dma_w1(n); SB();
    }
    asm volatile("" : "+v"(ev));
    {
      const u64 e1 = (((u64)__builtin_amdgcn_readfirstlane(ev[1]) << 32) | (u64)__builtin_amdgcn_readfirstlane(ev[0])) + 1ull;
      tlo = (u32)e1; thi = (u32)(e1 >> 32);
    }
    if (prod2) mod_issue(wm, cv, rwm, rc, mq, mblk, cw, lane);
    {
    const rsrc_t rt = w == 0 ? rwo : w == 1 ? rwg : w == 2 ? rwu : w == 3 ? rwd : w == 4 ? rout : rws;
    warm(Lb + L_SCRAP, rt, (u32)lane << 20, 0u);
  }

    if (c8 < 2) {
#pragma unroll
      for (int n = 0; n < 10; ++n) *(LAS float*)(L + L_LNP + (n * 8 + r8l) * 80 + (w * 2 + c8) * 4) = pv[n];
    }
    bar();
    f32x4 qa[2][4];
#pragma unroll
    for (int k = 0; k < 2; ++k)
#pragma unroll
      for (int e = 0; e < 4; ++e) qa[k][e] = *(const LAS f32x4*)(L + L_LNP + (k ? 64 + i16 : lane) * 80 + e * 16);
    dma_w1(10);
    float mus[2];
#pragma unroll
    for (int k = 0; k < 2; ++k) {
      const f32x4 a = (qa[k][0] + qa[k][1]) + (qa[k][2] + qa[k][3]);
      const float mu = (a[0] + a[2]) * (1.f / 512.f);
      const float var = fmaxf((a[1] + a[3]) * (1.f / 512.f) - mu * mu, 0.f);
      const float rs = __builtin_amdgcn_rcpf(rbf(__builtin_amdgcn_sqrtf(rbf(rbf(var) + 1e-5f))));
      *(LAS f32x2*)(L + L_RSNB + (k ? 64 + i16 : lane) * 8) = f32x2{rs, -rbf(mu) * rs};
      mus[k] = mu;
    }
    asm volatile("" ::: "memory");
    f32x2 rn[10];
#pragma unroll
    for (int n = 0; n < 10; ++n) rn[n] = *(const LAS f32x2*)(L + L_RSNB + (n * 8 + r8l) * 8);
    if (w == 0) {
#pragma unroll
      for (int k = 0; k < 2; ++k) { int r = (k ? 64 + i16 : lane) + 8 * nrot; r = r >= 80 ? r - 80 : r; ((LAS float*)(L + L_MU1 - 64))[r] = mus[k]; }
    }
#pragma unroll
    for (int n = 0; n < 10; ++n) {
#pragma unroll
      for (int e = 0; e < 4; ++e) xh[n][e] = __builtin_elementwise_fma(xh[n][e], f32x2{rn[n][0], rn[n][0]}, f32x2{rn[n][1], rn[n][1]});
      if (n < 7) { asm volatile("" : "+v"(xh[n][0]), "+v"(xh[n][1]), "+v"(xh[n][2]), "+v"(xh[n][3])); dma_w1(11 + n); SB(); }
    }
  }
#pragma unroll
  for (int n = 0; n < 10; ++n) asm volatile("" : "+v"(xh[n][0]), "+v"(xh[n][1]), "+v"(xh[n][2]), "+v"(xh[n][3]));
  STAMP(4);
  const u32 glo = tlo & 0xffu, ghi = (tlo >> 8) | (thi << 24);
  const u32 gvo = (u32)(w * 512 + tok * 16 + h * 4096);
  Gath g1_;
#pragma unroll
  for (int k = 0; k < 4; ++k) ald128_sc1<0>(g1_.q[k], rws, gvo, WS_MODG + (u32)k * 24576u);
  if (prod2) {
    asm volatile("s_waitcnt vmcnt(12)" : "+v"(wm[0]), "+v"(wm[1]), "+v"(wm[2]), "+v"(wm[3]), "+v"(cv) :: "memory");
    mod_finish(L, L_PRED, wm, cv, tlo, thi, rws, mq, mblk, cw, lane);
  }
  waitv<13>();
  bar();
  if (isC) {
    LAS char* w1rd = L + ((8 * g + (i16 >> 2)) * 128 + ((cw ^ (((i16 >> 3) & 1) | ((g & 1) << 1))) * 32) + (lane & 3) * 8);

#pragma unroll
    for (int n = 0; n < 6; ++n)
#pragma unroll
      for (int pp = 0; pp < 3; ++pp) wr[3 * n + pp] = trd2(w1rd + n * L_W1S + pp * 4096, 512);
  }
  STAMP(5);
  waitv4<0>(g1_.q[0], g1_.q[1], g1_.q[2], g1_.q[3]);
  if (!__all(gath_ok(g1_, glo, ghi))) {
    const u64 t0 = __builtin_amdgcn_s_memrealtime();
    for (;;) {
      gath_load(g1_, rws, gvo, 0u);
      if (__all(gath_ok(g1_, glo, ghi))) break;
      if (spin_out(t0)) break;
    }
  }
  {
    const u32 v2 = gath_val(g1_);
    const float a = bflo(v2), c = bfhi(v2);
    *(LAS f32x2*)(L + (h ? L_G1 : L_S1) + (w * 32 + tok) * 8) = h ? f32x2{rbf(1.f + a), rbf(1.f + c)} : f32x2{a, c};
  }
  STAMP(6);
  bar();
  {
    const int c8 = lane & 7, r8l = lane >> 3;
    const LAS f32x4* gp = (const LAS f32x4*)(L + L_G1 + (w * 64 + c8 * 8) * 4);
    const LAS f32x4* sp = (const LAS f32x4*)(L + L_S1 + (w * 64 + c8 * 8) * 4);
    const f32x4 g0 = gp[0], g1 = gp[1], s0 = sp[0], s1 = sp[1];
    LAS char* ab = L + L_AIMG + (2 * w + (c8 >> 2)) * 1024 + (c8 & 3) * 256 + ((r8l ^ c8) * 16);
#pragma unroll
    for (int n = 0; n < 10; ++n) {
      const f32x2 h0 = __builtin_elementwise_fma(xh[n][0], f32x2{g0[0], g0[1]}, f32x2{s0[0], s0[1]}), h1 = __builtin_elementwise_fma(xh[n][1], f32x2{g0[2], g0[3]}, f32x2{s0[2], s0[3]});
      const f32x2 h2 = __builtin_elementwise_fma(xh[n][2], f32x2{g1[0], g1[1]}, f32x2{s1[0], s1[1]}), h3 = __builtin_elementwise_fma(xh[n][3], f32x2{g1[2], g1[3]}, f32x2{s1[2], s1[3]});
      const u32x4 o = u32x4{pk2(h0[0], h0[1]), pk2(h1[0], h1[1]), pk2(h2[0], h2[1]), pk2(h3[0], h3[1])};
      int r8 = n + nrot; r8 = r8 >= 10 ? r8 - 10 : r8;
      *(LAS u32x4*)(ab + (r8 >> 1) * 16384 + (r8 & 1) * 128) = o;
    }
  }
  bar();
  STAMP(7);
}

template <bool isC> DI void role_body(const Params& p, LAS char* L, const int tid, const int w, const int b, u32x4 (&wr)[18], const u32 tlo, const u32 thi STAMP_PARAM) {
  const u32 Lb = (u32)(size_t)L;
  const int lane = tid & 63, cw = w & 3;
  const int xcd = b & 7, idx = b >> 3;
  const int tt = 8 * (xcd >> 1) + (idx >> 2), hgl = idx & 3, hg = 4 * (xcd & 1) + hgl;
  const int jt = 8 * (xcd & 1) + 4 * (tt & 1) + hgl;
  const int T = tt >> 1, cb = hg;
  const rsrc_t rx = mk(p.x, S_ * D_ * 2), rw1 = mk(p.wqkv, D_ * 3 * D_ * 2), rwo = mk(p.wo, D_ * D_ * 2);
  const rsrc_t rwg = mk(p.wg, D_ * F_ * 2), rwu = mk(p.wu, D_ * F_ * 2), rwd = mk(p.wd, F_ * D_ * 2);
  const rsrc_t rc = mk(p.c, D_ * 2), rwm = mk(p.wmod, D_ * 6 * D_ * 2), rout = mk(p.out, S_ * D_ * 2), rws = mk(p.ws, WS_BYTES);
  const int g = lane >> 4, i16 = lane & 15, h = lane >> 5, tok = lane & 31, t4 = tid & 255;

  const int nrot = 2 * hgl, rot = 2 * (tt & 7);
  const u32 w1v = (u32)((lane >> 3) * 3072 + (((((lane >> 1) & 3) ^ ((lane >> 4) & 1)) ^ ((cw & 1) << 1)) * 32) + (lane & 1) * 16);
  auto w1slot = [](int n) { return n < 6 ? L_RW + n * L_W1S : (n < 12 ? (n - 6) * L_W1S : L_RW + (n - 12) * L_W1S); };
  LAS char* w1rd = L + ((8 * g + (i16 >> 2)) * 128 + ((cw ^ (((i16 >> 3) & 1) | ((g & 1) << 1))) * 32) + (lane & 3) * 8);
  const u32 glo = tlo & 0xffu, ghi = (tlo >> 8) | (thi << 24);
  const int p0 = jt & 3, rotg = (4 * (T & 3) + 2 * (jt & 7)) & 15, rotd = (tt + hgl) & 3;
  const u32 wov = (u32)((lane >> 3) * 1024 + ((((lane >> 2) & 1) ^ ((lane >> 4) & 1)) * 64) + (lane & 3) * 16);
  const int mrow = (lane >> 4) & 1, q4 = i16 >> 2;
  const u32 p1base = (u32)(mrow * 4096 + (4 * h + q4) * 128 + (lane & 3) * 8), p1x = (u32)((q4 >> 1) | (mrow << 1));
  const u32 p2base = (u32)(mrow * L_P2M + (4 * h + q4) * 96 + (lane & 3) * 8);

  if constexpr (!isC) {
    const u32 gch = h ? (u32)((cw == 0 ? 1024 : cw == 1 ? 2048 : 2560) + 64 * cb + 2 * tok) : (u32)((lane & 16 ? 1536 : 2048) + 32 * T + 2 * i16);
    Gath g2_;
#pragma unroll
    for (int k = 0; k < 4; ++k) ald128_sc1<0>(g2_.q[k], rws, gch * 8u, WS_MODG + (u32)k * 24576u);
    const rsrc_t rgu = (cw & 1) ? rwu : rwg;
    const int uc = 7 * (cw >> 1) + ((lane & 7) < 7 ? (lane & 7) : 6);
    u32x4 uw[4];
    u32 uso = (u32)(T * (32 * 3584) + jt * 224); asm volatile("" : "+s"(uso));
#pragma unroll
    for (int i = 0; i < 4; ++i) ald128<0>(uw[i], rgu, (u32)((lane >> 3) * 3584 + uc * 16), uso + (u32)(8 * i * 3584));
    bar();
#pragma unroll 1
    for (int n = 0; n < 4; ++n)
#pragma unroll
      for (int pp = 0; pp < 3; ++pp) dma16(rw1, w1v, (u32)((32 * ((12 + n + rot) & 15) + 8 * cw) * 3072 + pp * 1024 + hg * 128), Lb + (u32)(L_RW + n * L_W1S + pp * 4096 + cw * 1024));
    {
      waitv4<16>(g2_.q[0], g2_.q[1], g2_.q[2], g2_.q[3]);
      if (!__all(gath_ok(g2_, glo, ghi))) {
        const u64 t0 = __builtin_amdgcn_s_memrealtime();
        for (;;) {
          gath_load(g2_, rws, gch * 8u, 0u);
          if (__all(gath_ok(g2_, glo, ghi))) break;
          if (spin_out(t0)) break;
        }
      }
      const u32 v2 = gath_val(g2_);
      const bool one = h ? cw == 1 : !(lane & 16);
      const float a = bflo(v2), c = bfhi(v2);
      const u32 dst = h ? (cw == 0 ? L_GA : cw == 1 ? L_G2C : cw == 2 ? L_GBV : L_SCRAP) + tok * 8 : L_SL + cw * 256 + tok * 8;
      *(LAS f32x2*)(L + dst) = one ? f32x2{rbf(1.f + a), rbf(1.f + c)} : f32x2{a, c};
    }
    waitv<0>(); bar();
    STAMP(8);
    {
      asm volatile("" : "+v"(uw[0]), "+v"(uw[1]), "+v"(uw[2]), "+v"(uw[3]));
      float a[16];
#pragma unroll
      for (int e = 0; e < 16; ++e) a[e] = 0.f;
#pragma unroll
      for (int i = 0; i < 4; ++i) {
        const float gk = ((const LAS float*)(L + L_SL + cw * 256))[8 * i + (lane >> 3)], sk = ((const LAS float*)(L + L_SL + cw * 256))[32 + 8 * i + (lane >> 3)];
#pragma unroll
        for (int e = 0; e < 4; ++e) {
          const float lo = bflo(uw[i][e]), hi = bfhi(uw[i][e]);
          a[2 * e] = fmaf(gk, lo, a[2 * e]); a[2 * e + 1] = fmaf(gk, hi, a[2 * e + 1]);
          a[8 + 2 * e] = fmaf(sk, lo, a[8 + 2 * e]); a[9 + 2 * e] = fmaf(sk, hi, a[9 + 2 * e]);
        }
      }
#pragma unroll
      for (int e = 0; e < 16; ++e) a[e] = dpp_add<0x128>(a[e]);
      float b8[8], ut4[4];
#pragma unroll
      for (int e = 0; e < 8; ++e) b8[e] = swap16_add(a[e], a[e + 8]);
#pragma unroll
      for (int e = 0; e < 4; ++e) ut4[e] = swap32_add(b8[e], b8[e + 4]);
      auto r0 = __builtin_amdgcn_permlane16_swap(u_(ut4[0]), u_(ut4[2]), false, false);
      auto r1 = __builtin_amdgcn_permlane16_swap(u_(ut4[1]), u_(ut4[3]), false, false);
      const u32 a0 = r0[0], b0 = r0[1], a1 = r1[0], b1 = r1[1];
      const int pr = uc >> 1, slot = pr >= p0 && pr < p0 + 4 ? pr - p0 : (pr < p0 ? 4 + pr : pr);
      if (i16 < 7) st128_sc1(u32x4{a0, b0, a1, b1}, rws, (u32)((slot * 32 + (cw & 1) * 16 + (uc & 1) * 8 + h * 4 + (g & 1) * 2) * 8), WS_UT + (u32)((jt * 16 + T) * 1792));
    }
    STAMP(9);
    bar();
#pragma unroll 1
    for (int q = 0; q < 16; ++q) { const int qq = (q + rot) & 15; dma16(rwo, wov, (u32)((128 * cw + 8 * qq) * 1024 + cb * 128), Lb + (u32)(L_WO + (16 * cw + qq) * 1024)); }
#pragma unroll
    for (int q = 0; q < 2; ++q) dma16(rx, (u32)((lane >> 3) * 1024 + (lane & 7) * 16), (u32)((64 * tt + 16 * cw + 8 * q) * 1024 + cb * 128), Lb + (u32)(L_XRES + (2 * cw + q) * 1024));
    const u32 p1v = (u32)((lane >> 3) * 3584 + ((((lane >> 1) & 3) ^ ((lane >> 4) & 1)) * 32) + (lane & 1) * 16);
    auto dma_p1 = [&](int k) {
      const u32 so = (u32)((32 * ((k + rotg) & 15) + 8 * cw) * 3584 + jt * 224 + p0 * 32);
      dma16(rwg, p1v, so, Lb + (u32)(k * L_P1B + cw * 1024));
      dma16(rwu, p1v ^ 64u, so, Lb + (u32)(k * L_P1B + 4096 + cw * 1024));
    };
    dma_p1(0); dma_p1(1);
    waitv<22>(); bar();
    if (tid == 256) st64_sc1(u32x2{tlo, thi}, rws, 0u, WS_FU + (u32)(jt * 256 + T * 8));
    waitv<4>(); STAMP(10); bar();
#pragma unroll 1
    for (int k = 2; k < 8; ++k) dma_p1(k);
    STAMP(11); bar();
#pragma unroll 1
    for (int k = 8; k < 16; ++k) dma_p1(k);
    waitv<16>(); bar();
    {
      wait_flags<16>(rws, WS_FU + (u32)(jt * 256), lane, tlo, thi);
      u32x2 pt[16];
#pragma unroll
      for (int k = 0; k < 16; ++k) ald64_sc1(pt[k], rws, (u32)(t4 < 224 ? t4 : 0) * 8u, WS_UT + (u32)((jt * 16 + k) * 1792));
      SB();
      float uu = 0.f, tt_ = 0.f;
      waitv<0>();
#pragma unroll
      for (int k = 0; k < 16; ++k) { asm volatile("" : "+v"(pt[k])); uu += f_(pt[k][0]); tt_ += f_(pt[k][1]); }
      const u32 hi = pk2(uu, tt_), lo = pk2(uu - bflo(hi), tt_ - bfhi(hi));
      const u32 u1 = hi & 0xffffu, t1 = hi >> 16, u2 = lo & 0xffffu, t2 = lo >> 16;
      const u32x4 o = u32x4{u1 | (u1 << 16), u2 | (u2 << 16), t1 | (t1 << 16), t2 | (t2 << 16)};
      if (t4 < 224) { LAS char* d = L + L_FIX + (t4 >> 5) * 1024 + (t4 & 31) * 16; *(LAS u32x4*)d = o; *(LAS u32x4*)(d + 512) = o; }
    }
    u32 p2v[3];
#pragma unroll
    for (int i = 0; i < 3; ++i) { const int n = 64 * i + lane, row = n / 6, c = n - row * 6; p2v[i] = (u32)(row * 3584 + c * 16 + ((c >> 1) >= p0 ? 128 : 0)); }
    auto dma_p2 = [&](int k0, int k1) {
      if (ELIM & 1) return;
#pragma unroll 1
      for (int k = k0; k < k1; ++k) {
        const int pos = 2 * k + (cw >> 1);
#pragma unroll
        for (int i = 0; i < 3; ++i) dma16(rgu, p2v[i], (u32)(((pos + rotg) & 15) * 32 * 3584 + jt * 224), Lb + (u32)(pos * L_P2B + (cw & 1) * L_P2M + i * 1024));
      }
    };
    auto dma_wd = [&](int u) {
      if (ELIM & 4) return;
#pragma unroll
      for (int q = 0; q < 8; ++q) {
        const int n = 8 * (u - 4) + q;
        const int jj = ((u < 4 ? u : n / 6) + rotd) & 3, r6 = n % 6;
        const int row = u < 4 ? 112 * jj + 16 * jj + 8 * q : 112 * jj + 8 * r6 + ((r6 >> 1) >= jj ? 64 : 0);
        dma16(rwd, wov, (u32)((448 * cw + row) * 1024 + cb * 128), Lb + (u32)(wd_unit_off(u) + cw * 8192 + q * 1024));
      }
    };
    waitv<0>();
    STAMP(12); bar();
    dma_p2(0, 5);
    if (b == 0 && cw == 3) {
      const u64 t0 = __builtin_amdgcn_s_memrealtime();
      for (;;) {
        bool ok = true;
#pragma unroll
        for (int k = 0; k < 4; ++k) { const int f = 64 * k + lane; const u32x2 v = ld64_sc1(rws, (u32)((f >> 3) * 1024 + (f & 7) * 8), WS_FO); ok = ok && v[0] == tlo && v[1] == thi; }
        if (__all(ok)) break;
        if (spin_out(t0)) break;
      }
      if (lane == 0) st64_sc1(u32x2{tlo, thi}, rws, 0u, WS_E);
    }
    waitv<0>(); STAMP(15); bar();
    dma_p2(5, 8);
    bar();
    dma_wd(0);
    waitv<8>(); STAMP(16); bar();
    dma_wd(1);
    bar();
    dma_wd(2);
    waitv<16>(); bar();
    dma_wd(3);
    waitv<16>(); STAMP(17); bar();
    dma_wd(4);
    waitv<16>(); bar();
    dma_wd(5);
    waitv<16>(); bar();
    dma_wd(6);
    waitv<16>(); STAMP(18); bar();
    waitv<8>(); bar();
    waitv<0>(); bar();
    bar();
    asm volatile(".Lfb_end:");
  } else {
    f32x4 m0, m1, m0f;
#pragma unroll
    for (int r = 0; r < 4; ++r) {
      const int d1 = i16 - 4 * g - r, d0 = d1 + 16;
      m0[r] = (d0 <= 14 && !(d0 & 1)) ? 0.f : -1e30f;
      m1[r] = (d1 >= 0 && d1 <= 14 && !(d1 & 1)) ? 0.f : -1e30f;
      m0f[r] = tt == 0 ? -1e30f : m0[r];
    }
    f32x4 Kt[5], Qt[4], Vn[5];
#pragma unroll
    for (int r = 0; r < 5; ++r) { Kt[r] = f32x4{0, 0, 0, 0}; Vn[r] = f32x4{0, 0, 0, 0}; if (r < 4) Qt[r] = f32x4{0, 0, 0, 0}; }
    {
      u32x4 a[2][5], wl[2][3];
      LAS char* const w1a = opq(w1rd + L_RW); LAS char* const w1b = opq(w1rd + L_RW + 4 * L_W1S);
      auto ld = [&](int n, int bi) {
        LAS char* ab = L + L_AIMG + ((n + rot) & 15) * 1024 + g * 256 + ((i16 ^ (((n & 1) << 2) | g)) * 16);
#pragma unroll
        for (int rb = 0; rb < 5; ++rb) a[bi][rb] = *(const LAS u32x4*)(ab + rb * 16384);
        if (n < 6 || n >= 12) {
#pragma unroll
          for (int pp = 0; pp < 3; ++pp) wl[bi][pp] = trd2((n % 6 < 4 ? w1a : w1b) + (n % 6 % 4) * L_W1S + pp * 4096, 512);
        }
      };
      ld(0, 0); SB();
#pragma unroll
      for (int n = 0; n < 16; ++n) {
        if (n == 3) bar();
        if (n == 8) asm volatile("" : "+v"(m0), "+v"(m1), "+v"(m0f));
        if (n == 11) WACC(24, bar());
        if (n < 15) ld(n + 1, (n + 1) & 1);
        const bool rg = n >= 6 && n < 12;
        const u32x4 wq = rg ? wr[rg ? 3 * (n - 6) : 0] : wl[n & 1][0], wk = rg ? wr[rg ? 3 * (n - 6) + 1 : 0] : wl[n & 1][1], wv = rg ? wr[rg ? 3 * (n - 6) + 2 : 0] : wl[n & 1][2];
#pragma unroll
        for (int rb = 0; rb < 5; ++rb) {
          Kt[rb] = mfma16(wk, a[n & 1][rb], Kt[rb]);
          Vn[rb] = mfma16(a[n & 1][rb], wv, Vn[rb]);
          if (rb > 0) Qt[rb - 1] = mfma16(wq, a[n & 1][rb], Qt[rb - 1]);
        }
#pragma unroll
        for (int k = 0; k < 11; ++k) { SGB_MFMA(1); SGB_DSR(1); }
        SGB_MFMA(3);
        SB();
      }
    }
    bar();
    STAMP(8);
    const int tb = cw & 1, kh = cw >> 1;
    {
      u32x2 kp[5], vp[5], qp[4];
#pragma unroll
      for (int r = 0; r < 5; ++r) { kp[r] = u32x2{pk2(Kt[r][0], Kt[r][1]), pk2(Kt[r][2], Kt[r][3])}; if (r < 4) qp[r] = u32x2{pk2(Qt[r][0], Qt[r][1]), pk2(Qt[r][2], Qt[r][3])}; }
      f32x4 s0[4], s1[4];
#pragma unroll
      for (int qb = 0; qb < 4; ++qb) { s0[qb] = mfma16k16(kp[qb], qp[qb], qb == 0 ? m0f : m0); s1[qb] = mfma16k16(kp[qb + 1], qp[qb], m1); }
#pragma unroll
      for (int r = 0; r < 5; ++r) vp[r] = u32x2{pk2(Vn[r][0], Vn[r][1]), pk2(Vn[r][2], Vn[r][3])};
      float mx[4];
#pragma unroll
      for (int qb = 0; qb < 4; ++qb) mx[qb] = mx2(mx2(mx2(s0[qb][0], s0[qb][1]), mx2(s0[qb][2], s0[qb][3])), mx2(mx2(s1[qb][0], s1[qb][1]), mx2(s1[qb][2], s1[qb][3])));
#pragma unroll
      for (int qb = 0; qb < 4; ++qb) { auto r = __builtin_amdgcn_permlane16_swap(u_(mx[qb]), u_(mx[qb]), false, false); const u32 r0 = r[0], r1 = r[1]; mx[qb] = mx2(f_(r0), f_(r1)); }
#pragma unroll
      for (int qb = 0; qb < 4; ++qb) { auto r = __builtin_amdgcn_permlane32_swap(u_(mx[qb]), u_(mx[qb]), false, false); const u32 r0 = r[0], r1 = r[1]; mx[qb] = mx2(f_(r0), f_(r1)); }
      const float cs = 0.25f * 1.44269504088896f;
      const u32x4 ones = u32x4{0x3f803f80u, 0x3f803f80u, 0x3f803f80u, 0x3f803f80u};
      const f32x4 z = f32x4{0, 0, 0, 0};
      f32x4 ot[4], sm[4];
#pragma unroll
      for (int qb = 0; qb < 4; ++qb) {
        const float nm = -mx[qb] * cs;
        float e0[4], e1[4];
#pragma unroll
        for (int r = 0; r < 4; ++r) { e0[r] = __builtin_amdgcn_exp2f(fmaf(s0[qb][r], cs, nm)); e1[r] = __builtin_amdgcn_exp2f(fmaf(s1[qb][r], cs, nm)); }
        const u32x4 pf = u32x4{pk2(e0[0], e0[1]), pk2(e0[2], e0[3]), pk2(e1[0], e1[1]), pk2(e1[2], e1[3])};
        sm[qb] = mfma16(ones, pf, z);
        ot[qb] = mfma16(u32x4{vp[qb][0], vp[qb][1], vp[qb + 1][0], vp[qb + 1][1]}, pf, z);
      }
      u32x2 op[4];
#pragma unroll
      for (int qb = 0; qb < 4; ++qb) {
        const float ri = __builtin_amdgcn_rcpf(sm[qb][0]);
        op[qb] = u32x2{pk2(ot[qb][0] * ri, ot[qb][1] * ri), pk2(ot[qb][2] * ri, ot[qb][3] * ri)};
      }
#pragma unroll
      for (int t2 = 0; t2 < 2; ++t2) {
        auto r0 = __builtin_amdgcn_permlane16_swap(op[2 * t2][0], op[2 * t2 + 1][0], false, false);
        auto r1 = __builtin_amdgcn_permlane16_swap(op[2 * t2][1], op[2 * t2 + 1][1], false, false);
        const u32 a0 = r0[0], b0 = r0[1], a1 = r1[0], b1 = r1[1];
        st128_sc1(u32x4{a0, a1, b0, b1}, rws, (u32)(lane * 16), WS_O + (u32)(((2 * tt + t2) * 32 + 4 * hg + cw) * 1024));
      }
    }
    STAMP(9);
    pubbar<0>(8, rws, WS_FO + (u32)(tt * 1024 + hg * 8), tlo, thi, tid);
    STAMP(10);
    wait_flags<8>(rws, WS_FO + (u32)(tt * 1024 + cb * 128), lane, tlo, thi);
    STAMP(11);
    u32 x1p[8];
    {
      u32x4 of[16];
      const u32 ob = WS_O + (u32)(((2 * tt + tb) * 32 + 16 * kh) * 1024);
#pragma unroll
      for (int s = 0; s < 16; ++s) { of[s] = ld128_sc1(rws, (u32)(lane * 16), ob + (u32)(((s + 4 * hgl) & 15) * 1024)); SB(); }
      SB();
      WACC(24, bar());
      LAS char* q0 = L + (L_WO + (256 * kh + 8 * h + q4) * 128 + mrow * 32 + (lane & 3) * 8);
      LAS char* qa = q0 + ((q4 >> 1) ^ kh) * 64; LAS char* qb = q0 + ((q4 >> 1) ^ kh ^ 1) * 64;
      f32x16 a0, a1;
#pragma unroll
      for (int r = 0; r < 16; ++r) { a0[r] = 0.f; a1[r] = 0.f; }
      u32x4 wl[2][2];
      wl[0][0] = trd2(qa + hgl * 8192, 512); wl[0][1] = trd2(qb + hgl * 8192, 512); SB();
#pragma unroll
      for (int s = 0; s < 16; ++s) {
        if (s < 15) { const int o2 = ((((s + 1) >> 2) + hgl) & 3) * 8192 + ((s + 1) & 3) * 2048; wl[(s + 1) & 1][0] = trd2(qa + o2, 512); wl[(s + 1) & 1][1] = trd2(qb + o2, 512); }
        a0 = mfma32(wl[s & 1][0], of[s], a0); a1 = mfma32(wl[s & 1][1], of[s], a1);
        SGB_MFMA(1); SGB_DSR(2); SGB_MFMA(1); SGB_DSR(2);
        SB();
      }
      u32x2 xres[4]; f32x4 ga4[4], g24[4];
#pragma unroll
      for (int q = 0; q < 4; ++q) {
        xres[q] = *(const LAS u32x2*)(L + L_XRES + (32 * tb + tok) * 128 + (32 * kh + 8 * q + 4 * h) * 2);
        ga4[q] = *(const LAS f32x4*)(L + L_GA + (32 * kh + 8 * q + 4 * h) * 4); g24[q] = *(const LAS f32x4*)(L + L_G2C + (32 * kh + 8 * q + 4 * h) * 4);
      }
      const float mu1 = ((const LAS float*)(L + L_MU1))[32 * tb + tok];
#pragma unroll
      for (int q = 0; q < 4; ++q) *(LAS f32x4*)(L + L_RED2 + cw * 4096 + q * 1024 + lane * 16) = f32x4{a1[4 * q], a1[4 * q + 1], a1[4 * q + 2], a1[4 * q + 3]};
      f32x2 xa[8];
#pragma unroll
      for (int q = 0; q < 4; ++q) {
        xa[2 * q] = __builtin_elementwise_fma(f32x2{ga4[q][0], ga4[q][1]}, f32x2{a0[4 * q], a0[4 * q + 1]}, f32x2{bflo(xres[q][0]), bfhi(xres[q][0])});
        xa[2 * q + 1] = __builtin_elementwise_fma(f32x2{ga4[q][2], ga4[q][3]}, f32x2{a0[4 * q + 2], a0[4 * q + 3]}, f32x2{bflo(xres[q][1]), bfhi(xres[q][1])});
      }
#pragma unroll
      for (int q = 0; q < 8; q += 4) asm volatile("" : "+v"(xa[q]), "+v"(xa[q + 1]), "+v"(xa[q + 2]), "+v"(xa[q + 3]));
      STAMP(19);
      bar();
      STAMP(20);
      f32x2 sv1 = f32x2{0.f, 0.f}, sv2 = f32x2{0.f, 0.f}; u32 yp[8];
#pragma unroll
      for (int q = 0; q < 4; ++q) {
        const f32x4 o4 = *(const LAS f32x4*)(L + L_RED2 + (cw ^ 2) * 4096 + q * 1024 + lane * 16);
#pragma unroll
        for (int e = 0; e < 2; ++e) {
          const f32x2 v = __builtin_elementwise_fma(f32x2{ga4[q][2 * e], ga4[q][2 * e + 1]}, f32x2{o4[2 * e], o4[2 * e + 1]}, xa[2 * q + e]);
          x1p[2 * q + e] = pk2(v[0], v[1]);
          const f32x2 d = f32x2{bflo(x1p[2 * q + e]), bfhi(x1p[2 * q + e])} - f32x2{mu1, mu1};
          sv1 += d; sv2 = __builtin_elementwise_fma(d, d, sv2);
          const f32x2 y = d * f32x2{g24[q][2 * e], g24[q][2 * e + 1]};
          yp[2 * q + e] = pk2(y[0], y[1]);
        }
      }
      float s1 = sv1[0] + sv1[1], s2 = sv2[0] + sv2[1];
      const u32 yb = WS_Y + (u32)(((2 * tt + tb) * 32 + 4 * cb + 2 * kh) * 1024);
      st128_sc1(u32x4{yp[0], yp[1], yp[2], yp[3]}, rws, (u32)(lane * 16), yb);
      st128_sc1(u32x4{yp[4], yp[5], yp[6], yp[7]}, rws, (u32)(lane * 16), yb + 1024);
      s1 = swap32_add(s1, s1); s2 = swap32_add(s2, s2);
      if (lane < 32 && cb == 0 && kh == 0) __builtin_amdgcn_raw_buffer_store_b32(u_(mu1), rws, (u32)(tok * 4), WS_MU + (u32)((2 * tt + tb) * 128), 16);
      if (lane < 32) st64_sc1(u32x2{u_(s1), u_(s2)}, rws, (u32)(tok * 8), WS_ST + (u32)(((2 * tt + tb) * 16 + 2 * cb + kh) * 256));
    }
#pragma unroll
    for (int q = 0; q < 8; q += 4) asm volatile("" : "+v"(x1p[q]), "+v"(x1p[q + 1]), "+v"(x1p[q + 2]), "+v"(x1p[q + 3]));
    STAMP(12);
    pubbar<0>(16, rws, WS_FY + (u32)(tt * 2048 + cb * 8), tlo, thi, tid);
    STAMP(13);
    wait_flags<8>(rws, WS_FY + (u32)((2 * T + (cw >> 1)) * 2048 + jt * 128), lane, tlo, thi);
    STAMP(14);
    const int tb32 = 4 * T + cw;
    u32x4 yf[32];
    auto ldy = [&](int s) { yf[s] = ld128_sc1(rws, (u32)(lane * 16), WS_Y + (u32)((tb32 * 32 + ((s + 2 * rotg) & 31)) * 1024)); };
#pragma unroll
    for (int s = 0; s < (EXPW ? 32 : 10); ++s) { ldy(s); SB(); }
    const u32 stv8 = __builtin_amdgcn_raw_buffer_load_b32(rws, (u32)(tok * 4), WS_MU + (u32)(tb32 * 128), 16);
    u32x2 stv[8];
#pragma unroll
    for (int q = 0; q < 8; ++q) stv[q] = ld64_sc1(rws, (u32)(tok * 8 + h * 2048), WS_ST + (u32)((tb32 * 16 + q) * 256));
    SB();
    u32 mz, sz; float r2;
    auto act_store = [&](const f32x16& ac, int pp) {
      const float r1 = __builtin_amdgcn_sqrtf(r2);
      u32x4 o;
#pragma unroll
      for (int e = 0; e < 4; ++e) {
        const f32x2 gsc = f32x2{ac[2 * e], ac[2 * e + 1]} * f32x2{r2, r2};
        const f32x2 av = f32x2{fmaxf(gsc[0], 0.f), fmaxf(gsc[1], 0.f)} * f32x2{ac[8 + 2 * e], ac[9 + 2 * e]};
        o[e] = pk2(av[0], av[1]);
      }
      st128_sc1(o, rws, (u32)(lane * 16), WS_ACT + (u32)((tb32 * 112 + 7 * jt + pp) * 1024));
    };
    {
      f32x16 acc[4];
#pragma unroll
      for (int q = 0; q < 4; ++q)
#pragma unroll
        for (int r = 0; r < 16; ++r) acc[q][r] = 0.f;
      LAS char* pa[4];
#pragma unroll
      for (int pp = 0; pp < 4; ++pp) pa[pp] = L + (p1base + (((u32)pp ^ p1x) * 32));
      u32x4 wf[2][4], fx, fxp;
      auto ldw = [&](int st, int bi) {
#pragma unroll
        for (int pp = 0; pp < 4; ++pp) { if (st == 16) pa[pp] = opq(pa[pp] + 8 * L_P1B); wf[bi][pp] = trd2(pa[pp] + ((st >> 1) & 7) * L_P1B + (st & 1) * 2048, 1024); }
      };
      ldw(0, 0); SB();
#pragma unroll
      for (int st = 0; st < 32; ++st) {
        if (st == 15) WACC(25, bar());
        if (st == 12) {
      float s1 = 0.f, s2 = 0.f;
#pragma unroll
      for (int q = 0; q < 8; ++q) { s1 += f_(stv[q][0]); s2 += f_(stv[q][1]); }
      s1 = swap32_add(s1, s1); s2 = swap32_add(s2, s2);
      const float md0 = s1 * (1.f / 512.f), mu1t = f_(stv8);
      const float sg = rbf(__builtin_amdgcn_sqrtf(rbf(rbf(fmaxf(s2 * (1.f / 512.f) - md0 * md0, 0.f)) + 1e-5f)));
      const float md = rbf(md0 + mu1t) - mu1t;
      r2 = __builtin_amdgcn_rcpf(sg * sg);
      const u32 hi = pk2(-md, sg), lo = pk2(-md - bflo(hi), sg - bfhi(hi));
      const u32 mm = (hi & 0xffffu) | (lo << 16), ss = (hi >> 16) | (lo & 0xffff0000u);
      mz = h ? 0u : mm; sz = h ? 0u : ss;
      asm volatile("" : "+v"(mz), "+v"(sz), "+v"(r2));
    }
        if (st + 1 < 32) ldw(st + 1, (st + 1) & 1);
        if (OPT & 2) { if (2 * st + 10 < 32) { ldy(2 * st + 10); SB(); ldy(2 * st + 11); } }
        else if (!EXPW && st + 10 < 32) ldy(st + 10);
#pragma unroll
        for (int pp = 0; pp < 4; ++pp) acc[pp] = mfma32(wf[st & 1][pp], yf[st], acc[pp]);
#pragma unroll
        for (int pp = 0; pp < 4; ++pp) { SGB_MFMA(1); SGB_DSR(2); }
        SB();
        if (st >= 24 && st < 28) fx = *(const LAS u32x4*)(L + L_FIX + (st - 24) * 1024 + lane * 16), SB();
        if (st >= 25 && st < 29) acc[st - 25 < 4 && st >= 25 ? st - 25 : 0] = mfma32(fxp, u32x4{mz, mz, sz, sz}, acc[st - 25 < 4 && st >= 25 ? st - 25 : 0]), SB();
        fxp = fx;
      }
      STAMP(15);
#pragma unroll
      for (int pp = 0; pp < 4; ++pp) act_store(acc[pp], p0 + pp);
      WACC(26, bar());
    }
    u32x4 af[16][2]; u32x2 fl;
    auto lda = [&](int n) { const int s = n >> 1, t2 = n & 1; af[s][t2] = ld128_sc1(rws, (u32)(lane * 16), WS_ACT + (u32)(((2 * tt + (t2 ^ tb)) * 112 + 28 * cw + 8 * (((s >> 2) + rotd) & 3) + (s & 3)) * 1024)); };
    {
      f32x16 acc[3], z16;
#pragma unroll
      for (int r = 0; r < 16; ++r) z16[r] = 0.f;
#pragma unroll
      for (int pp = 0; pp < 3; ++pp) acc[pp] = mfma32(*(const LAS u32x4*)(L + L_FIX + (4 + pp) * 1024 + lane * 16), u32x4{mz, mz, sz, sz}, z16);
      LAS char* pb = L + p2base;
      u32x4 wf[2][3];
      auto ldw = [&](int st, int bi) {
        if (st == 16) pb = opq(pb + 8 * L_P2B);
#pragma unroll
        for (int pp = 0; pp < 3; ++pp) wf[bi][pp] = trd2(pb + ((st >> 1) & 7) * L_P2B + (st & 1) * 1536 + pp * 32, 768);
      };
      ldw(0, 0); SB();
#pragma unroll
      for (int st = 0; st < 32; ++st) {
        if (st == S0P) WACC(30, pubbar<0>(16, rws, WS_FA + (u32)(T * 2048 + jt * 8), tlo, thi, tid));
        if (st == 15 || st == 23) WACC(27, bar());
        if (st == FA1P) fl = peek_flags<4>(rws, WS_FA + (u32)(T * 2048 + ((tt & 1) * 8 + cb) * 128 + cw * 32), lane);
        if (st == FA1P + 4) { STAMP(23); WACC(28, wait_flags_from<4>(fl, rws, WS_FA + (u32)(T * 2048 + ((tt & 1) * 8 + cb) * 128 + cw * 32), lane, tlo, thi)); }
        if (st + 1 < 32) ldw(st + 1, (st + 1) & 1);
        if (st >= FA1P + 4) { const int k = st - FA1P - 4; if (A1R == 1) lda(k); else if (A1R == 2) { lda(2 * k); SB(); lda(2 * k + 1); } else { lda(3 * (k >> 1) + (k & 1) * 2); if (!(k & 1)) { SB(); lda(3 * (k >> 1) + 1); } } }
#pragma unroll
        for (int pp = 0; pp < 3; ++pp) acc[pp] = mfma32(wf[st & 1][pp], yf[st], acc[pp]);
#pragma unroll
        for (int pp = 0; pp < 3; ++pp) { SGB_MFMA(1); SGB_DSR(2); }
        SB();
      }
      WACC(27, bar());
      STAMP(16);
#pragma unroll
      for (int pp = 0; pp < 3; ++pp) act_store(acc[pp], pp >= p0 ? pp + 4 : pp);
#pragma unroll
      for (int n = A1N; n < 24; ++n) { lda(n); SB(); }
    }
    SB();
    {
      f32x16 acc[4];
#pragma unroll
      for (int q = 0; q < 4; ++q)
#pragma unroll
        for (int r = 0; r < 16; ++r) acc[q][r] = 0.f;
      LAS char* d0 = L + (cw * 8192 + (4 * h + q4) * 128 + mrow * 32 + (lane & 3) * 8);
      LAS char* da = d0 + ((q4 >> 1) ^ kh) * 64; LAS char* db = d0 + ((q4 >> 1) ^ kh ^ 1) * 64;
      u32x4 wl[2][2], a2[12][2];
      auto lda2 = [&](int n) {
        const int s2 = n >> 1, t2 = n & 1, jj = (s2 / 3 + rotd) & 3, pp = s2 % 3;
        a2[s2][t2] = ld128_sc1(rws, (u32)(lane * 16), WS_ACT + (u32)(((2 * tt + (t2 ^ tb)) * 112 + 28 * cw + 7 * jj + (pp >= jj ? pp + 4 : pp)) * 1024));
      };
      LAS char* const dah = opq(da + 65536); LAS char* const dbh = opq(db + 65536);
      auto ldw = [&](int s, int bi) { const u32 o = wd_unit_off(s >> 2) + (s & 3) * 2048; wl[bi][0] = trd2(o < 65536 ? da + o : dah + (o - 65536), 1024); wl[bi][1] = trd2(o < 65536 ? db + o : dbh + (o - 65536), 1024); };
      ldw(0, 0); SB();
#pragma unroll
      for (int s = 0; s < 28; ++s) {
        if (s == 3) WACC(30, pubbar<(A1N < 24 ? 24 - A1N : 0) + (A1N <= 24 ? 3 : (A1N < 27 ? 27 - A1N : 0))>(16, rws, WS_FA2 + (u32)(T * 2048 + jt * 8), tlo, thi, tid));
        if ((s & 3) == 3 && s > 3 && s < 24) WACC(29, bar());
        if (!D0 && s == 12) {
          STAMP(17);
          wait_flags_from<4>(fl, rws, WS_FA2 + (u32)(T * 2048 + ((tt & 1) * 8 + cb) * 128 + cw * 32), lane, tlo, thi);
          STAMP(21);
#pragma unroll
          for (int n = 0; n < 12; ++n) { lda2(n); SB(); }
          SB();
        }
        if (s < 8 && 24 + s >= A1N) lda(24 + s);
        if (!D0 && s == 8) fl = peek_flags<4>(rws, WS_FA2 + (u32)(T * 2048 + ((tt & 1) * 8 + cb) * 128 + cw * 32), lane);
        if (!D0 && s >= 13 && s < 19) { lda2(12 + 2 * (s - 13)); SB(); lda2(13 + 2 * (s - 13)); }
        if (s + 1 < 28) ldw(s + 1, (s + 1) & 1);
        const u32x4 b0 = s < 16 ? af[s < 16 ? s : 0][0] : a2[s < 16 ? 0 : s - 16][0], b1 = s < 16 ? af[s < 16 ? s : 0][1] : a2[s < 16 ? 0 : s - 16][1];
        acc[0] = mfma32(wl[s & 1][0], b0, acc[0]); acc[2] = mfma32(wl[s & 1][1], b0, acc[2]);
        acc[1] = mfma32(wl[s & 1][0], b1, acc[1]); acc[3] = mfma32(wl[s & 1][1], b1, acc[3]);
        SGB_MFMA(1); SGB_DSR(2); SGB_MFMA(1); SGB_DSR(2); SGB_MFMA(2);
        SB();
      }
      STAMP(22);
      int ln2 = lane; asm volatile("" : "+v"(ln2));
#pragma unroll
      for (int i = 1; i < 4; ++i)
#pragma unroll
        for (int q = 0; q < 4; ++q) *(LAS f32x4*)(L + L_RED4 + ((i ^ cw) * 3 + i - 1) * 4096 + q * 1024 + ln2 * 16) = f32x4{acc[i][4 * q], acc[i][4 * q + 1], acc[i][4 * q + 2], acc[i][4 * q + 3]};
      f32x4 gb4[4]; f32x2 xa[8];
#pragma unroll
      for (int q = 0; q < 4; ++q) {
        gb4[q] = *(const LAS f32x4*)(L + L_GBV + (32 * kh + 8 * q + 4 * (ln2 >> 5)) * 4);
#pragma unroll
        for (int e = 0; e < 2; ++e)
          xa[2 * q + e] = __builtin_elementwise_fma(f32x2{gb4[q][2 * e], gb4[q][2 * e + 1]}, f32x2{acc[0][4 * q + 2 * e], acc[0][4 * q + 2 * e + 1]}, f32x2{bflo(x1p[2 * q + e]), bfhi(x1p[2 * q + e])});
      }
#pragma unroll
      for (int q = 0; q < 8; q += 4) asm volatile("" : "+v"(xa[q]), "+v"(xa[q + 1]), "+v"(xa[q + 2]), "+v"(xa[q + 3]));
      bar();
      u32 ob[8];
#pragma unroll
      for (int q = 0; q < 4; ++q) {
        const LAS char* rp = L + L_RED4 + cw * 3 * 4096 + q * 1024 + ln2 * 16;
        const f32x4 rs = (*(const LAS f32x4*)rp + *(const LAS f32x4*)(rp + 4096)) + *(const LAS f32x4*)(rp + 8192);
#pragma unroll
        for (int e = 0; e < 2; ++e) {
          const f32x2 v = __builtin_elementwise_fma(f32x2{gb4[q][2 * e], gb4[q][2 * e + 1]}, f32x2{rs[2 * e], rs[2 * e + 1]}, xa[2 * q + e]);
          ob[2 * q + e] = pk2(v[0], v[1]);
        }
      }
#pragma unroll
      for (int m = 0; m < 2; ++m) {
        auto r0 = __builtin_amdgcn_permlane32_swap(ob[4 * m], ob[4 * m + 2], false, false);
        auto r1 = __builtin_amdgcn_permlane32_swap(ob[4 * m + 1], ob[4 * m + 3], false, false);
        const u32 a0 = r0[0], b0 = r0[1], a1 = r1[0], b1 = r1[1];
        __builtin_amdgcn_raw_buffer_store_b128(u32x4{a0, a1, b0, b1}, rout, (u32)((64 * tt + 32 * tb + (ln2 & 31)) * 1024 + (64 * cb + 32 * kh + 16 * m + 8 * (ln2 >> 5)) * 2), 0, 0);
      }
    }
    STAMP(18);
  }
  if (isC && cw == 0 && b == ENCB) {
    const u32 target = stamp[0] + 6000u + (stamp[ENC] - stamp[0]);
    for (int sp = 0; sp < 2000000; ++sp) { if ((int)((u32)__builtin_amdgcn_s_memrealtime() - target) >= 0) break; __builtin_amdgcn_s_sleep(1); }
  }
}

__global__ __launch_bounds__(512, 2) void fused_block(Params p) {
  extern __shared__ __attribute__((aligned(16))) char smem_[];
  asm volatile("" :: "s"(p.x), "s"(p.wqkv), "s"(p.wo), "s"(p.wg), "s"(p.wu), "s"(p.wd), "s"(p.c), "s"(p.wmod), "s"(p.out), "s"(p.ws));
  int w = __builtin_amdgcn_readfirstlane(threadIdx.x >> 6);
  u32 stamp[31];
#pragma unroll
  for (int i = 0; i < 31; ++i) stamp[i] = 0;
  u32x4 wr[18]; u32 tlo, thi;
  front(p, (LAS char*)smem_, threadIdx.x, w, blockIdx.x, wr, tlo, thi STAMP_ARG);
  if (w >= 4) {
    int b = blockIdx.x, tid = threadIdx.x; asm volatile("; H role" : "+s"(b), "+s"(w)); asm volatile("; H role" : "+v"(tid));
    role_body<false>(p, (LAS char*)smem_, tid, w, b, wr, tlo, thi STAMP_ARG);
  } else {
    int b = blockIdx.x, tid = threadIdx.x; asm volatile("; C role" : "+s"(b), "+s"(w)); asm volatile("; C role" : "+v"(tid));
    role_body<true>(p, (LAS char*)smem_, tid, w, b, wr, tlo, thi STAMP_ARG);
  }
}

extern "C" void kernel_launch(void* const* d_in, const int* in_sizes, int n_in, void* d_out, int out_size, void* d_ws, size_t ws_size, hipStream_t stream) {
  Params p;
  p.x = d_in[0]; p.wqkv = d_in[1]; p.wo = d_in[2]; p.wg = d_in[4]; p.wu = d_in[5]; p.wd = d_in[6]; p.c = d_in[7]; p.wmod = d_in[8];
  p.out = d_out; p.ws = d_ws;
  (void)hipFuncSetAttribute((const void*)fused_block, hipFuncAttributeMaxDynamicSharedMemorySize, LDS_BYTES);
  fused_block<<<256, 512, LDS_BYTES, stream>>>(p);
}
```

```cpp
#include <hip/hip_runtime.h>
#include <stdint.h>

typedef unsigned u32;
typedef unsigned long long u64;
typedef u32 u32x2 __attribute__((ext_vector_type(2)));
typedef u32 u32x4 __attribute__((ext_vector_type(4)));
typedef short s16x4 __attribute__((ext_vector_type(4)));
typedef __bf16 bf16x2 __attribute__((ext_vector_type(2)));
typedef __bf16 bf16x8 __attribute__((ext_vector_type(8)));
typedef float f32x2 __attribute__((ext_vector_type(2)));
typedef float f32x4 __attribute__((ext_vector_type(4)));
typedef float f32x16 __attribute__((ext_vector_type(16)));
typedef __amdgpu_buffer_rsrc_t rsrc_t;
#define LAS __attribute__((address_space(3)))
#define DI __device__ __forceinline__

constexpr int S_ = 2048, D_ = 512, F_ = 1792;
constexpr u32 WS_E = 0, WS_MODG = 393216, WS_FU = 57344, WS_FO = 8192, WS_FY = 983040, WS_FA = 327680, WS_FA2 = 360448;
constexpr u32 WS_MU = 499712;
constexpr u32 WS_UT = 524288;
constexpr u32 WS_O = 1u << 20, WS_Y = 3u << 20, WS_ST = 5u << 20, WS_ACT = 6u << 20, WS_BYTES = 13u << 20;
constexpr u32 L_AIMG = 0, L_RW = 81920, L_W1S = 12288;
constexpr u32 L_LNP = 73728, L_PRED = L_LNP + 6400, L_RSNB = L_PRED + 1088;
constexpr u32 L_XRES = 65536, L_WO = 73728, L_RED2 = 139264;
constexpr u32 L_P1B = 8192, L_P2B = 6400, L_P2M = 3200;
constexpr u32 L_RED4 = 65536;
constexpr u32 L_MISC = 155648, L_G1 = L_MISC, L_S1 = L_MISC + 2048, L_FIX = L_MISC;
constexpr u32 L_SL = L_MISC + 4096, L_SCRAP = L_MISC + 6656;
constexpr u32 L_MU1 = L_MISC + 7168, L_GA = L_MU1 + 256, L_G2C = L_GA + 256, L_GBV = L_G2C + 256;
constexpr u32 LDS_BYTES = 163840;
__host__ __device__ constexpr u32 wd_unit_off(int u) { return u == 0 || u == 4 ? 102400u : (u == 1 || u == 5 ? 0u : (u == 2 || u == 6 ? 32768u : 65536u)); }

struct Params { const void *x, *wqkv, *wo, *wg, *wu, *wd, *c, *wmod; void *out; void *ws; };

DI rsrc_t mk(const void* p, u32 bytes) { return __builtin_amdgcn_make_buffer_rsrc((void*)p, 0, bytes, 0x00020000); }
DI float bflo(u32 v) { return __builtin_bit_cast(float, v << 16); }
DI float bfhi(u32 v) { return __builtin_bit_cast(float, v & 0xffff0000u); }
DI u32 pk2(float a, float b) { bf16x2 r; r[0] = (__bf16)a; r[1] = (__bf16)b; return __builtin_bit_cast(u32, r); }
DI float rbf(float a) { return bflo(pk2(a, a)); }
DI f32x4 mfma16(u32x4 a, u32x4 b, f32x4 c) { return __builtin_amdgcn_mfma_f32_16x16x32_bf16(__builtin_bit_cast(bf16x8, a), __builtin_bit_cast(bf16x8, b), c, 0, 0, 0); }
DI f32x4 mfma16k16(u32x2 a, u32x2 b, f32x4 c) { return __builtin_amdgcn_mfma_f32_16x16x16bf16_1k(__builtin_bit_cast(s16x4, a), __builtin_bit_cast(s16x4, b), c, 0, 0, 0); }
DI f32x16 mfma32(u32x4 a, u32x4 b, f32x16 c) { return __builtin_amdgcn_mfma_f32_32x32x16_bf16(__builtin_bit_cast(bf16x8, a), __builtin_bit_cast(bf16x8, b), c, 0, 0, 0); }
DI u32x2 trd(LAS char* p) { return __builtin_bit_cast(u32x2, __builtin_amdgcn_ds_read_tr16_b64_v4i16((LAS s16x4*)p)); }
DI u32x4 trd2(LAS char* p, int d) { u32x2 a = trd(p), b = trd(p + d); return u32x4{a[0], a[1], b[0], b[1]}; }
DI LAS char* opq(LAS char* p) { asm volatile("" : "+v"(p)); return p; }
DI void dma16(rsrc_t rs, u32 voff, u32 soff, u32 lds) {
  asm volatile("s_mov_b32 m0, %0\n\ts_nop 0\n\tbuffer_load_dwordx4 %1, %2, %3 offen lds" :: "s"(lds), "v"(voff), "s"(rs), "s"(soff) : "m0", "memory");
}
DI void ald128(u32x4& d, rsrc_t rs, u32 voff, u32 soff) {
  asm volatile("buffer_load_dwordx4 %0, %1, %2, %3 offen" : "=v"(d) : "v"(voff), "s"(rs), "s"(soff) : "memory");
}
DI void ald128_sc1(u32x4& d, rsrc_t rs, u32 voff, u32 soff) {
  asm volatile("buffer_load_dwordx4 %0, %1, %2, %3 offen sc1" : "=v"(d) : "v"(voff), "s"(rs), "s"(soff) : "memory");
}
DI void ald64_sc1(u32x2& d, rsrc_t rs, u32 voff, u32 soff) {
  asm volatile("buffer_load_dwordx2 %0, %1, %2, %3 offen sc1" : "=v"(d) : "v"(voff), "s"(rs), "s"(soff) : "memory");
}
DI void ald64(u32x2& d, rsrc_t rs, u32 voff, u32 soff) {
  asm volatile("buffer_load_dwordx2 %0, %1, %2, %3 offen" : "=v"(d) : "v"(voff), "s"(rs), "s"(soff) : "memory");
}
DI void warm(u32 lds, rsrc_t rs, u32 voff, u32 soff) {
  asm volatile("s_mov_b32 m0, %0\n\ts_nop 0\n\tbuffer_load_dword %1, %2, %3 offen sc1 lds" :: "s"(lds), "v"(voff), "s"(rs), "s"(soff) : "m0", "memory");
}
template <int N> DI void waitv() { asm volatile("s_waitcnt vmcnt(%c0)" :: "i"(N) : "memory"); }
template <int N> DI void waitv4(u32x4& a, u32x4& b, u32x4& c, u32x4& d) { asm volatile("s_waitcnt vmcnt(%c4)" : "+v"(a), "+v"(b), "+v"(c), "+v"(d) : "i"(N) : "memory"); }
template <int N> DI void waitv1(u32x4& a) { asm volatile("s_waitcnt vmcnt(%c1)" : "+v"(a) : "i"(N) : "memory"); }
DI void bar() { __builtin_amdgcn_sched_barrier(0); asm volatile("s_waitcnt lgkmcnt(0)\n\ts_barrier" ::: "memory"); __builtin_amdgcn_sched_barrier(0); }
DI u32x4 ld128_sc1(rsrc_t rs, u32 voff, u32 soff) { return __builtin_amdgcn_raw_buffer_load_b128(rs, voff, soff, 16); }
DI u32x2 ld64_sc1(rsrc_t rs, u32 voff, u32 soff) { return __builtin_amdgcn_raw_buffer_load_b64(rs, voff, soff, 16); }
DI void st128_sc1(u32x4 v, rsrc_t rs, u32 voff, u32 soff) { __builtin_amdgcn_raw_buffer_store_b128(v, rs, voff, soff, 16); }
DI void st64_sc1(u32x2 v, rsrc_t rs, u32 voff, u32 soff) { __builtin_amdgcn_raw_buffer_store_b64(v, rs, voff, soff, 16); }
DI float f_(u32 v) { return __builtin_bit_cast(float, v); }
DI u32 u_(float v) { return __builtin_bit_cast(u32, v); }
DI float swap16_add(float a, float b) { auto r = __builtin_amdgcn_permlane16_swap(u_(a), u_(b), false, false); u32 r0 = r[0], r1 = r[1]; return f_(r0) + f_(r1); }
DI float swap32_add(float a, float b) { auto r = __builtin_amdgcn_permlane32_swap(u_(a), u_(b), false, false); u32 r0 = r[0], r1 = r[1]; return f_(r0) + f_(r1); }
DI float mx2(float a, float b) { return __builtin_amdgcn_fmed3f(a, b, __builtin_inff()); }
template <int CTRL> DI float dpp_add(float a) { return a + f_(__builtin_amdgcn_update_dpp(0, u_(a), CTRL, 0xf, 0xf, true)); }

constexpr u64 SPIN_TICKS = 2000000;
DI bool spin_out(u64 t0) { asm volatile("" ::: "memory"); __builtin_amdgcn_s_sleep(1); return (__builtin_amdgcn_s_memrealtime() - t0) > SPIN_TICKS; }

#define SB() __builtin_amdgcn_sched_barrier(0)
#define SGB_MFMA(n) __builtin_amdgcn_sched_group_barrier(0x8, n, 0)
#define SGB_DSR(n) __builtin_amdgcn_sched_group_barrier(0x100, n, 0)

template <int YOUNGER> DI void pubbar(int NC, rsrc_t rws, u32 flag_off, u32 tlo, u32 thi, int tid) {
  waitv<YOUNGER>(); bar();
  if (tid < NC) st64_sc1(u32x2{tlo, thi}, rws, (u32)tid * 128u, flag_off);
}
template <int NF> DI u32x2 peek_flags(rsrc_t rws, u32 off, int lane) { return ld64_sc1(rws, (u32)(lane & (NF - 1)) * 8u, off); }
template <int NF> DI void wait_flags(rsrc_t rws, u32 off, int lane, u32 tlo, u32 thi) {
  const u32 vo = (u32)(lane & (NF - 1)) * 8u;
  const u64 t0 = __builtin_amdgcn_s_memrealtime();
  for (;;) {
    u32x2 f = ld64_sc1(rws, vo, off);
    if (__all(f[0] == tlo && f[1] == thi)) break;
    if (spin_out(t0)) break;
  }
  __builtin_amdgcn_fence(__ATOMIC_ACQUIRE, "wavefront");
}
DI bool gran_ok(u32x4 g, u32 glo, u32 ghi) { return (g[0] & 0xffu) == glo && g[1] == ghi && (g[2] & 0xffu) == glo && g[3] == ghi; }

template <int NF> DI void wait_flags_from(u32x2 seen, rsrc_t rws, u32 off, int lane, u32 tlo, u32 thi) {
  if (!__all(seen[0] == tlo && seen[1] == thi)) wait_flags<NF>(rws, off, lane, tlo, thi);
  __builtin_amdgcn_fence(__ATOMIC_ACQUIRE, "wavefront");
}
struct Gath { u32x4 q[4]; };
DI void gath_load(Gath& G, rsrc_t rws, u32 voff, u32 soff) {
#pragma unroll
  for (int k = 0; k < 4; ++k) G.q[k] = ld128_sc1(rws, voff, WS_MODG + soff + (u32)k * 24576u);
}
DI bool gath_ok(const Gath& G, u32 glo, u32 ghi) { return gran_ok(G.q[0], glo, ghi) && gran_ok(G.q[1], glo, ghi) && gran_ok(G.q[2], glo, ghi) && gran_ok(G.q[3], glo, ghi); }
DI u32 gath_val(const Gath& G) {
  const float a = (f_(G.q[0][0] & 0xffffff00u) + f_(G.q[1][0] & 0xffffff00u)) + (f_(G.q[2][0] & 0xffffff00u) + f_(G.q[3][0] & 0xffffff00u));
  const float b = (f_(G.q[0][2] & 0xffffff00u) + f_(G.q[1][2] & 0xffffff00u)) + (f_(G.q[2][2] & 0xffffff00u) + f_(G.q[3][2] & 0xffffff00u));
  return pk2(a, b);
}
DI void mod_issue(u32x4 (&wm)[4], u32x2& cv, rsrc_t rwm, rsrc_t rc, int mq, int mblk, int cw, int lane) {
    const int r0 = 128 * mq + 32 * cw;
#pragma unroll
    for (int i = 0; i < 4; ++i) ald128(wm[i], rwm, (u32)((lane >> 3) * 4 * 6144 + (lane & 7) * 16), (u32)((r0 + i) * 6144 + mblk * 128));
    ald64(cv, rc, (u32)((lane >> 3) * 8), (u32)(r0 * 2));
}
DI void mod_finish(LAS char* L, u32 pred, const u32x4 (&wm)[4], const u32x2& cv, u32 tlo, u32 thi, rsrc_t rws, int mq, int mblk, int cw, int lane) {
    const int g = lane >> 4, i16 = lane & 15;
    float a8[8];
#pragma unroll
    for (int e = 0; e < 8; ++e) a8[e] = 0.f;
#pragma unroll
    for (int i = 0; i < 4; ++i) {
      const float cf = (i & 1) ? bfhi(cv[i >> 1]) : bflo(cv[i >> 1]);
      const float sv = rbf(cf * __builtin_amdgcn_rcpf(1.f + __expf(-cf)));
#pragma unroll
      for (int e = 0; e < 4; ++e) { a8[2 * e] = fmaf(sv, bflo(wm[i][e]), a8[2 * e]); a8[2 * e + 1] = fmaf(sv, bfhi(wm[i][e]), a8[2 * e + 1]); }
    }
#pragma unroll
    for (int e = 0; e < 8; ++e) a8[e] = dpp_add<0x128>(a8[e]);
    float b4[4], b2[2];
#pragma unroll
    for (int e = 0; e < 4; ++e) b4[e] = swap16_add(a8[e], a8[e + 4]);
#pragma unroll
    for (int e = 0; e < 2; ++e) b2[e] = swap32_add(b4[e], b4[e + 2]);
    if (i16 < 8) *(LAS f32x2*)(L + pred + cw * 256 + (i16 * 8 + (g & 1) * 4 + (g >> 1) * 2) * 4) = f32x2{b2[0], b2[1]};
    asm volatile("s_waitcnt lgkmcnt(0)" ::: "memory");
    if (lane == 0) *(volatile LAS u32x2*)(L + pred + 1024 + cw * 8) = u32x2{tlo, thi};
    if (cw == 0) {
      const u64 t0 = __builtin_amdgcn_s_memrealtime();
      for (;;) {
        const u32x2 tg = *(volatile LAS u32x2*)(L + pred + 1024 + (lane & 3) * 8);
        if (__all(tg[0] == tlo && tg[1] == thi)) break;
        if ((__builtin_amdgcn_s_memrealtime() - t0) > SPIN_TICKS) break;
      }
      asm volatile("" ::: "memory");
      LAS float* s = (LAS float*)(L + pred) + lane;
      const float v = (s[0] + s[64]) + (s[128] + s[192]);
      st64_sc1(u32x2{((u_(v) + 0x80u) & 0xffffff00u) | (tlo & 0xffu), (tlo >> 8) | (thi << 24)}, rws, (u32)(lane * 8), WS_MODG + (u32)(mq * 24576 + mblk * 512));
    }
}
DI void front(const Params& p, LAS char* L, const int tid, const int w, const int b, u32x4 (&wr)[18], u32& tlo, u32& thi) {
  const bool isC = w < 4;
  f32x2 xh[10][4];
  const u32 Lb = (u32)(size_t)L;
  const int lane = tid & 63, cw = w & 3;
  const int xcd = b & 7, idx = b >> 3;
  const int tt = 8 * (xcd >> 1) + (idx >> 2), hgl = idx & 3, hg = 4 * (xcd & 1) + hgl;
  const rsrc_t rx = mk(p.x, S_ * D_ * 2), rw1 = mk(p.wqkv, D_ * 3 * D_ * 2), rwo = mk(p.wo, D_ * D_ * 2);
  const rsrc_t rwg = mk(p.wg, D_ * F_ * 2), rwu = mk(p.wu, D_ * F_ * 2), rwd = mk(p.wd, F_ * D_ * 2);
  const rsrc_t rc = mk(p.c, D_ * 2), rwm = mk(p.wmod, D_ * 6 * D_ * 2), rout = mk(p.out, S_ * D_ * 2), rws = mk(p.ws, WS_BYTES);
  const int g = lane >> 4, i16 = lane & 15, h = lane >> 5, tok = lane & 31;

  const int nrot = 2 * hgl, rot = 2 * (tt & 7);
  const u32 w1v = (u32)((lane >> 3) * 3072 + (((((lane >> 1) & 3) ^ ((lane >> 4) & 1)) ^ ((cw & 1) << 1)) * 32) + (lane & 1) * 16);
  {
    u64 pc; u32 len;
    asm volatile("s_getpc_b64 %0\n.Lfb_pc:\n\ts_mov_b32 %1, .Lfb_end-.Lfb_pc" : "=&s"(pc), "=s"(len));
    const rsrc_t rcode = mk((const void*)pc, len);
    warm(Lb + L_SCRAP, rcode, (u32)((idx * 8 + w) * 2 + (lane & 1)) * 128u, 0u);
  }
  u32x2 ev; ald64_sc1(ev, rws, 0u, WS_E);
  const bool prod = isC && idx < 8, prod2 = isC && idx >= 8 && idx < 24;
  const int mq = idx & 3, mblk = idx < 8 ? xcd * 2 + (idx >> 2) : 16 + xcd * 4 + ((idx - 8) >> 2);
  auto dma_w1 = [&](int i) {
    const int n = (i < 9 ? 6 : 0) + 2 * ((i % 9) / 3) + (w >> 2), pp = i % 3;
    dma16(rw1, w1v, (u32)((32 * ((n + rot) & 15) + 8 * cw) * 3072 + pp * 1024 + hg * 128), Lb + (u32)((i < 9 ? (n - 6) * L_W1S : L_RW + n * L_W1S) + pp * 4096 + cw * 1024));
  };
  u32x2 cv; u32x4 wm[4];
  if (prod) mod_issue(wm, cv, rwm, rc, mq, mblk, cw, lane);
  u32x4 xr[10];
#pragma unroll
  for (int n = 0; n < 10; ++n) {
    int r8 = n + nrot; r8 = r8 >= 10 ? r8 - 10 : r8;
    int row = 64 * tt - 16 + 8 * r8 + (lane >> 3); row = row < 0 ? 0 : row;
    ald128(xr[n], rx, (u32)(row * 1024 + (lane & 7) * 16), (u32)(w * 128));
  }
  if (prod) {
    asm volatile("s_waitcnt vmcnt(10)" : "+v"(wm[0]), "+v"(wm[1]), "+v"(wm[2]), "+v"(wm[3]), "+v"(cv), "+v"(ev) :: "memory");
    const u64 e1 = (((u64)__builtin_amdgcn_readfirstlane(ev[1]) << 32) | (u64)__builtin_amdgcn_readfirstlane(ev[0])) + 1ull;
    mod_finish(L, L_PRED, wm, cv, (u32)e1, (u32)(e1 >> 32), rws, mq, mblk, cw, lane);
  }
  {
    const int c8 = lane & 7, r8l = lane >> 3;
    float pv[10];
#pragma unroll
    for (int n = 0; n < 10; ++n) {
      waitv1<9>(xr[n]);
#pragma unroll
      for (int e = 0; e < 4; ++e) xh[n][e] = f32x2{bflo(xr[n][e]), bfhi(xr[n][e])};
      const f32x2 a1 = (xh[n][0] + xh[n][1]) + (xh[n][2] + xh[n][3]);
      const f32x2 a2 = __builtin_elementwise_fma(xh[n][0], xh[n][0], xh[n][1] * xh[n][1]) + __builtin_elementwise_fma(xh[n][2], xh[n][2], xh[n][3] * xh[n][3]);
      const float s1 = a1[0] + a1[1], s2 = a2[0] + a2[1];
      const float keep = (c8 & 1) ? s2 : s1, give = (c8 & 1) ? s1 : s2;
      float v = keep + f_(__builtin_amdgcn_update_dpp(0, u_(give), 0xB1, 0xf, 0xf, true));
      v = dpp_add<0x4E>(v);
      pv[n] = v + f_(__builtin_amdgcn_update_dpp(0, u_(v), 0x104, 0xf, 0xf, true));
      asm volatile("" : "+v"(pv[n])); dma_w1(n); SB();
    }
    asm volatile("" : "+v"(ev));
    {
      const u64 e1 = (((u64)__builtin_amdgcn_readfirstlane(ev[1]) << 32) | (u64)__builtin_amdgcn_readfirstlane(ev[0])) + 1ull;
      tlo = (u32)e1; thi = (u32)(e1 >> 32);
    }
    if (prod2) mod_issue(wm, cv, rwm, rc, mq, mblk, cw, lane);
    {
      const rsrc_t rt = w == 0 ? rwo : w == 1 ? rwg : w == 2 ? rwu : w == 3 ? rwd : w == 4 ? rout : rws;
      warm(Lb + L_SCRAP, rt, (u32)lane << 20, 0u);
    }
    if (c8 < 2) {
#pragma unroll
      for (int n = 0; n < 10; ++n) *(LAS float*)(L + L_LNP + (n * 8 + r8l) * 80 + (w * 2 + c8) * 4) = pv[n];
    }
    bar();
    f32x4 qa[2][4];
#pragma unroll
    for (int k = 0; k < 2; ++k)
#pragma unroll
      for (int e = 0; e < 4; ++e) qa[k][e] = *(const LAS f32x4*)(L + L_LNP + (k ? 64 + i16 : lane) * 80 + e * 16);
    dma_w1(10);
    float mus[2];
#pragma unroll
    for (int k = 0; k < 2; ++k) {
      const f32x4 a = (qa[k][0] + qa[k][1]) + (qa[k][2] + qa[k][3]);
      const float mu = (a[0] + a[2]) * (1.f / 512.f);
      const float var = fmaxf((a[1] + a[3]) * (1.f / 512.f) - mu * mu, 0.f);
      const float rs = __builtin_amdgcn_rcpf(rbf(__builtin_amdgcn_sqrtf(rbf(rbf(var) + 1e-5f))));
      *(LAS f32x2*)(L + L_RSNB + (k ? 64 + i16 : lane) * 8) = f32x2{rs, -rbf(mu) * rs};
      mus[k] = mu;
    }
    asm volatile("" ::: "memory");
    f32x2 rn[10];
#pragma unroll
    for (int n = 0; n < 10; ++n) rn[n] = *(const LAS f32x2*)(L + L_RSNB + (n * 8 + r8l) * 8);
    if (w == 0) {
#pragma unroll
      for (int k = 0; k < 2; ++k) { int r = (k ? 64 + i16 : lane) + 8 * nrot; r = r >= 80 ? r - 80 : r; ((LAS float*)(L + L_MU1 - 64))[r] = mus[k]; }
    }
#pragma unroll
    for (int n = 0; n < 10; ++n) {
#pragma unroll
      for (int e = 0; e < 4; ++e) xh[n][e] = __builtin_elementwise_fma(xh[n][e], f32x2{rn[n][0], rn[n][0]}, f32x2{rn[n][1], rn[n][1]});
      if (n < 7) { asm volatile("" : "+v"(xh[n][0]), "+v"(xh[n][1]), "+v"(xh[n][2]), "+v"(xh[n][3])); dma_w1(11 + n); SB(); }
    }
  }
#pragma unroll
  for (int n = 0; n < 10; ++n) asm volatile("" : "+v"(xh[n][0]), "+v"(xh[n][1]), "+v"(xh[n][2]), "+v"(xh[n][3]));
  const u32 glo = tlo & 0xffu, ghi = (tlo >> 8) | (thi << 24);
  const u32 gvo = (u32)(w * 512 + tok * 16 + h * 4096);
  Gath g1_;
#pragma unroll
  for (int k = 0; k < 4; ++k) ald128_sc1(g1_.q[k], rws, gvo, WS_MODG + (u32)k * 24576u);
  if (prod2) {
    asm volatile("s_waitcnt vmcnt(12)" : "+v"(wm[0]), "+v"(wm[1]), "+v"(wm[2]), "+v"(wm[3]), "+v"(cv) :: "memory");
    mod_finish(L, L_PRED, wm, cv, tlo, thi, rws, mq, mblk, cw, lane);
  }
  waitv<13>();
  bar();
  if (isC) {
    LAS char* w1rd = L + ((8 * g + (i16 >> 2)) * 128 + ((cw ^ (((i16 >> 3) & 1) | ((g & 1) << 1))) * 32) + (lane & 3) * 8);

#pragma unroll
    for (int n = 0; n < 6; ++n)
#pragma unroll
      for (int pp = 0; pp < 3; ++pp) wr[3 * n + pp] = trd2(w1rd + n * L_W1S + pp * 4096, 512);
  }
  waitv4<0>(g1_.q[0], g1_.q[1], g1_.q[2], g1_.q[3]);
  if (!__all(gath_ok(g1_, glo, ghi))) {
    const u64 t0 = __builtin_amdgcn_s_memrealtime();
    for (;;) {
      gath_load(g1_, rws, gvo, 0u);
      if (__all(gath_ok(g1_, glo, ghi))) break;
      if (spin_out(t0)) break;
    }
  }
  {
    const u32 v2 = gath_val(g1_);
    const float a = bflo(v2), c = bfhi(v2);
    *(LAS f32x2*)(L + (h ? L_G1 : L_S1) + (w * 32 + tok) * 8) = h ? f32x2{rbf(1.f + a), rbf(1.f + c)} : f32x2{a, c};
  }
  asm volatile("" ::: "memory");
  const LAS f32x4* gp = (const LAS f32x4*)(L + L_G1 + (w * 64 + (lane & 7) * 8) * 4);
  const LAS f32x4* sp = (const LAS f32x4*)(L + L_S1 + (w * 64 + (lane & 7) * 8) * 4);
  const f32x4 g0 = gp[0], g1 = gp[1], s0 = sp[0], s1 = sp[1];
  bar();
  {
    const int c8 = lane & 7, r8l = lane >> 3;
    LAS char* ab = L + L_AIMG + (2 * w + (c8 >> 2)) * 1024 + (c8 & 3) * 256 + ((r8l ^ c8) * 16);
#pragma unroll
    for (int n = 0; n < 10; ++n) {
      const f32x2 h0 = __builtin_elementwise_fma(xh[n][0], f32x2{g0[0], g0[1]}, f32x2{s0[0], s0[1]}), h1 = __builtin_elementwise_fma(xh[n][1], f32x2{g0[2], g0[3]}, f32x2{s0[2], s0[3]});
      const f32x2 h2 = __builtin_elementwise_fma(xh[n][2], f32x2{g1[0], g1[1]}, f32x2{s1[0], s1[1]}), h3 = __builtin_elementwise_fma(xh[n][3], f32x2{g1[2], g1[3]}, f32x2{s1[2], s1[3]});
      const u32x4 o = u32x4{pk2(h0[0], h0[1]), pk2(h1[0], h1[1]), pk2(h2[0], h2[1]), pk2(h3[0], h3[1])};
      int r8 = n + nrot; r8 = r8 >= 10 ? r8 - 10 : r8;
      *(LAS u32x4*)(ab + (r8 >> 1) * 16384 + (r8 & 1) * 128) = o;
    }
  }
  bar();
}

template <bool isC> DI void role_body(const Params& p, LAS char* L, const int tid, const int w, const int b, u32x4 (&wr)[18], const u32 tlo, const u32 thi) {
  const u32 Lb = (u32)(size_t)L;
  const int lane = tid & 63, cw = w & 3;
  const int xcd = b & 7, idx = b >> 3;
  const int tt = 8 * (xcd >> 1) + (idx >> 2), hgl = idx & 3, hg = 4 * (xcd & 1) + hgl;
  const int jt = 8 * (xcd & 1) + 4 * (tt & 1) + hgl;
  const int T = tt >> 1, cb = hg;
  const rsrc_t rx = mk(p.x, S_ * D_ * 2), rw1 = mk(p.wqkv, D_ * 3 * D_ * 2), rwo = mk(p.wo, D_ * D_ * 2);
  const rsrc_t rwg = mk(p.wg, D_ * F_ * 2), rwu = mk(p.wu, D_ * F_ * 2), rwd = mk(p.wd, F_ * D_ * 2);
  const rsrc_t rout = mk(p.out, S_ * D_ * 2), rws = mk(p.ws, WS_BYTES);
  const int g = lane >> 4, i16 = lane & 15, h = lane >> 5, tok = lane & 31, t4 = tid & 255;

  const int rot = 2 * (tt & 7);
  const u32 w1v = (u32)((lane >> 3) * 3072 + (((((lane >> 1) & 3) ^ ((lane >> 4) & 1)) ^ ((cw & 1) << 1)) * 32) + (lane & 1) * 16);
  LAS char* w1rd = L + ((8 * g + (i16 >> 2)) * 128 + ((cw ^ (((i16 >> 3) & 1) | ((g & 1) << 1))) * 32) + (lane & 3) * 8);
  const u32 glo = tlo & 0xffu, ghi = (tlo >> 8) | (thi << 24);
  const int p0 = jt & 3, rotg = (4 * (T & 3) + 2 * (jt & 7)) & 15, rotd = (tt + hgl) & 3;
  const u32 wov = (u32)((lane >> 3) * 1024 + ((((lane >> 2) & 1) ^ ((lane >> 4) & 1)) * 64) + (lane & 3) * 16);
  const int mrow = (lane >> 4) & 1, q4 = i16 >> 2;
  const u32 p1base = (u32)(mrow * 4096 + (4 * h + q4) * 128 + (lane & 3) * 8), p1x = (u32)((q4 >> 1) | (mrow << 1));
  const u32 p2base = (u32)(mrow * L_P2M + (4 * h + q4) * 96 + (lane & 3) * 8);

  if constexpr (!isC) {
    const u32 gch = h ? (u32)((cw == 0 ? 1024 : cw == 1 ? 2048 : 2560) + 64 * cb + 2 * tok) : (u32)((lane & 16 ? 1536 : 2048) + 32 * T + 2 * i16);
    Gath g2_;
#pragma unroll
    for (int k = 0; k < 4; ++k) ald128_sc1(g2_.q[k], rws, gch * 8u, WS_MODG + (u32)k * 24576u);
    const rsrc_t rgu = (cw & 1) ? rwu : rwg;
    const int uc = 7 * (cw >> 1) + ((lane & 7) < 7 ? (lane & 7) : 6);
    u32x4 uw[4];
    u32 uso = (u32)(T * (32 * 3584) + jt * 224); asm volatile("" : "+s"(uso));
#pragma unroll
    for (int i = 0; i < 4; ++i) ald128(uw[i], rgu, (u32)((lane >> 3) * 3584 + uc * 16), uso + (u32)(8 * i * 3584));
    bar();
#pragma unroll 1
    for (int n = 0; n < 4; ++n)
#pragma unroll
      for (int pp = 0; pp < 3; ++pp) dma16(rw1, w1v, (u32)((32 * ((12 + n + rot) & 15) + 8 * cw) * 3072 + pp * 1024 + hg * 128), Lb + (u32)(L_RW + n * L_W1S + pp * 4096 + cw * 1024));
    warm(Lb + L_SCRAP, cw == 0 ? rwg : cw == 1 ? rwu : cw == 2 ? rwd : rwo, (u32)(b + 256 * lane) * 128u, 0u);
    {
      waitv4<16>(g2_.q[0], g2_.q[1], g2_.q[2], g2_.q[3]);
      if (!__all(gath_ok(g2_, glo, ghi))) {
        const u64 t0 = __builtin_amdgcn_s_memrealtime();
        for (;;) {
          gath_load(g2_, rws, gch * 8u, 0u);
          if (__all(gath_ok(g2_, glo, ghi))) break;
          if (spin_out(t0)) break;
        }
      }
      const u32 v2 = gath_val(g2_);
      const bool one = h ? cw == 1 : !(lane & 16);
      const float a = bflo(v2), c = bfhi(v2);
      const u32 dst = h ? (cw == 0 ? L_GA : cw == 1 ? L_G2C : cw == 2 ? L_GBV : L_SCRAP + 256) + tok * 8 : L_SL + cw * 256 + tok * 8;
      *(LAS f32x2*)(L + dst) = one ? f32x2{rbf(1.f + a), rbf(1.f + c)} : f32x2{a, c};
    }
    waitv<1>(); bar();
    {
      asm volatile("" : "+v"(uw[0]), "+v"(uw[1]), "+v"(uw[2]), "+v"(uw[3]));
      float a[16];
#pragma unroll
      for (int e = 0; e < 16; ++e) a[e] = 0.f;
#pragma unroll
      for (int i = 0; i < 4; ++i) {
        const float gk = ((const LAS float*)(L + L_SL + cw * 256))[8 * i + (lane >> 3)], sk = ((const LAS float*)(L + L_SL + cw * 256))[32 + 8 * i + (lane >> 3)];
#pragma unroll
        for (int e = 0; e < 4; ++e) {
          const float lo = bflo(uw[i][e]), hi = bfhi(uw[i][e]);
          a[2 * e] = fmaf(gk, lo, a[2 * e]); a[2 * e + 1] = fmaf(gk, hi, a[2 * e + 1]);
          a[8 + 2 * e] = fmaf(sk, lo, a[8 + 2 * e]); a[9 + 2 * e] = fmaf(sk, hi, a[9 + 2 * e]);
        }
      }
#pragma unroll
      for (int e = 0; e < 16; ++e) a[e] = dpp_add<0x128>(a[e]);
      float b8[8], ut4[4];
#pragma unroll
      for (int e = 0; e < 8; ++e) b8[e] = swap16_add(a[e], a[e + 8]);
#pragma unroll
      for (int e = 0; e < 4; ++e) ut4[e] = swap32_add(b8[e], b8[e + 4]);
      auto r0 = __builtin_amdgcn_permlane16_swap(u_(ut4[0]), u_(ut4[2]), false, false);
      auto r1 = __builtin_amdgcn_permlane16_swap(u_(ut4[1]), u_(ut4[3]), false, false);
      const u32 a0 = r0[0], b0 = r0[1], a1 = r1[0], b1 = r1[1];
      const int pr = uc >> 1, slot = pr >= p0 && pr < p0 + 4 ? pr - p0 : (pr < p0 ? 4 + pr : pr);
      if (i16 < 7) st128_sc1(u32x4{a0, b0, a1, b1}, rws, (u32)((slot * 32 + (cw & 1) * 16 + (uc & 1) * 8 + h * 4 + (g & 1) * 2) * 8), WS_UT + (u32)((jt * 16 + T) * 1792));
    }
    bar();
#pragma unroll 1
    for (int q = 0; q < 16; ++q) { const int qq = (q + rot) & 15; dma16(rwo, wov, (u32)((128 * cw + 8 * qq) * 1024 + cb * 128), Lb + (u32)(L_WO + (16 * cw + qq) * 1024)); }
#pragma unroll
    for (int q = 0; q < 2; ++q) dma16(rx, (u32)((lane >> 3) * 1024 + (lane & 7) * 16), (u32)((64 * tt + 16 * cw + 8 * q) * 1024 + cb * 128), Lb + (u32)(L_XRES + (2 * cw + q) * 1024));
    const u32 p1v = (u32)((lane >> 3) * 3584 + ((((lane >> 1) & 3) ^ ((lane >> 4) & 1)) * 32) + (lane & 1) * 16);
    auto dma_p1 = [&](int k) {
      const u32 so = (u32)((32 * ((k + rotg) & 15) + 8 * cw) * 3584 + jt * 224 + p0 * 32);
      dma16(rwg, p1v, so, Lb + (u32)(k * L_P1B + cw * 1024));
      dma16(rwu, p1v ^ 64u, so, Lb + (u32)(k * L_P1B + 4096 + cw * 1024));
    };
    dma_p1(0); dma_p1(1);
    waitv<22>(); bar();
    if (tid == 256) st64_sc1(u32x2{tlo, thi}, rws, 0u, WS_FU + (u32)(jt * 256 + T * 8));
    waitv<4>(); bar();
#pragma unroll 1
    for (int k = 2; k < 8; ++k) dma_p1(k);
    u32x2 fu; ald64_sc1(fu, rws, (u32)(lane & 15) * 8u, WS_FU + (u32)(jt * 256));
    bar();
#pragma unroll 1
    for (int k = 8; k < 16; ++k) dma_p1(k);
    waitv<16>(); bar();
    {
      asm volatile("" : "+v"(fu));
      wait_flags_from<16>(fu, rws, WS_FU + (u32)(jt * 256), lane, tlo, thi);
      u32x2 pt[16];
#pragma unroll
      for (int k = 0; k < 16; ++k) ald64_sc1(pt[k], rws, (u32)(t4 < 224 ? t4 : 0) * 8u, WS_UT + (u32)((jt * 16 + k) * 1792));
      SB();
      float uu = 0.f, tt_ = 0.f;
      waitv<0>();
#pragma unroll
      for (int k = 0; k < 16; ++k) { asm volatile("" : "+v"(pt[k])); uu += f_(pt[k][0]); tt_ += f_(pt[k][1]); }
      const u32 hi = pk2(uu, tt_), lo = pk2(uu - bflo(hi), tt_ - bfhi(hi));
      const u32 u1 = hi & 0xffffu, t1 = hi >> 16, u2 = lo & 0xffffu, t2 = lo >> 16;
      const u32x4 o = u32x4{u1 | (u1 << 16), u2 | (u2 << 16), t1 | (t1 << 16), t2 | (t2 << 16)};
      if (t4 < 224) { LAS char* d = L + L_FIX + (t4 >> 5) * 1024 + (t4 & 31) * 16; *(LAS u32x4*)d = o; *(LAS u32x4*)(d + 512) = o; }
    }
    u32 p2v[3];
#pragma unroll
    for (int i = 0; i < 3; ++i) { const int n = 64 * i + lane, row = n / 6, c = n - row * 6; p2v[i] = (u32)(row * 3584 + c * 16 + ((c >> 1) >= p0 ? 128 : 0)); }
    auto dma_p2 = [&](int k0, int k1) {
#pragma unroll 1
      for (int k = k0; k < k1; ++k) {
        const int pos = 2 * k + (cw >> 1);
#pragma unroll
        for (int i = 0; i < 3; ++i) dma16(rgu, p2v[i], (u32)(((pos + rotg) & 15) * 32 * 3584 + jt * 224), Lb + (u32)(pos * L_P2B + (cw & 1) * L_P2M + i * 1024));
      }
    };
    auto dma_wd = [&](int u) {
#pragma unroll
      for (int q = 0; q < 8; ++q) {
        const int n = 8 * (u - 4) + q;
        const int jj = ((u < 4 ? u : n / 6) + rotd) & 3, r6 = n % 6;
        const int row = u < 4 ? 112 * jj + 16 * jj + 8 * q : 112 * jj + 8 * r6 + ((r6 >> 1) >= jj ? 64 : 0);
        dma16(rwd, wov, (u32)((448 * cw + row) * 1024 + cb * 128), Lb + (u32)(wd_unit_off(u) + cw * 8192 + q * 1024));
      }
    };
    waitv<0>(); bar();
    dma_p2(0, 5);
    if (b == 0 && cw == 3) {
      const u64 t0 = __builtin_amdgcn_s_memrealtime();
      for (;;) {
        bool ok = true;
#pragma unroll
        for (int k = 0; k < 4; ++k) { const int f = 64 * k + lane; const u32x2 v = ld64_sc1(rws, (u32)((f >> 3) * 1024 + (f & 7) * 8), WS_FO); ok = ok && v[0] == tlo && v[1] == thi; }
        if (__all(ok)) break;
        if (spin_out(t0)) break;
      }
      if (lane == 0) st64_sc1(u32x2{tlo, thi}, rws, 0u, WS_E);
    }
    waitv<0>(); bar();
    dma_p2(5, 8);
    bar();
    dma_wd(0);
    waitv<8>(); bar();
    dma_wd(1);
    bar();
    dma_wd(2);
    waitv<16>(); bar();
    dma_wd(3);
    bar();
    waitv<16>(); bar();
    dma_wd(4);
    waitv<16>(); bar();
    dma_wd(5);
    waitv<16>(); bar();
    dma_wd(6);
    waitv<16>(); bar();
    waitv<8>(); bar();
    waitv<0>(); bar();
    bar();
    asm volatile(".Lfb_end:");
  } else {
    f32x4 m0, m1, m0f;
    f32x4 Kt[5], Qt[4], Vn[5];
#pragma unroll
    for (int r = 0; r < 5; ++r) { Kt[r] = f32x4{0, 0, 0, 0}; Vn[r] = f32x4{0, 0, 0, 0}; if (r < 4) Qt[r] = f32x4{0, 0, 0, 0}; }
    {
      u32x4 a[2][5], wl[2][3];
      LAS char* const w1a = opq(w1rd + L_RW); LAS char* const w1b = opq(w1rd + L_RW + 4 * L_W1S);
      auto ld = [&](int n, int bi) {
        LAS char* ab = L + L_AIMG + ((n + rot) & 15) * 1024 + g * 256 + ((i16 ^ (((n & 1) << 2) | g)) * 16);
#pragma unroll
        for (int rb = 0; rb < 5; ++rb) a[bi][rb] = *(const LAS u32x4*)(ab + rb * 16384);
        if (n < 6 || n >= 12) {
#pragma unroll
          for (int pp = 0; pp < 3; ++pp) wl[bi][pp] = trd2((n % 6 < 4 ? w1a : w1b) + (n % 6 % 4) * L_W1S + pp * 4096, 512);
        }
      };
      ld(0, 0); SB();
#pragma unroll
      for (int n = 0; n < 16; ++n) {
        if (n == 3) bar();
        if (n == 8) {
          int lm = lane; asm volatile("" : "+v"(lm));
#pragma unroll
          for (int r = 0; r < 4; ++r) {
            const int d1 = (lm & 15) - 4 * (lm >> 4) - r, d0 = d1 + 16;
            m0[r] = (d0 <= 14 && !(d0 & 1)) ? 0.f : -1e30f;
            m1[r] = (d1 >= 0 && d1 <= 14 && !(d1 & 1)) ? 0.f : -1e30f;
            m0f[r] = tt == 0 ? -1e30f : m0[r];
          }
        }
        if (n == 9) asm volatile("" : "+v"(m0), "+v"(m1), "+v"(m0f));
        if (n == 11) bar();
        if (n < 15) ld(n + 1, (n + 1) & 1);
        const bool rg = n >= 6 && n < 12;
        const u32x4 wq = rg ? wr[rg ? 3 * (n - 6) : 0] : wl[n & 1][0], wk = rg ? wr[rg ? 3 * (n - 6) + 1 : 0] : wl[n & 1][1], wv = rg ? wr[rg ? 3 * (n - 6) + 2 : 0] : wl[n & 1][2];
#pragma unroll
        for (int rb = 0; rb < 5; ++rb) {
          Kt[rb] = mfma16(wk, a[n & 1][rb], Kt[rb]);
          Vn[rb] = mfma16(a[n & 1][rb], wv, Vn[rb]);
          if (rb > 0) Qt[rb - 1] = mfma16(wq, a[n & 1][rb], Qt[rb - 1]);
        }
#pragma unroll
        for (int k = 0; k < 11; ++k) { SGB_MFMA(1); SGB_DSR(1); }
        SGB_MFMA(3);
        SB();
      }
    }
    bar();
    const int tb = cw & 1, kh = cw >> 1;
    {
      u32x2 kp[5], vp[5], qp[4];
#pragma unroll
      for (int r = 0; r < 5; ++r) { kp[r] = u32x2{pk2(Kt[r][0], Kt[r][1]), pk2(Kt[r][2], Kt[r][3])}; if (r < 4) qp[r] = u32x2{pk2(Qt[r][0], Qt[r][1]), pk2(Qt[r][2], Qt[r][3])}; }
      f32x4 s0[4], s1[4];
#pragma unroll
      for (int qb = 0; qb < 4; ++qb) { s0[qb] = mfma16k16(kp[qb], qp[qb], qb == 0 ? m0f : m0); s1[qb] = mfma16k16(kp[qb + 1], qp[qb], m1); }
#pragma unroll
      for (int r = 0; r < 5; ++r) vp[r] = u32x2{pk2(Vn[r][0], Vn[r][1]), pk2(Vn[r][2], Vn[r][3])};
      float mx[4];
#pragma unroll
      for (int qb = 0; qb < 4; ++qb) mx[qb] = mx2(mx2(mx2(s0[qb][0], s0[qb][1]), mx2(s0[qb][2], s0[qb][3])), mx2(mx2(s1[qb][0], s1[qb][1]), mx2(s1[qb][2], s1[qb][3])));
#pragma unroll
      for (int qb = 0; qb < 4; ++qb) { auto r = __builtin_amdgcn_permlane16_swap(u_(mx[qb]), u_(mx[qb]), false, false); const u32 r0 = r[0], r1 = r[1]; mx[qb] = mx2(f_(r0), f_(r1)); }
#pragma unroll
      for (int qb = 0; qb < 4; ++qb) { auto r = __builtin_amdgcn_permlane32_swap(u_(mx[qb]), u_(mx[qb]), false, false); const u32 r0 = r[0], r1 = r[1]; mx[qb] = mx2(f_(r0), f_(r1)); }
      const float cs = 0.25f * 1.44269504088896f;
      const u32x4 ones = u32x4{0x3f803f80u, 0x3f803f80u, 0x3f803f80u, 0x3f803f80u};
      const f32x4 z = f32x4{0, 0, 0, 0};
      f32x4 ot[4], sm[4];
#pragma unroll
      for (int qb = 0; qb < 4; ++qb) {
        const float nm = -mx[qb] * cs;
        float e0[4], e1[4];
#pragma unroll
        for (int r = 0; r < 4; ++r) { e0[r] = __builtin_amdgcn_exp2f(fmaf(s0[qb][r], cs, nm)); e1[r] = __builtin_amdgcn_exp2f(fmaf(s1[qb][r], cs, nm)); }
        const u32x4 pf = u32x4{pk2(e0[0], e0[1]), pk2(e0[2], e0[3]), pk2(e1[0], e1[1]), pk2(e1[2], e1[3])};
        sm[qb] = mfma16(ones, pf, z);
        ot[qb] = mfma16(u32x4{vp[qb][0], vp[qb][1], vp[qb + 1][0], vp[qb + 1][1]}, pf, z);
      }
      u32x2 op[4];
#pragma unroll
      for (int qb = 0; qb < 4; ++qb) {
        const float ri = __builtin_amdgcn_rcpf(sm[qb][0]);
        op[qb] = u32x2{pk2(ot[qb][0] * ri, ot[qb][1] * ri), pk2(ot[qb][2] * ri, ot[qb][3] * ri)};
      }
#pragma unroll
      for (int t2 = 0; t2 < 2; ++t2) {
        auto r0 = __builtin_amdgcn_permlane16_swap(op[2 * t2][0], op[2 * t2 + 1][0], false, false);
        auto r1 = __builtin_amdgcn_permlane16_swap(op[2 * t2][1], op[2 * t2 + 1][1], false, false);
        const u32 a0 = r0[0], b0 = r0[1], a1 = r1[0], b1 = r1[1];
        st128_sc1(u32x4{a0, a1, b0, b1}, rws, (u32)(lane * 16), WS_O + (u32)(((2 * tt + t2) * 32 + 4 * hg + cw) * 1024));
      }
    }
    pubbar<0>(8, rws, WS_FO + (u32)(tt * 1024 + hg * 8), tlo, thi, tid);
    wait_flags<8>(rws, WS_FO + (u32)(tt * 1024 + cb * 128), lane, tlo, thi);
    u32 x1p[8];
    {
      u32x4 of[16];
      const u32 ob = WS_O + (u32)(((2 * tt + tb) * 32 + 16 * kh) * 1024);
#pragma unroll
      for (int s = 0; s < 16; ++s) { of[s] = ld128_sc1(rws, (u32)(lane * 16), ob + (u32)(((s + 4 * hgl) & 15) * 1024)); SB(); }
      SB();
      bar();
      LAS char* q0 = L + (L_WO + (256 * kh + 8 * h + q4) * 128 + mrow * 32 + (lane & 3) * 8);
      LAS char* qa = q0 + ((q4 >> 1) ^ kh) * 64; LAS char* qb = q0 + ((q4 >> 1) ^ kh ^ 1) * 64;
      f32x16 a0, a1;
#pragma unroll
      for (int r = 0; r < 16; ++r) { a0[r] = 0.f; a1[r] = 0.f; }
      u32x4 wl[2][2];
      wl[0][0] = trd2(qa + hgl * 8192, 512); wl[0][1] = trd2(qb + hgl * 8192, 512); SB();
#pragma unroll
      for (int s = 0; s < 16; ++s) {
        if (s < 15) { const int o2 = ((((s + 1) >> 2) + hgl) & 3) * 8192 + ((s + 1) & 3) * 2048; wl[(s + 1) & 1][0] = trd2(qa + o2, 512); wl[(s + 1) & 1][1] = trd2(qb + o2, 512); }
        a0 = mfma32(wl[s & 1][0], of[s], a0); a1 = mfma32(wl[s & 1][1], of[s], a1);
        SGB_MFMA(1); SGB_DSR(2); SGB_MFMA(1); SGB_DSR(2);
        SB();
      }
      u32x2 xres[4]; f32x4 ga4[4], g24[4];
#pragma unroll
      for (int q = 0; q < 4; ++q) {
        xres[q] = *(const LAS u32x2*)(L + L_XRES + (32 * tb + tok) * 128 + (32 * kh + 8 * q + 4 * h) * 2);
        ga4[q] = *(const LAS f32x4*)(L + L_GA + (32 * kh + 8 * q + 4 * h) * 4); g24[q] = *(const LAS f32x4*)(L + L_G2C + (32 * kh + 8 * q + 4 * h) * 4);
      }
      const float mu1 = ((const LAS float*)(L + L_MU1))[32 * tb + tok];
#pragma unroll
      for (int q = 0; q < 4; ++q) *(LAS f32x4*)(L + L_RED2 + cw * 4096 + q * 1024 + lane * 16) = f32x4{a1[4 * q], a1[4 * q + 1], a1[4 * q + 2], a1[4 * q + 3]};
      f32x2 xa[8];
#pragma unroll
      for (int q = 0; q < 4; ++q) {
        xa[2 * q] = __builtin_elementwise_fma(f32x2{ga4[q][0], ga4[q][1]}, f32x2{a0[4 * q], a0[4 * q + 1]}, f32x2{bflo(xres[q][0]), bfhi(xres[q][0])});
        xa[2 * q + 1] = __builtin_elementwise_fma(f32x2{ga4[q][2], ga4[q][3]}, f32x2{a0[4 * q + 2], a0[4 * q + 3]}, f32x2{bflo(xres[q][1]), bfhi(xres[q][1])});
      }
#pragma unroll
      for (int q = 0; q < 8; q += 4) asm volatile("" : "+v"(xa[q]), "+v"(xa[q + 1]), "+v"(xa[q + 2]), "+v"(xa[q + 3]));
      bar();
      f32x2 sv1 = f32x2{0.f, 0.f}, sv2 = f32x2{0.f, 0.f}; u32 yp[8];
#pragma unroll
      for (int q = 0; q < 4; ++q) {
        const f32x4 o4 = *(const LAS f32x4*)(L + L_RED2 + (cw ^ 2) * 4096 + q * 1024 + lane * 16);
#pragma unroll
        for (int e = 0; e < 2; ++e) {
          const f32x2 v = __builtin_elementwise_fma(f32x2{ga4[q][2 * e], ga4[q][2 * e + 1]}, f32x2{o4[2 * e], o4[2 * e + 1]}, xa[2 * q + e]);
          x1p[2 * q + e] = pk2(v[0], v[1]);
          const f32x2 d = f32x2{bflo(x1p[2 * q + e]), bfhi(x1p[2 * q + e])} - f32x2{mu1, mu1};
          sv1 += d; sv2 = __builtin_elementwise_fma(d, d, sv2);
          const f32x2 y = d * f32x2{g24[q][2 * e], g24[q][2 * e + 1]};
          yp[2 * q + e] = pk2(y[0], y[1]);
        }
      }
      float s1 = sv1[0] + sv1[1], s2 = sv2[0] + sv2[1];
      const u32 yb = WS_Y + (u32)(((2 * tt + tb) * 32 + 4 * cb + 2 * kh) * 1024);
      st128_sc1(u32x4{yp[0], yp[1], yp[2], yp[3]}, rws, (u32)(lane * 16), yb);
      st128_sc1(u32x4{yp[4], yp[5], yp[6], yp[7]}, rws, (u32)(lane * 16), yb + 1024);
      s1 = swap32_add(s1, s1); s2 = swap32_add(s2, s2);
      if (lane < 32 && cb == 0 && kh == 0) __builtin_amdgcn_raw_buffer_store_b32(u_(mu1), rws, (u32)(tok * 4), WS_MU + (u32)((2 * tt + tb) * 128), 16);
      if (lane < 32) st64_sc1(u32x2{u_(s1), u_(s2)}, rws, (u32)(tok * 8), WS_ST + (u32)(((2 * tt + tb) * 16 + 2 * cb + kh) * 256));
    }
#pragma unroll
    for (int q = 0; q < 8; q += 4) asm volatile("" : "+v"(x1p[q]), "+v"(x1p[q + 1]), "+v"(x1p[q + 2]), "+v"(x1p[q + 3]));
    pubbar<0>(16, rws, WS_FY + (u32)(tt * 2048 + cb * 8), tlo, thi, tid);
    wait_flags<8>(rws, WS_FY + (u32)((2 * T + (cw >> 1)) * 2048 + jt * 128), lane, tlo, thi);
    const int tb32 = 4 * T + cw;
    u32x4 yf[32];
    auto ldy = [&](int s) { yf[s] = ld128_sc1(rws, (u32)(lane * 16), WS_Y + (u32)((tb32 * 32 + ((s + 2 * rotg) & 31)) * 1024)); };
#pragma unroll
    for (int s = 0; s < 10; ++s) { ldy(s); SB(); }
    const u32 stv8 = __builtin_amdgcn_raw_buffer_load_b32(rws, (u32)(tok * 4), WS_MU + (u32)(tb32 * 128), 16);
    u32x2 stv[8];
#pragma unroll
    for (int q = 0; q < 8; ++q) stv[q] = ld64_sc1(rws, (u32)(tok * 8 + h * 2048), WS_ST + (u32)((tb32 * 16 + q) * 256));
    SB();
    u32 mz, sz; float r2;
    auto act_store = [&](const f32x16& ac, int pp) {
      u32x4 o;
#pragma unroll
      for (int e = 0; e < 4; ++e) {
        const f32x2 gsc = f32x2{ac[2 * e], ac[2 * e + 1]} * f32x2{r2, r2};
        const f32x2 av = f32x2{fmaxf(gsc[0], 0.f), fmaxf(gsc[1], 0.f)} * f32x2{ac[8 + 2 * e], ac[9 + 2 * e]};
        o[e] = pk2(av[0], av[1]);
      }
      st128_sc1(o, rws, (u32)(lane * 16), WS_ACT + (u32)((tb32 * 112 + 7 * jt + pp) * 1024));
    };
    {
      f32x16 acc[4];
#pragma unroll
      for (int q = 0; q < 4; ++q)
#pragma unroll
        for (int r = 0; r < 16; ++r) acc[q][r] = 0.f;
      LAS char* pa[4];
#pragma unroll
      for (int pp = 0; pp < 4; ++pp) pa[pp] = L + (p1base + (((u32)pp ^ p1x) * 32));
      u32x4 wf[2][4], fx, fxp;
      auto ldw = [&](int st, int bi) {
#pragma unroll
        for (int pp = 0; pp < 4; ++pp) { if (st == 16) pa[pp] = opq(pa[pp] + 8 * L_P1B); wf[bi][pp] = trd2(pa[pp] + ((st >> 1) & 7) * L_P1B + (st & 1) * 2048, 1024); }
      };
      ldw(0, 0); SB();
#pragma unroll
      for (int st = 0; st < 32; ++st) {
        if (st == 15) bar();
        if (st == 12) {
          float s1 = 0.f, s2 = 0.f;
#pragma unroll
          for (int q = 0; q < 8; ++q) { s1 += f_(stv[q][0]); s2 += f_(stv[q][1]); }
          s1 = swap32_add(s1, s1); s2 = swap32_add(s2, s2);
          const float md0 = s1 * (1.f / 512.f), mu1t = f_(stv8);
          const float ve = fmaxf(s2 * (1.f / 512.f) - md0 * md0, 0.f) + 1e-5f, sg = __builtin_amdgcn_sqrtf(ve);
          const float md = rbf(md0 + mu1t) - mu1t;
          r2 = __builtin_amdgcn_rcpf(ve);
          const u32 hi = pk2(-md, sg), lo = pk2(-md - bflo(hi), sg - bfhi(hi));
          const u32 mm = (hi & 0xffffu) | (lo << 16), ss = (hi >> 16) | (lo & 0xffff0000u);
          mz = h ? 0u : mm; sz = h ? 0u : ss;
          asm volatile("" : "+v"(mz), "+v"(sz), "+v"(r2));
        }
        if (st + 1 < 32) ldw(st + 1, (st + 1) & 1);
        if (st + 10 < 32) ldy(st + 10);
#pragma unroll
        for (int pp = 0; pp < 4; ++pp) acc[pp] = mfma32(wf[st & 1][pp], yf[st], acc[pp]);
#pragma unroll
        for (int pp = 0; pp < 4; ++pp) { SGB_MFMA(1); SGB_DSR(2); }
        SB();
        if (st >= 24 && st < 28) fx = *(const LAS u32x4*)(L + L_FIX + (st - 24) * 1024 + lane * 16), SB();
        if (st >= 25 && st < 29) acc[st - 25 < 4 && st >= 25 ? st - 25 : 0] = mfma32(fxp, u32x4{mz, mz, sz, sz}, acc[st - 25 < 4 && st >= 25 ? st - 25 : 0]), SB();
        fxp = fx;
      }
#pragma unroll
      for (int pp = 0; pp < 4; ++pp) act_store(acc[pp], p0 + pp);
      bar();
    }
    u32x4 af[16][2]; u32x2 fl;
    auto lda = [&](int n) { const int s = n >> 1, t2 = n & 1; af[s][t2] = ld128_sc1(rws, (u32)(lane * 16), WS_ACT + (u32)(((2 * tt + (t2 ^ tb)) * 112 + 28 * cw + 8 * (((s >> 2) + rotd) & 3) + (s & 3)) * 1024)); };
    {
      f32x16 acc[3], z16;
#pragma unroll
      for (int r = 0; r < 16; ++r) z16[r] = 0.f;
#pragma unroll
      for (int pp = 0; pp < 3; ++pp) acc[pp] = mfma32(*(const LAS u32x4*)(L + L_FIX + (4 + pp) * 1024 + lane * 16), u32x4{mz, mz, sz, sz}, z16);
      LAS char* pb = L + p2base;
      u32x4 wf[2][3];
      auto ldw = [&](int st, int bi) {
        if (st == 16) pb = opq(pb + 8 * L_P2B);
#pragma unroll
        for (int pp = 0; pp < 3; ++pp) wf[bi][pp] = trd2(pb + ((st >> 1) & 7) * L_P2B + (st & 1) * 1536 + pp * 32, 768);
      };
      ldw(0, 0); SB();
#pragma unroll
      for (int st = 0; st < 32; ++st) {
        if (st == 11) pubbar<0>(16, rws, WS_FA + (u32)(T * 2048 + jt * 8), tlo, thi, tid);
        if (st == 15 || st == 23) bar();
        if (st == 20) fl = peek_flags<4>(rws, WS_FA + (u32)(T * 2048 + ((tt & 1) * 8 + cb) * 128 + cw * 32), lane);
        if (st == 24) wait_flags_from<4>(fl, rws, WS_FA + (u32)(T * 2048 + ((tt & 1) * 8 + cb) * 128 + cw * 32), lane, tlo, thi);
        if (st + 1 < 32) ldw(st + 1, (st + 1) & 1);
        if (st >= 24) { lda(2 * (st - 24)); SB(); lda(2 * (st - 24) + 1); }
#pragma unroll
        for (int pp = 0; pp < 3; ++pp) acc[pp] = mfma32(wf[st & 1][pp], yf[st], acc[pp]);
#pragma unroll
        for (int pp = 0; pp < 3; ++pp) { SGB_MFMA(1); SGB_DSR(2); }
        SB();
      }
      bar();
#pragma unroll
      for (int pp = 0; pp < 3; ++pp) act_store(acc[pp], pp >= p0 ? pp + 4 : pp);
#pragma unroll
      for (int n = 16; n < 24; ++n) { lda(n); SB(); }
    }
    SB();
    {
      f32x16 acc[4];
#pragma unroll
      for (int q = 0; q < 4; ++q)
#pragma unroll
        for (int r = 0; r < 16; ++r) acc[q][r] = 0.f;
      LAS char* d0 = L + (cw * 8192 + (4 * h + q4) * 128 + mrow * 32 + (lane & 3) * 8);
      LAS char* da = d0 + ((q4 >> 1) ^ kh) * 64; LAS char* db = d0 + ((q4 >> 1) ^ kh ^ 1) * 64;
      u32x4 wl[2][2], a2[12][2];
      auto lda2 = [&](int n) {
        const int s2 = n >> 1, t2 = n & 1, jj = (s2 / 3 + rotd) & 3, pp = s2 % 3;
        a2[s2][t2] = ld128_sc1(rws, (u32)(lane * 16), WS_ACT + (u32)(((2 * tt + (t2 ^ tb)) * 112 + 28 * cw + 7 * jj + (pp >= jj ? pp + 4 : pp)) * 1024));
      };
      LAS char* const dah = opq(da + 65536); LAS char* const dbh = opq(db + 65536);
      auto ldw = [&](int s, int bi) { const u32 o = wd_unit_off(s >> 2) + (s & 3) * 2048; wl[bi][0] = trd2(o < 65536 ? da + o : dah + (o - 65536), 1024); wl[bi][1] = trd2(o < 65536 ? db + o : dbh + (o - 65536), 1024); };
      ldw(0, 0); SB();
      pubbar<8>(16, rws, WS_FA2 + (u32)(T * 2048 + jt * 8), tlo, thi, tid);
#pragma unroll
      for (int s = 0; s < 28; ++s) {
        if ((s & 3) == 3 && s < 24) bar();
        if (s == 8) fl = peek_flags<4>(rws, WS_FA2 + (u32)((tt >> 1) * 2048 + ((tt & 1) * 8 + cb) * 128 + cw * 32), lane);
        if (s == 11) wait_flags_from<4>(fl, rws, WS_FA2 + (u32)((tt >> 1) * 2048 + ((tt & 1) * 8 + cb) * 128 + cw * 32), lane, tlo, thi);
        if (s >= 11 && s < 19) { lda2(3 * (s - 11)); SB(); lda2(3 * (s - 11) + 1); SB(); lda2(3 * (s - 11) + 2); }
        if (s < 8) lda(24 + s);
        if (s + 1 < 28) ldw(s + 1, (s + 1) & 1);
        const u32x4 b0 = s < 16 ? af[s < 16 ? s : 0][0] : a2[s < 16 ? 0 : s - 16][0], b1 = s < 16 ? af[s < 16 ? s : 0][1] : a2[s < 16 ? 0 : s - 16][1];
        acc[0] = mfma32(wl[s & 1][0], b0, acc[0]); acc[2] = mfma32(wl[s & 1][1], b0, acc[2]);
        acc[1] = mfma32(wl[s & 1][0], b1, acc[1]); acc[3] = mfma32(wl[s & 1][1], b1, acc[3]);
        SGB_MFMA(1); SGB_DSR(2); SGB_MFMA(1); SGB_DSR(2); SGB_MFMA(2);
        SB();
      }
      int ln2 = lane; asm volatile("" : "+v"(ln2));
#pragma unroll
      for (int i = 1; i < 4; ++i)
#pragma unroll
        for (int q = 0; q < 4; ++q) *(LAS f32x4*)(L + L_RED4 + ((i ^ cw) * 3 + i - 1) * 4096 + q * 1024 + ln2 * 16) = f32x4{acc[i][4 * q], acc[i][4 * q + 1], acc[i][4 * q + 2], acc[i][4 * q + 3]};
      f32x4 gb4[4]; f32x2 xa[8];
#pragma unroll
      for (int q = 0; q < 4; ++q) {
        gb4[q] = *(const LAS f32x4*)(L + L_GBV + (32 * kh + 8 * q + 4 * (ln2 >> 5)) * 4);
#pragma unroll
        for (int e = 0; e < 2; ++e)
          xa[2 * q + e] = __builtin_elementwise_fma(f32x2{gb4[q][2 * e], gb4[q][2 * e + 1]}, f32x2{acc[0][4 * q + 2 * e], acc[0][4 * q + 2 * e + 1]}, f32x2{bflo(x1p[2 * q + e]), bfhi(x1p[2 * q + e])});
      }
#pragma unroll
      for (int q = 0; q < 8; q += 4) asm volatile("" : "+v"(xa[q]), "+v"(xa[q + 1]), "+v"(xa[q + 2]), "+v"(xa[q + 3]));
      bar();
      u32 ob[8];
#pragma unroll
      for (int q = 0; q < 4; ++q) {
        const LAS char* rp = L + L_RED4 + cw * 3 * 4096 + q * 1024 + ln2 * 16;
        const f32x4 rs = (*(const LAS f32x4*)rp + *(const LAS f32x4*)(rp + 4096)) + *(const LAS f32x4*)(rp + 8192);
#pragma unroll
        for (int e = 0; e < 2; ++e) {
          const f32x2 v = __builtin_elementwise_fma(f32x2{gb4[q][2 * e], gb4[q][2 * e + 1]}, f32x2{rs[2 * e], rs[2 * e + 1]}, xa[2 * q + e]);
          ob[2 * q + e] = pk2(v[0], v[1]);
        }
      }
#pragma unroll
      for (int m = 0; m < 2; ++m) {
        auto r0 = __builtin_amdgcn_permlane32_swap(ob[4 * m], ob[4 * m + 2], false, false);
        auto r1 = __builtin_amdgcn_permlane32_swap(ob[4 * m + 1], ob[4 * m + 3], false, false);
        const u32 a0 = r0[0], b0 = r0[1], a1 = r1[0], b1 = r1[1];
        __builtin_amdgcn_raw_buffer_store_b128(u32x4{a0, a1, b0, b1}, rout, (u32)((64 * tt + 32 * tb + (ln2 & 31)) * 1024 + (64 * cb + 32 * kh + 16 * m + 8 * (ln2 >> 5)) * 2), 0, 0);
      }
    }
  }
}

__global__ __launch_bounds__(512, 2) void fused_block(Params p) {
  extern __shared__ __attribute__((aligned(16))) char smem_[];
  asm volatile("" :: "s"(p.x), "s"(p.wqkv), "s"(p.wo), "s"(p.wg), "s"(p.wu), "s"(p.wd), "s"(p.c), "s"(p.wmod), "s"(p.out), "s"(p.ws));
  int w = __builtin_amdgcn_readfirstlane(threadIdx.x >> 6);
  u32x4 wr[18]; u32 tlo, thi;
  front(p, (LAS char*)smem_, threadIdx.x, w, blockIdx.x, wr, tlo, thi);
  if (w >= 4) {
    int b = blockIdx.x, tid = threadIdx.x; asm volatile("; H role" : "+s"(b), "+s"(w)); asm volatile("; H role" : "+v"(tid));
    role_body<false>(p, (LAS char*)smem_, tid, w, b, wr, tlo, thi);
  } else {
    int b = blockIdx.x, tid = threadIdx.x; asm volatile("; C role" : "+s"(b), "+s"(w)); asm volatile("; C role" : "+v"(tid));
    role_body<true>(p, (LAS char*)smem_, tid, w, b, wr, tlo, thi);
  }
}

extern "C" void kernel_launch(void* const* d_in, const int* in_sizes, int n_in, void* d_out, int out_size, void* d_ws, size_t ws_size, hipStream_t stream) {
  Params p;
  p.x = d_in[0]; p.wqkv = d_in[1]; p.wo = d_in[2]; p.wg = d_in[4]; p.wu = d_in[5]; p.wd = d_in[6]; p.c = d_in[7]; p.wmod = d_in[8];
  p.out = d_out; p.ws = d_ws;
  (void)hipFuncSetAttribute((const void*)fused_block, hipFuncAttributeMaxDynamicSharedMemorySize, LDS_BYTES);
  fused_block<<<256, 512, LDS_BYTES, stream>>>(p);
}
```
